```python
import torch

def _softmax(x, axis=-1):
    return torch.softmax(x, dim=axis)
def _gelu(x):
    return torch.nn.functional.gelu(x, approximate="tanh")
def _silu(x):
    return torch.nn.functional.silu(x)
def _sigmoid(x):
    return torch.sigmoid(x)
def _layernorm(x, axis=-1, eps=1e-5):
    m = x.mean(dim=axis, keepdim=True)
    v = x.var(dim=axis, keepdim=True, unbiased=False)
    return (x - m) / torch.sqrt(v + eps)
def _scatter_add(tgt, idx, src, axis=0):
    return tgt.clone().index_add_(axis, idx, src)
def _segment_sum(src, seg, n):
    out = torch.zeros((n,) + src.shape[1:], dtype=src.dtype)
    return out.index_add_(0, seg, src)
def _pad(x, pw):
    flat = tuple(v for pair in reversed(pw) for v in pair)
    return torch.nn.functional.pad(x, flat)

kernel_name = 'standalone__weight_only_int_quip__prompt'

def setup_inputs():

    _w = (lambda a: (lambda t: t.to(torch.bfloat16) if t.is_floating_point() else t)(torch.as_tensor(a)))
    B, S, D, F = 1, 2048, 640, 1536
    G = 32 if 32 > 0 else D
    D = (D // G) * G
    ng = D // G
    bits = 3 if 3 > 0 else 4
    qmax = 1 << bits
    qw = torch.randint(0, qmax, (D, F)).to(torch.int32)
    U, _ = (lambda *_a, **_k: torch.linalg.qr(*[torch.as_tensor(_x).float() for _x in _a], **_k))((lambda *s: torch.randn(*s, dtype=torch.bfloat16) * 0.05)(D, D))
    V, _ = (lambda *_a, **_k: torch.linalg.qr(*[torch.as_tensor(_x).float() for _x in _a], **_k))((lambda *s: torch.randn(*s, dtype=torch.bfloat16) * 0.05)(F, F))
    return {
        "x": _w((lambda *s: torch.randn(*s, dtype=torch.bfloat16) * 0.05)(B, S, D)),
        "qw": _w(qw),
        "scales": _w(torch.abs((lambda *s: torch.randn(*s, dtype=torch.bfloat16) * 0.05)(ng, F)) * 0.02 + 0.005),
        "zeros": _w(torch.full((ng, F), qmax / 2.0, dtype=float)),
        "U": _w(U.to(float)),
        "V": _w(V.to(float)),
    }

def reference(x, qw, scales, zeros, U, V):
    B, S, D = x.shape
    ng, F = scales.shape
    G = D // ng
    s = (lambda x, n, axis: torch.repeat_interleave(x, n, dim=axis))(scales, G, axis=0)
    z = (lambda x, n, axis: torch.repeat_interleave(x, n, dim=axis))(zeros, G, axis=0)
    wq = ((lambda x, dt: x.to(dt))(qw, x.dtype) - z) * s
    xr = torch.einsum("bsd,de->bse", x, U)
    h  = torch.einsum("bse,ef->bsf", xr, wq)
    y  = torch.einsum("bsf,fg->bsg", h, V)
    return y
```

```cpp
#include <hip/hip_runtime.h>
#include <hip/hip_bf16.h>
#include <stdint.h>

typedef unsigned short u16;
__device__ __forceinline__ float bf2f(u16 v) { return __uint_as_float(((unsigned)v) << 16); }
__device__ __forceinline__ u16 f2bf(float f) { __hip_bfloat16 b = __float2bfloat16(f); return *(u16*)&b; }

constexpr int S = 2048, D = 640, F = 1536, G = 32;

template<int MODE>
__global__ void gemm_naive(const void* Aptr, const u16* B, u16* C, int M, int N, int K, const u16* sc, const u16* zr) {
  __shared__ float As[16][17], Bs[16][17];
  int tx = threadIdx.x & 15, ty = threadIdx.x >> 4;
  int row = blockIdx.y * 16 + ty, col = blockIdx.x * 16 + tx;
  float acc = 0.f;
  for (int k0 = 0; k0 < K; k0 += 16) {
    if (MODE == 0) As[ty][tx] = bf2f(((const u16*)Aptr)[row * K + k0 + tx]);
    else {
      int q = ((const int*)Aptr)[row * K + k0 + tx];
      int g = row / G;
      float z = bf2f(zr[g * K + k0 + tx]), s = bf2f(sc[g * K + k0 + tx]);
      float t = bf2f(f2bf(bf2f(f2bf((float)q)) - z));
      As[ty][tx] = bf2f(f2bf(t * s));
    }
    Bs[ty][tx] = bf2f(B[(k0 + ty) * N + col]);
    __syncthreads();
    for (int k = 0; k < 16; ++k) acc += As[ty][k] * Bs[k][tx];
    __syncthreads();
  }
  C[row * N + col] = f2bf(acc);
}

extern "C" void kernel_launch(void* const* d_in, const int* in_sizes, int n_in,
                              void* d_out, int out_size, void* d_ws, size_t ws_size,
                              hipStream_t stream) {
  const u16* x = (const u16*)d_in[0];
  const int* qw = (const int*)d_in[1];
  const u16* sc = (const u16*)d_in[2];
  const u16* zr = (const u16*)d_in[3];
  const u16* U = (const u16*)d_in[4];
  const u16* V = (const u16*)d_in[5];
  u16* xr = (u16*)d_ws;
  u16* W2 = xr + S * D;
  gemm_naive<0><<<dim3(D / 16, S / 16), 256, 0, stream>>>(x, U, xr, S, D, D, nullptr, nullptr);
  gemm_naive<1><<<dim3(F / 16, D / 16), 256, 0, stream>>>(qw, V, W2, D, F, F, sc, zr);
  gemm_naive<0><<<dim3(F / 16, S / 16), 256, 0, stream>>>(xr, W2, (u16*)d_out, S, F, D, nullptr, nullptr);
}
```

```cpp
#include <hip/hip_runtime.h>
#include <stdint.h>

#define LAS __attribute__((address_space(3)))
#define GAS __attribute__((address_space(1)))
typedef unsigned short u16;
typedef unsigned long long u64;
typedef short s16x4 __attribute__((ext_vector_type(4)));
typedef short s16x8 __attribute__((ext_vector_type(8)));
typedef __bf16 bf16x8 __attribute__((ext_vector_type(8)));
typedef __bf16 bf16x2 __attribute__((ext_vector_type(2)));
typedef float f32x2 __attribute__((ext_vector_type(2)));
typedef float f32x4 __attribute__((ext_vector_type(4)));
typedef float f32x16 __attribute__((ext_vector_type(16)));
typedef unsigned u32x4 __attribute__((ext_vector_type(4)));
typedef int i32x4 __attribute__((ext_vector_type(4)));
typedef GAS u64 gu64;

constexpr int S_ = 2048, D_ = 640, F_ = 1536;
constexpr unsigned WS_EPOCH = 0;
constexpr unsigned WS_DF = 4096;
constexpr unsigned WS_XF = 16384;
constexpr unsigned WS_WF = 20480;
constexpr unsigned WS_STAMP = 32768;
constexpr unsigned WS_WQ = 1u << 20;
constexpr unsigned WS_XR = WS_WQ + D_ * F_ * 2;
constexpr unsigned WS_W2 = WS_XR + S_ * D_ * 2;
constexpr unsigned WS_END = WS_W2 + D_ * F_ * 2;
constexpr int LDS_BYTES = 160 * 1024;

#define ROTC 2
#define AAUX 16
#define XL 5
#define YL 6
#define WD 2

enum { PH_D = 1, PH_X = 2, PH_W = 4, PH_Y = 8, PH_ALL = 15 };

struct Params {
  const char* x; const char* qw; const char* sc; const char* zr; const char* U; const char* V; char* y; char* ws;
};

#define RLX_AGENT __ATOMIC_RELAXED, __HIP_MEMORY_SCOPE_AGENT
#define WAIT_VM(n) asm volatile("s_waitcnt vmcnt(%0)" :: "n"(n) : "memory")
#define WAIT_LGKM0() asm volatile("s_waitcnt lgkmcnt(0)" ::: "memory")
#define BAR() do { asm volatile("" ::: "memory"); __builtin_amdgcn_s_barrier(); asm volatile("" ::: "memory"); } while (0)
#define FENCE() asm volatile("" ::: "memory")

#define STAMP(i) do {} while (0)
#define STAMPC(i) do {} while (0)

template <int I> struct IC { static constexpr int value = I; };
template <int B, int E, class Fn>
__device__ __forceinline__ void static_for(Fn&& fn) {
  if constexpr (B < E) {
    if constexpr (E - B == 1) fn(IC<B>{});
    else { static_for<B, (B + E) / 2>(fn); static_for<(B + E) / 2, E>(fn); }
  }
}

template <int OFF, bool SC1>
__device__ __forceinline__ void glds(const char* sbase, unsigned voff, unsigned lds) {
  if constexpr (SC1)
    asm volatile("s_mov_b32 m0, %2\n\ts_nop 0\n\tglobal_load_lds_dwordx4 %0, %1 offset:%3 sc1" :: "v"(voff), "s"(sbase), "s"(lds), "n"(OFF) : "memory");
  else
    asm volatile("s_mov_b32 m0, %2\n\ts_nop 0\n\tglobal_load_lds_dwordx4 %0, %1 offset:%3" :: "v"(voff), "s"(sbase), "s"(lds), "n"(OFF) : "memory");
}
__device__ __forceinline__ void glds4(const char* sbase, unsigned v0, unsigned v1, unsigned v2, unsigned v3, unsigned lds) {
  asm volatile("s_mov_b32 m0, %5\n\ts_nop 0\n\tglobal_load_lds_dwordx4 %0, %4\n\tglobal_load_lds_dwordx4 %1, %4 offset:1024\n\t"
               "global_load_lds_dwordx4 %2, %4 offset:2048\n\tglobal_load_lds_dwordx4 %3, %4 offset:3072"
               :: "v"(v0), "v"(v1), "v"(v2), "v"(v3), "s"(sbase), "s"(lds) : "memory");
}
__device__ __forceinline__ void glds_v(const char* vaddr, unsigned lds) {
  asm volatile("s_mov_b32 m0, %1\n\ts_nop 0\n\tglobal_load_lds_dwordx4 %0, off" :: "v"(vaddr), "s"(lds) : "memory");
}

__device__ __forceinline__ unsigned pack_bf16(float a, float b) {
  f32x2 v = {a, b};
  return __builtin_bit_cast(unsigned, __builtin_convertvector(v, bf16x2));
}
__device__ __forceinline__ float bf_lo(unsigned v) { return __builtin_bit_cast(float, v << 16); }
__device__ __forceinline__ float bf_hi(unsigned v) { return __builtin_bit_cast(float, v & 0xffff0000u); }

__device__ __forceinline__ u32x4 lds_rd128(unsigned a) { return *(LAS u32x4*)(size_t)a; }
__device__ __forceinline__ void lds_wr128(unsigned a, u32x4 v) { *(LAS u32x4*)(size_t)a = v; }
__device__ __forceinline__ bf16x8 lds_tr2(unsigned a0, unsigned a1) {
  s16x4 lo = __builtin_amdgcn_ds_read_tr16_b64_v4i16((LAS s16x4*)(size_t)a0);
  s16x4 hi = __builtin_amdgcn_ds_read_tr16_b64_v4i16((LAS s16x4*)(size_t)a1);
  return __builtin_bit_cast(bf16x8, __builtin_shufflevector(lo, hi, 0, 1, 2, 3, 4, 5, 6, 7));
}

__device__ __forceinline__ u64 mix64(u64 z) {
  z += 0x9E3779B97F4A7C15ull;
  z = (z ^ (z >> 30)) * 0xBF58476D1CE4E5B9ull;
  z = (z ^ (z >> 27)) * 0x94D049BB133111EBull;
  return z ^ (z >> 31);
}

__device__ __forceinline__ u64 load_tag(const Params& p) {
  const char* e = p.ws + WS_EPOCH;
  asm volatile("" : "+s"(e));
  return mix64(*(const __attribute__((address_space(4))) u64*)e);
}

__device__ __forceinline__ void wait_flags(const char* ws, unsigned off, u64 tag) {
  gu64* f = (gu64*)(ws + off);
  for (unsigned spins = 0; spins < (1u << 20); ++spins) {
    u64 v = __hip_atomic_load(f, RLX_AGENT);
    if (__all(v == tag)) break;
    __builtin_amdgcn_s_sleep(2);
  }
  FENCE();
}
__device__ __forceinline__ void wait_flags2(__amdgpu_buffer_rsrc_t wsr, unsigned off, u64 tag) {
  const unsigned lo = (unsigned)tag, hi = (unsigned)(tag >> 32);
  for (unsigned spins = 0; spins < (1u << 20); ++spins) {
    FENCE();
    const u32x4 v = __builtin_amdgcn_raw_buffer_load_b128(wsr, off, 0, 16);
    if (__all(v[0] == lo && v[1] == hi && v[2] == lo && v[3] == hi)) break;
    __builtin_amdgcn_s_sleep(2);
  }
  FENCE();
}
__device__ __forceinline__ void publish(char* ws, unsigned off, u64 tag, int lane) {
  WAIT_VM(0);
  if (lane == 0) __hip_atomic_store((gu64*)(ws + off), tag, RLX_AGENT);
  FENCE();
}

__device__ __forceinline__ unsigned dequant2(int q0, int q1, float z0, float z1, float s0, float s1) {
  unsigned qb = pack_bf16((float)q0, (float)q1);
  unsigned t = pack_bf16(bf_lo(qb) - z0, bf_hi(qb) - z1);
  return pack_bf16(bf_lo(t) * s0, bf_hi(t) * s1);
}

__device__ __forceinline__ u64 phase_x(const Params& p, __amdgpu_buffer_rsrc_t wsr, int wave, int lane, int xcd, int k) {
  const int st = 2 * xcd + (k >> 2), etl = k & 3;
  constexpr unsigned STG = 36 * 1024, UOFF = 16 * 1024;
  if (wave >= 4) {
    const int lw = wave - 4;
    const int rot = (3 * etl + 5 * (k >> 2) + xcd) % 10;
    unsigned xo[4];
#pragma unroll
    for (int i = 0; i < 4; ++i) {
      const int row = 8 * i + (lane >> 3);
      xo[i] = row * 1280 + (((lane & 7) ^ ((row >> 1) & 7)) << 4) - i * 1024;
    }
    unsigned uo[5];
#pragma unroll
    for (int i = 0; i < 5; ++i) {
      const int u = i * 64 + lane, row = (u * 3277) >> 16;
      uo[i] = row * 1280 + (u - row * 20) * 16 - (i & 3) * 1024;
    }
    const char* xb = p.x + (size_t)(st * 128 + lw * 32) * 1280;
    const char* ub = p.U + etl * 320 + lw * (16 * 1280);
    auto issue = [&](int i) {
      int kt = i + rot; if (kt >= 10) kt -= 10;
      const unsigned slot = (i & 3) * STG;
      const char* us = ub + (size_t)kt * (64 * 1280);
      glds4(us, uo[0], uo[1], uo[2], uo[3], slot + UOFF + lw * 5120);
      glds<0, false>(us, uo[4], slot + UOFF + lw * 5120 + 4096);
      glds4(xb + kt * 128, xo[0], xo[1], xo[2], xo[3], slot + lw * 4096);
    };
    issue(0); issue(1);
    const u64 tag = load_tag(p);
    asm volatile("" :: "s"(tag));
    WAIT_VM(9); BAR();
    issue(2); issue(3);
    WAIT_VM(18); BAR();
    WAIT_VM(9); BAR();
#pragma unroll
    for (int i = 3; i < 10; ++i) {
      if (i + 1 < 10) { issue(i + 1); WAIT_VM(9); } else WAIT_VM(0);
      BAR();
    }
    BAR();
    return tag;
  }
  const int r = lane & 31, h = lane >> 5;
  unsigned xa[4];
#pragma unroll
  for (int kk = 0; kk < 4; ++kk) xa[kk] = wave * 4096 + r * 128 + (((2 * kk + h) ^ ((r >> 1) & 7)) << 4);
  const unsigned ua = UOFF + (8 * h + ((lane & 15) >> 2)) * 320 + ((lane >> 4) & 1) * 32 + (lane & 3) * 8;
  f32x16 acc[5];
#pragma unroll
  for (int t = 0; t < 5; ++t)
#pragma unroll
    for (int j = 0; j < 16; ++j) acc[t][j] = 0.f;
  const u64 tag = load_tag(p);
  asm volatile("" :: "s"(tag));
  {
    constexpr int L = XL;
    bf16x8 au[L + 1], bx[3];
    static_for<0, 200 + L>([&](auto ic) {
      constexpr int n = decltype(ic)::value;
      constexpr int j = n - L;
      if constexpr (n < 200) {
        constexpr int i = n / 20, kk = (n % 20) / 5, t = n % 5;
        constexpr unsigned slot = (i & 3) * STG;
        if constexpr (n % 20 == 0) {
          BAR();
          if constexpr (i == 0) STAMP(1); if constexpr (i == 5) STAMP(4);
        }
        if constexpr (t == 0) bx[(n / 5) % 3] = __builtin_bit_cast(bf16x8, lds_rd128(slot + xa[kk]));
        const unsigned ad = slot + ua + kk * 16 * 320 + t * 64;
        au[n % (L + 1)] = lds_tr2(ad, ad + 4 * 320);
      }
      if constexpr (j >= 0) acc[j % 5] = __builtin_amdgcn_mfma_f32_32x32x16_bf16(au[j % (L + 1)], bx[(j / 5) % 3], acc[j % 5], 0, 0, 0);
      __builtin_amdgcn_sched_barrier(0);
    });
  }
  BAR();
  STAMP(6);
  const unsigned ob = WS_XR + ((st * 4 + wave) * 40 + etl * 10) * 1024;
#pragma unroll
  for (int t = 0; t < 5; ++t)
#pragma unroll
    for (int s2 = 0; s2 < 2; ++s2) {
      u32x4 v;
#pragma unroll
      for (int j = 0; j < 4; ++j) v[j] = pack_bf16(acc[t][8 * s2 + 2 * j], acc[t][8 * s2 + 2 * j + 1]);
      __builtin_amdgcn_raw_buffer_store_b128(v, wsr, lane * 16, ob + (2 * t + s2) * 1024, 16);
    }
  STAMP(7);
  publish(p.ws, WS_XF + ((st * 4 + wave) * 4 + etl) * 8, tag, lane);
  return tag;
}

template <int PH>
__device__ __forceinline__ u64 phase_dw(const Params& p, __amdgpu_buffer_rsrc_t wsr, int wave, int lane, int xcd, int k) {
  const int et = 4 * (xcd >> 2) + k / 6, gt = 6 * (xcd & 3) + k % 6;
  const int dj = xcd * 24 + k, det = xcd, dkp = k;
  const int rot = ROTC * (k % 6);
  const int kq = wave & 3;
  unsigned vo[4];
#pragma unroll
  for (int pc = 0; pc < 4; ++pc) {
    const int row = 8 * pc + (lane >> 3), swz = ((row >> 1) & 1) | (((row >> 3) & 1) << 1);
    vo[pc] = row * 3072 + ((((lane & 7) >> 1) ^ swz) << 5) + ((lane & 1) << 4) - pc * 1024;
  }
  const char* vb = p.V + gt * 128 + (size_t)kq * (12 * 32 * 3072);
  auto kstep = [&](int i) { int t = i + rot; return t >= 12 ? t - 12 : t; };
  auto issue_v = [&](int i, int sl) {
    glds4(vb + (size_t)kstep(i) * (32 * 3072), vo[0], vo[1], vo[2], vo[3], (kq * 10 + sl) * 4096);
  };
  u64 tag;
  if (wave >= 4) {
    if constexpr (PH & PH_W) {
#pragma unroll
      for (int c = 0; c < 8; ++c) {
        const int n = (c + 2 * (et & 3)) & 7;
        issue_v(n, n);
      }
    }
    tag = load_tag(p);
  } else {
    const unsigned dl = (wave * 10 + 8) * 4096;
    if constexpr (PH & PH_D) {
      const int k2 = wave & 1, nfr = wave < 2 ? 3 : 2;
      {
        unsigned qo[2];
#pragma unroll
        for (int i = 0; i < 2; ++i) {
          const int row = 8 * i + (lane >> 3);
          qo[i] = row * 6144 + (((lane & 7) ^ ((row >> 1) & 7)) << 4) - i * 1024;
        }
        const char* qb = p.qw + (size_t)(80 * det + 16 * (wave >> 1)) * 6144 + (64 * dkp + 32 * k2) * 4;
#pragma unroll
        for (int i = 0; i < 3; ++i)
          if (i < nfr) {
            glds<0, false>(qb + (size_t)i * (32 * 6144), qo[0], dl + i * 2048);
            glds<1024, false>(qb + (size_t)i * (32 * 6144), qo[1], dl + i * 2048);
          }
        int i = (lane >> 3) & 3; if (i >= nfr) i = 0;
        const int grp = (5 * det + (wave >> 1) + 2 * i) >> 1;
        const char* sa = (((lane >> 2) & 1) ? p.zr : p.sc) + (size_t)grp * 3072 + (64 * dkp + 32 * k2) * 2 + (lane & 3) * 16;
        glds_v(sa, dl + 6144);
      }
      tag = load_tag(p);
      WAIT_VM(0);
      STAMP(1);
      const int r = lane & 15, q = lane >> 4;
      i32x4 q0[3], q1[3]; u32x4 sv[3], zv[3];
#pragma unroll
      for (int i = 0; i < 3; ++i) {
        const unsigned a = dl + i * 2048 + r * 128;
        q0[i] = __builtin_bit_cast(i32x4, lds_rd128(a + (((2 * q) ^ ((r >> 1) & 7)) << 4)));
        q1[i] = __builtin_bit_cast(i32x4, lds_rd128(a + (((2 * q + 1) ^ ((r >> 1) & 7)) << 4)));
        sv[i] = lds_rd128(dl + 6144 + i * 128 + q * 16);
        zv[i] = lds_rd128(dl + 6144 + i * 128 + 64 + q * 16);
      }
      FENCE();
#pragma unroll
      for (int i = 0; i < 3; ++i)
        if (i < nfr) {
          const u32x4 s = sv[i], z = zv[i];
          u32x4 o;
          o[0] = dequant2(q0[i][0], q0[i][1], bf_lo(z[0]), bf_hi(z[0]), bf_lo(s[0]), bf_hi(s[0]));
          o[1] = dequant2(q0[i][2], q0[i][3], bf_lo(z[1]), bf_hi(z[1]), bf_lo(s[1]), bf_hi(s[1]));
          o[2] = dequant2(q1[i][0], q1[i][1], bf_lo(z[2]), bf_hi(z[2]), bf_lo(s[2]), bf_hi(s[2]));
          o[3] = dequant2(q1[i][2], q1[i][3], bf_lo(z[3]), bf_hi(z[3]), bf_lo(s[3]), bf_hi(s[3]));
          __builtin_amdgcn_raw_buffer_store_b128(o, wsr, lane * 16, WS_WQ + ((det * 48 + 2 * dkp + k2) * 5 + (wave >> 1) + 2 * i) * 1024, 16);
        }
      WAIT_LGKM0();
      STAMP(2);
      publish(p.ws, WS_DF + (dj * 4 + wave) * 8, tag, lane);
      STAMP(3);
    } else {
      tag = load_tag(p);
    }
    if constexpr (PH & PH_W) {
      issue_v(8, 8);
      issue_v(9, 9);
    }
  }
  if constexpr (PH & PH_W) {
    STAMP(4);
    if constexpr (PH & PH_D) {
      if (wave == 0) wait_flags2(wsr, WS_DF + et * 96 * 8 + (lane < 48 ? lane : 0) * 16, tag);
    }
    asm volatile("" :: "s"(tag));
    WAIT_VM(0);
    BAR();
    STAMP(5);
    unsigned va[4];
    {
      const int g = lane >> 4, qq = (lane & 15) >> 2, sw = ((qq >> 1) & 1) | ((g & 1) << 1);
#pragma unroll
      for (int c = 0; c < 4; ++c) va[c] = kq * 40960 + 128 * (8 * g + qq) + 32 * (c ^ sw) + 8 * (lane & 3);
    }
    const unsigned red = kq * 40960 + 8192 + lane * 16;
    auto body = [&](auto nbc) {
      constexpr int NB = decltype(nbc)::value, M0 = NB == 3 ? 0 : 3, DEPTH = WD;
      const unsigned ab = WS_WQ + ((et * 48 + kq * 12) * 5 + M0) * 1024;
      u32x4 af[DEPTH + 1][NB];
      f32x4 acc[NB][4];
#pragma unroll
      for (int m = 0; m < NB; ++m)
#pragma unroll
        for (int c = 0; c < 4; ++c) acc[m][c] = (f32x4){0.f, 0.f, 0.f, 0.f};
      s16x4 bh[2][8];
      static_for<0, 12 + DEPTH>([&](auto ic) {
        constexpr int n = decltype(ic)::value;
        constexpr int i = n - DEPTH;
        if constexpr (i + 1 == 10) BAR();
        const unsigned so = ab + kstep(n < 12 ? n : 0) * 5120;
        static_for<0, NB * 4>([&](auto jc) {
          constexpr int j = decltype(jc)::value, m = j / 4, c = j % 4;
          if constexpr (i >= 0) {
            const bf16x8 bv = __builtin_bit_cast(bf16x8, __builtin_shufflevector(bh[i & 1][2 * c], bh[i & 1][2 * c + 1], 0, 1, 2, 3, 4, 5, 6, 7));
            acc[m][c] = __builtin_amdgcn_mfma_f32_16x16x32_bf16(__builtin_bit_cast(bf16x8, af[i % (DEPTH + 1)][m]), bv, acc[m][c], 0, 0, 0);
          }
          if constexpr (i + 1 >= 0 && i + 1 < 12 && j < 8)
            bh[(i + 1) & 1][j] = __builtin_amdgcn_ds_read_tr16_b64_v4i16((LAS s16x4*)(size_t)(va[j >> 1] + ((i + 1) % 10) * 4096 + (j & 1) * 512));
          if constexpr (n < 12 && c == 3) {
            af[n % (DEPTH + 1)][m] = __builtin_amdgcn_raw_buffer_load_b128(wsr, lane * 16 + m * 1024, so, AAUX);
          }
          __builtin_amdgcn_sched_barrier(0);
        });
        if constexpr (i == 1) {
          BAR();
          issue_v(10 + (NB == 3), NB == 3);
        }
      });
      STAMP(6);
      if constexpr (NB == 2) {
#pragma unroll
        for (int m = 0; m < NB; ++m)
#pragma unroll
          for (int c = 0; c < 4; ++c) lds_wr128(red + ((M0 + m) * 4 + c) * 1024, __builtin_bit_cast(u32x4, acc[m][c]));
        WAIT_LGKM0();
        BAR();
        BAR();
      } else {
        __builtin_amdgcn_s_setprio(3);
#pragma unroll
        for (int fi = 0; fi < 6; ++fi)
          if ((fi & 3) != kq) {
            lds_wr128(red + fi * 2048, __builtin_bit_cast(u32x4, acc[fi >> 1][2 * (fi & 1)]));
            lds_wr128(red + fi * 2048 + 1024, __builtin_bit_cast(u32x4, acc[fi >> 1][2 * (fi & 1) + 1]));
          }
        f32x4 sa[3], sb[3];
        sa[0] = kq == 0 ? acc[0][0] : kq == 1 ? acc[0][2] : kq == 2 ? acc[1][0] : acc[1][2];
        sb[0] = kq == 0 ? acc[0][1] : kq == 1 ? acc[0][3] : kq == 2 ? acc[1][1] : acc[1][3];
        sa[1] = kq == 0 ? acc[2][0] : acc[2][2];
        sb[1] = kq == 0 ? acc[2][1] : acc[2][3];
        WAIT_LGKM0();
        BAR();
        const unsigned rb = 8192 + lane * 16 + kq * 2048;
        const unsigned ol = ((lane & 15) + 16 * (lane >> 5) + 32 * ((lane >> 4) & 1)) * 16;
        auto rd = [&](int q, int j, int hf) { return __builtin_bit_cast(f32x4, lds_rd128(rb + q * 40960 + j * 8192 + hf * 1024)); };
        auto out = [&](int j) {
          const f32x4 a = sa[j], b = sb[j];
          auto r0 = __builtin_amdgcn_permlane32_swap(pack_bf16(a[0], a[1]), pack_bf16(b[0], b[1]), false, false);
          auto r1 = __builtin_amdgcn_permlane32_swap(pack_bf16(a[2], a[3]), pack_bf16(b[2], b[3]), false, false);
          const u32x4 v = {r0[0], r1[0], r0[1], r1[1]};
          const int fi = kq + 4 * j;
          __builtin_amdgcn_raw_buffer_store_b128(v, wsr, ol, WS_W2 + ((2 * gt + (fi & 1)) * 40 + 5 * et + (fi >> 1)) * 1024, 16);
        };
        const int q1 = (kq + 1) & 3, q2 = (kq + 2) & 3, q3 = (kq + 3) & 3;
        if (kq < 2) {
          sa[2] = rd(kq, 2, 0); sb[2] = rd(kq, 2, 1);
#pragma unroll
          for (int j = 0; j < 3; ++j) {
            sa[j] += rd(q1, j, 0) + (rd(q2, j, 0) + rd(q3, j, 0));
            sb[j] += rd(q1, j, 1) + (rd(q2, j, 1) + rd(q3, j, 1));
          }
          out(0); out(1); out(2);
        } else {
          sa[1] = rd(kq, 1, 0); sb[1] = rd(kq, 1, 1);
#pragma unroll
          for (int j = 0; j < 2; ++j) {
            sa[j] += rd(q1, j, 0) + (rd(q2, j, 0) + rd(q3, j, 0));
            sb[j] += rd(q1, j, 1) + (rd(q2, j, 1) + rd(q3, j, 1));
          }
          out(0); out(1);
        }
        BAR();
        STAMP(7);
        publish(p.ws, WS_WF + ((gt * 8 + et) * 4 + kq) * 8, tag, lane);
      }
    };
    if (wave >= 4) body(IC<3>{}); else body(IC<2>{});
  }
  return tag;
}

template <int PH>
__device__ __forceinline__ void phase_y(const Params& p, __amdgpu_buffer_rsrc_t wsr, int wave, int lane, int xcd, int idx, u64 tag) {
  const int st = 4 * (xcd >> 1) + (idx >> 3), gt = 8 * (xcd & 1) + (idx & 7);
  constexpr bool FUSED = (PH & (PH_X | PH_W)) != 0;
  STAMP(8);
  if (wave >= 4) {
    const int lw = wave - 4;
    __builtin_amdgcn_s_setprio(3);
    if constexpr (FUSED) {
      wait_flags(p.ws, WS_WF + ((3 * gt / 2) * 32 + lane) * 8, tag);
      if (blockIdx.x == 0) {
        const int w = lw * 64 + lane;
        wait_flags(p.ws, w < 64 ? WS_XF + ((w >> 2) * 16 + (w & 3)) * 8 : WS_WF + (w - 64) * 32, tag);
      }
    }
    STAMP(11);
    const char* wb = p.ws + WS_W2 + (size_t)(3 * gt * 40 + lw) * 1024;
    const unsigned vo = lane * 16;
    auto issue = [&](int c) {
#pragma unroll
      for (int t = 0; t < 3; ++t) glds<0, true>(wb + c * 4096 + t * 40960, vo, c * 12288 + lw * 1024 + t * 4096);
    };
    issue(0); issue(1); issue(2);
    WAIT_VM(6); BAR();
    if constexpr (FUSED) { if (blockIdx.x == 0 && threadIdx.x == 256) __hip_atomic_store((gu64*)(p.ws + WS_EPOCH), tag, RLX_AGENT); }
    issue(3); issue(4);
    WAIT_VM(9); BAR();
    issue(5); issue(6);
    WAIT_VM(9); BAR();
    issue(7); issue(8); issue(9);
    WAIT_VM(9); BAR();
    WAIT_VM(0); BAR();
    return;
  }
  if constexpr (FUSED) wait_flags(p.ws, WS_XF + ((st * 4 + wave) * 4 + (lane & 3)) * 8, tag);
  STAMP(9);
  u32x4 xf[40];
  const unsigned xbase = WS_XR + (st * 4 + wave) * 40 * 1024;
#pragma unroll
  for (int ks = 0; ks < 40; ++ks) xf[ks] = __builtin_amdgcn_raw_buffer_load_b128(wsr, lane * 16 + (ks & 3) * 1024, xbase + (ks >> 2) * 4096, 16);
  FENCE();
  STAMP(10);
  f32x16 acc[3];
#pragma unroll
  for (int t = 0; t < 3; ++t)
#pragma unroll
    for (int j = 0; j < 16; ++j) acc[t][j] = 0.f;
  {
    constexpr int L = YL;
    bf16x8 aw[L + 1];
    static_for<0, 120 + L>([&](auto ic) {
      constexpr int n = decltype(ic)::value, j = n - L;
      if constexpr (n < 120) {
        constexpr int c = n / 12, kk = (n % 12) / 3, t = n % 3;
        if constexpr (n % 12 == 0 && (c == 0 || c == 1 || c == 2 || c == 4 || c == 7)) {
          BAR();
          if constexpr (c == 0) STAMP(11);
        }
        aw[n % (L + 1)] = __builtin_bit_cast(bf16x8, lds_rd128(c * 12288 + t * 4096 + kk * 1024 + lane * 16));
      }
      if constexpr (j >= 0) acc[j % 3] = __builtin_amdgcn_mfma_f32_32x32x16_bf16(aw[j % (L + 1)], __builtin_bit_cast(bf16x8, xf[j / 3]), acc[j % 3], 0, 0, 0);
      __builtin_amdgcn_sched_barrier(0);
    });
  }
  STAMP(12);
  char* yb = p.y + (size_t)(st * 128 + wave * 32 + (lane & 31)) * 3072 + gt * 192 + (lane >> 5) * 16;
#pragma unroll
  for (int t = 0; t < 3; ++t)
#pragma unroll
    for (int i = 0; i < 4; i += 2) {
      auto r0 = __builtin_amdgcn_permlane32_swap(pack_bf16(acc[t][4 * i], acc[t][4 * i + 1]), pack_bf16(acc[t][4 * i + 4], acc[t][4 * i + 5]), false, false);
      auto r1 = __builtin_amdgcn_permlane32_swap(pack_bf16(acc[t][4 * i + 2], acc[t][4 * i + 3]), pack_bf16(acc[t][4 * i + 6], acc[t][4 * i + 7]), false, false);
      const u32x4 v = {r0[0], r1[0], r0[1], r1[1]};
      *(GAS u32x4*)(yb + t * 64 + i * 16) = v;
    }
}

template <int PH>
__global__ void __launch_bounds__(512, 2) quip_kernel(Params p) {
  const int wave = __builtin_amdgcn_readfirstlane(threadIdx.x >> 6), lane = threadIdx.x & 63;
  const int xcd = blockIdx.x & 7, idx = blockIdx.x >> 3;
  STAMP(0); STAMPC(14);
  const __amdgpu_buffer_rsrc_t wsr = __builtin_amdgcn_make_buffer_rsrc(p.ws, 0, WS_END, 0x00020000);
  u64 tag = 0;
  if (idx < 8) {
    if constexpr (PH & PH_X) tag = phase_x(p, wsr, wave, lane, xcd, idx);
  } else {
    if constexpr (PH & (PH_D | PH_W)) tag = phase_dw<PH>(p, wsr, wave, lane, xcd, idx - 8);
  }
  if constexpr (PH & PH_Y) phase_y<PH>(p, wsr, wave, lane, xcd, idx, tag);
  STAMP(13); STAMPC(15);
}

#define NLAUNCH 1

template <int PH>
static void launch(const Params& p, hipStream_t stream) {
  (void)hipFuncSetAttribute((const void*)quip_kernel<PH>, hipFuncAttributeMaxDynamicSharedMemorySize, LDS_BYTES);
  quip_kernel<PH><<<256, 512, LDS_BYTES, stream>>>(p);
}

extern "C" void kernel_launch(void* const* d_in, const int* in_sizes, int n_in, void* d_out, int out_size, void* d_ws,
                              size_t ws_size, hipStream_t stream) {
  Params p;
  p.x = (const char*)d_in[0]; p.qw = (const char*)d_in[1]; p.sc = (const char*)d_in[2]; p.zr = (const char*)d_in[3];
  p.U = (const char*)d_in[4]; p.V = (const char*)d_in[5]; p.y = (char*)d_out; p.ws = (char*)d_ws;
  launch<PH_ALL>(p, stream);
}
```

```cpp
#define PROBE 99
#define PROLE 1
#include <hip/hip_runtime.h>
#include <stdint.h>

#define LAS __attribute__((address_space(3)))
#define GAS __attribute__((address_space(1)))
typedef unsigned short u16;
typedef unsigned long long u64;
typedef short s16x4 __attribute__((ext_vector_type(4)));
typedef short s16x8 __attribute__((ext_vector_type(8)));
typedef __bf16 bf16x8 __attribute__((ext_vector_type(8)));
typedef __bf16 bf16x2 __attribute__((ext_vector_type(2)));
typedef float f32x2 __attribute__((ext_vector_type(2)));
typedef float f32x4 __attribute__((ext_vector_type(4)));
typedef float f32x16 __attribute__((ext_vector_type(16)));
typedef unsigned u32x4 __attribute__((ext_vector_type(4)));
typedef int i32x4 __attribute__((ext_vector_type(4)));
typedef GAS u64 gu64;

constexpr int S_ = 2048, D_ = 640, F_ = 1536;
constexpr unsigned WS_EPOCH = 0;
constexpr unsigned WS_DF = 4096;
constexpr unsigned WS_XF = 16384;
constexpr unsigned WS_WF = 20480;
constexpr unsigned WS_STAMP = 32768;
constexpr unsigned WS_WQ = 1u << 20;
constexpr unsigned WS_XR = WS_WQ + D_ * F_ * 2;
constexpr unsigned WS_W2 = WS_XR + S_ * D_ * 2;
constexpr unsigned WS_END = WS_W2 + D_ * F_ * 2;
constexpr int LDS_BYTES = 160 * 1024;

#define ROTC 2
#define AAUX 16
#define XL 5
#define YL 6
#define WD 2

enum { PH_D = 1, PH_X = 2, PH_W = 4, PH_Y = 8, PH_ALL = 15 };

struct Params {
  const char* x; const char* qw; const char* sc; const char* zr; const char* U; const char* V; char* y; char* ws;
  mutable unsigned long long t0, d;
};

#define g_t0 (p.t0)
#define g_d (p.d)
#define RLX_AGENT __ATOMIC_RELAXED, __HIP_MEMORY_SCOPE_AGENT
#define WAIT_VM(n) asm volatile("s_waitcnt vmcnt(%0)" :: "n"(n) : "memory")
#define WAIT_LGKM0() asm volatile("s_waitcnt lgkmcnt(0)" ::: "memory")
#define BAR() do { asm volatile("" ::: "memory"); __builtin_amdgcn_s_barrier(); asm volatile("" ::: "memory"); } while (0)
#define FENCE() asm volatile("" ::: "memory")

#define STAMP(i) do { if ((i) == 0) g_t0 = __builtin_amdgcn_s_memrealtime(); if ((i) == PROBE && (PROLE)) g_d = __builtin_amdgcn_s_memrealtime() - g_t0; } while (0)
#define STAMPC(i) do {} while (0)

template <int I> struct IC { static constexpr int value = I; };
template <int B, int E, class Fn>
__device__ __forceinline__ void static_for(Fn&& fn) {
  if constexpr (B < E) {
    if constexpr (E - B == 1) fn(IC<B>{});
    else { static_for<B, (B + E) / 2>(fn); static_for<(B + E) / 2, E>(fn); }
  }
}

template <int OFF, bool SC1>
__device__ __forceinline__ void glds(const char* sbase, unsigned voff, unsigned lds) {
  if constexpr (SC1)
    asm volatile("s_mov_b32 m0, %2\n\ts_nop 0\n\tglobal_load_lds_dwordx4 %0, %1 offset:%3 sc1" :: "v"(voff), "s"(sbase), "s"(lds), "n"(OFF) : "memory");
  else
    asm volatile("s_mov_b32 m0, %2\n\ts_nop 0\n\tglobal_load_lds_dwordx4 %0, %1 offset:%3" :: "v"(voff), "s"(sbase), "s"(lds), "n"(OFF) : "memory");
}
__device__ __forceinline__ void glds4(const char* sbase, unsigned v0, unsigned v1, unsigned v2, unsigned v3, unsigned lds) {
  asm volatile("s_mov_b32 m0, %5\n\ts_nop 0\n\tglobal_load_lds_dwordx4 %0, %4\n\tglobal_load_lds_dwordx4 %1, %4 offset:1024\n\t"
               "global_load_lds_dwordx4 %2, %4 offset:2048\n\tglobal_load_lds_dwordx4 %3, %4 offset:3072"
               :: "v"(v0), "v"(v1), "v"(v2), "v"(v3), "s"(sbase), "s"(lds) : "memory");
}
__device__ __forceinline__ void glds_v(const char* vaddr, unsigned lds) {
  asm volatile("s_mov_b32 m0, %1\n\ts_nop 0\n\tglobal_load_lds_dwordx4 %0, off" :: "v"(vaddr), "s"(lds) : "memory");
}

__device__ __forceinline__ unsigned pack_bf16(float a, float b) {
  f32x2 v = {a, b};
  return __builtin_bit_cast(unsigned, __builtin_convertvector(v, bf16x2));
}
__device__ __forceinline__ float bf_lo(unsigned v) { return __builtin_bit_cast(float, v << 16); }
__device__ __forceinline__ float bf_hi(unsigned v) { return __builtin_bit_cast(float, v & 0xffff0000u); }

__device__ __forceinline__ u32x4 lds_rd128(unsigned a) { return *(LAS u32x4*)(size_t)a; }
__device__ __forceinline__ void lds_wr128(unsigned a, u32x4 v) { *(LAS u32x4*)(size_t)a = v; }
__device__ __forceinline__ bf16x8 lds_tr2(unsigned a0, unsigned a1) {
  s16x4 lo = __builtin_amdgcn_ds_read_tr16_b64_v4i16((LAS s16x4*)(size_t)a0);
  s16x4 hi = __builtin_amdgcn_ds_read_tr16_b64_v4i16((LAS s16x4*)(size_t)a1);
  return __builtin_bit_cast(bf16x8, __builtin_shufflevector(lo, hi, 0, 1, 2, 3, 4, 5, 6, 7));
}

__device__ __forceinline__ u64 mix64(u64 z) {
  z += 0x9E3779B97F4A7C15ull;
  z = (z ^ (z >> 30)) * 0xBF58476D1CE4E5B9ull;
  z = (z ^ (z >> 27)) * 0x94D049BB133111EBull;
  return z ^ (z >> 31);
}

__device__ __forceinline__ u64 load_tag(const Params& p) {
  const char* e = p.ws + WS_EPOCH;
  asm volatile("" : "+s"(e));
  return mix64(*(const __attribute__((address_space(4))) u64*)e);
}

__device__ __forceinline__ void wait_flags(const char* ws, unsigned off, u64 tag) {
  gu64* f = (gu64*)(ws + off);
  for (unsigned spins = 0; spins < (1u << 20); ++spins) {
    u64 v = __hip_atomic_load(f, RLX_AGENT);
    if (__all(v == tag)) break;
    __builtin_amdgcn_s_sleep(2);
  }
  FENCE();
}
__device__ __forceinline__ void wait_flags2(__amdgpu_buffer_rsrc_t wsr, unsigned off, u64 tag) {
  const unsigned lo = (unsigned)tag, hi = (unsigned)(tag >> 32);
  for (unsigned spins = 0; spins < (1u << 20); ++spins) {
    FENCE();
    const u32x4 v = __builtin_amdgcn_raw_buffer_load_b128(wsr, off, 0, 16);
    if (__all(v[0] == lo && v[1] == hi && v[2] == lo && v[3] == hi)) break;
    __builtin_amdgcn_s_sleep(2);
  }
  FENCE();
}
__device__ __forceinline__ void publish(char* ws, unsigned off, u64 tag, int lane) {
  WAIT_VM(0);
  if (lane == 0) __hip_atomic_store((gu64*)(ws + off), tag, RLX_AGENT);
  FENCE();
}

__device__ __forceinline__ unsigned dequant2(int q0, int q1, float z0, float z1, float s0, float s1) {
  unsigned qb = pack_bf16((float)q0, (float)q1);
  unsigned t = pack_bf16(bf_lo(qb) - z0, bf_hi(qb) - z1);
  return pack_bf16(bf_lo(t) * s0, bf_hi(t) * s1);
}

__device__ __forceinline__ u64 phase_x(const Params& p, __amdgpu_buffer_rsrc_t wsr, int wave, int lane, int xcd, int k) {
  const int st = 2 * xcd + (k >> 2), etl = k & 3;
  constexpr unsigned STG = 36 * 1024, UOFF = 16 * 1024;
  if (wave >= 4) {
    const int lw = wave - 4;
    const int rot = (3 * etl + 5 * (k >> 2) + xcd) % 10;
    unsigned xo[4];
#pragma unroll
    for (int i = 0; i < 4; ++i) {
      const int row = 8 * i + (lane >> 3);
      xo[i] = row * 1280 + (((lane & 7) ^ ((row >> 1) & 7)) << 4) - i * 1024;
    }
    unsigned uo[5];
#pragma unroll
    for (int i = 0; i < 5; ++i) {
      const int u = i * 64 + lane, row = (u * 3277) >> 16;
      uo[i] = row * 1280 + (u - row * 20) * 16 - (i & 3) * 1024;
    }
    const char* xb = p.x + (size_t)(st * 128 + lw * 32) * 1280;
    const char* ub = p.U + etl * 320 + lw * (16 * 1280);
    auto issue = [&](int i) {
      int kt = i + rot; if (kt >= 10) kt -= 10;
      const unsigned slot = (i & 3) * STG;
      const char* us = ub + (size_t)kt * (64 * 1280);
      glds4(us, uo[0], uo[1], uo[2], uo[3], slot + UOFF + lw * 5120);
      glds<0, false>(us, uo[4], slot + UOFF + lw * 5120 + 4096);
      glds4(xb + kt * 128, xo[0], xo[1], xo[2], xo[3], slot + lw * 4096);
    };
    issue(0); issue(1);
    const u64 tag = load_tag(p);
    asm volatile("" :: "s"(tag));
    WAIT_VM(9); BAR();
    issue(2); issue(3);
    WAIT_VM(18); BAR();
    WAIT_VM(9); BAR();
#pragma unroll
    for (int i = 3; i < 10; ++i) {
      if (i + 1 < 10) { issue(i + 1); WAIT_VM(9); } else WAIT_VM(0);
      BAR();
    }
    BAR();
    return tag;
  }
  const int r = lane & 31, h = lane >> 5;
  unsigned xa[4];
#pragma unroll
  for (int kk = 0; kk < 4; ++kk) xa[kk] = wave * 4096 + r * 128 + (((2 * kk + h) ^ ((r >> 1) & 7)) << 4);
  const unsigned ua = UOFF + (8 * h + ((lane & 15) >> 2)) * 320 + ((lane >> 4) & 1) * 32 + (lane & 3) * 8;
  f32x16 acc[5];
#pragma unroll
  for (int t = 0; t < 5; ++t)
#pragma unroll
    for (int j = 0; j < 16; ++j) acc[t][j] = 0.f;
  const u64 tag = load_tag(p);
  asm volatile("" :: "s"(tag));
  {
    constexpr int L = XL;
    bf16x8 au[L + 1], bx[3];
    static_for<0, 200 + L>([&](auto ic) {
      constexpr int n = decltype(ic)::value;
      constexpr int j = n - L;
      if constexpr (n < 200) {
        constexpr int i = n / 20, kk = (n % 20) / 5, t = n % 5;
        constexpr unsigned slot = (i & 3) * STG;
        if constexpr (n % 20 == 0) {
          BAR();
          if constexpr (i == 0) STAMP(1); if constexpr (i == 5) STAMP(4);
        }
        if constexpr (t == 0) bx[(n / 5) % 3] = __builtin_bit_cast(bf16x8, lds_rd128(slot + xa[kk]));
        const unsigned ad = slot + ua + kk * 16 * 320 + t * 64;
        au[n % (L + 1)] = lds_tr2(ad, ad + 4 * 320);
      }
      if constexpr (j >= 0) acc[j % 5] = __builtin_amdgcn_mfma_f32_32x32x16_bf16(au[j % (L + 1)], bx[(j / 5) % 3], acc[j % 5], 0, 0, 0);
      __builtin_amdgcn_sched_barrier(0);
    });
  }
  BAR();
  STAMP(6);
  const unsigned ob = WS_XR + ((st * 4 + wave) * 40 + etl * 10) * 1024;
#pragma unroll
  for (int t = 0; t < 5; ++t)
#pragma unroll
    for (int s2 = 0; s2 < 2; ++s2) {
      u32x4 v;
#pragma unroll
      for (int j = 0; j < 4; ++j) v[j] = pack_bf16(acc[t][8 * s2 + 2 * j], acc[t][8 * s2 + 2 * j + 1]);
      __builtin_amdgcn_raw_buffer_store_b128(v, wsr, lane * 16, ob + (2 * t + s2) * 1024, 16);
    }
  STAMP(7);
  publish(p.ws, WS_XF + ((st * 4 + wave) * 4 + etl) * 8, tag, lane);
  return tag;
}

template <int PH>
__device__ __forceinline__ u64 phase_dw(const Params& p, __amdgpu_buffer_rsrc_t wsr, int wave, int lane, int xcd, int k) {
  const int et = 4 * (xcd >> 2) + k / 6, gt = 6 * (xcd & 3) + k % 6;
  const int dj = xcd * 24 + k, det = xcd, dkp = k;
  const int rot = ROTC * (k % 6);
  const int kq = wave & 3;
  unsigned vo[4];
#pragma unroll
  for (int pc = 0; pc < 4; ++pc) {
    const int row = 8 * pc + (lane >> 3), swz = ((row >> 1) & 1) | (((row >> 3) & 1) << 1);
    vo[pc] = row * 3072 + ((((lane & 7) >> 1) ^ swz) << 5) + ((lane & 1) << 4) - pc * 1024;
  }
  const char* vb = p.V + gt * 128 + (size_t)kq * (12 * 32 * 3072);
  auto kstep = [&](int i) { int t = i + rot; return t >= 12 ? t - 12 : t; };
  auto issue_v = [&](int i, int sl) {
    glds4(vb + (size_t)kstep(i) * (32 * 3072), vo[0], vo[1], vo[2], vo[3], (kq * 10 + sl) * 4096);
  };
  u64 tag;
  if (wave >= 4) {
    if constexpr (PH & PH_W) {
#pragma unroll
      for (int c = 0; c < 8; ++c) {
        const int n = (c + 2 * (et & 3)) & 7;
        issue_v(n, n);
      }
    }
    tag = load_tag(p);
  } else {
    const unsigned dl = (wave * 10 + 8) * 4096;
    if constexpr (PH & PH_D) {
      const int k2 = wave & 1, nfr = wave < 2 ? 3 : 2;
      {
        unsigned qo[2];
#pragma unroll
        for (int i = 0; i < 2; ++i) {
          const int row = 8 * i + (lane >> 3);
          qo[i] = row * 6144 + (((lane & 7) ^ ((row >> 1) & 7)) << 4) - i * 1024;
        }
        const char* qb = p.qw + (size_t)(80 * det + 16 * (wave >> 1)) * 6144 + (64 * dkp + 32 * k2) * 4;
#pragma unroll
        for (int i = 0; i < 3; ++i)
          if (i < nfr) {
            glds<0, false>(qb + (size_t)i * (32 * 6144), qo[0], dl + i * 2048);
            glds<1024, false>(qb + (size_t)i * (32 * 6144), qo[1], dl + i * 2048);
          }
        int i = (lane >> 3) & 3; if (i >= nfr) i = 0;
        const int grp = (5 * det + (wave >> 1) + 2 * i) >> 1;
        const char* sa = (((lane >> 2) & 1) ? p.zr : p.sc) + (size_t)grp * 3072 + (64 * dkp + 32 * k2) * 2 + (lane & 3) * 16;
        glds_v(sa, dl + 6144);
      }
      tag = load_tag(p);
      WAIT_VM(0);
      STAMP(1);
      const int r = lane & 15, q = lane >> 4;
      i32x4 q0[3], q1[3]; u32x4 sv[3], zv[3];
#pragma unroll
      for (int i = 0; i < 3; ++i) {
        const unsigned a = dl + i * 2048 + r * 128;
        q0[i] = __builtin_bit_cast(i32x4, lds_rd128(a + (((2 * q) ^ ((r >> 1) & 7)) << 4)));
        q1[i] = __builtin_bit_cast(i32x4, lds_rd128(a + (((2 * q + 1) ^ ((r >> 1) & 7)) << 4)));
        sv[i] = lds_rd128(dl + 6144 + i * 128 + q * 16);
        zv[i] = lds_rd128(dl + 6144 + i * 128 + 64 + q * 16);
      }
      FENCE();
#pragma unroll
      for (int i = 0; i < 3; ++i)
        if (i < nfr) {
          const u32x4 s = sv[i], z = zv[i];
          u32x4 o;
          o[0] = dequant2(q0[i][0], q0[i][1], bf_lo(z[0]), bf_hi(z[0]), bf_lo(s[0]), bf_hi(s[0]));
          o[1] = dequant2(q0[i][2], q0[i][3], bf_lo(z[1]), bf_hi(z[1]), bf_lo(s[1]), bf_hi(s[1]));
          o[2] = dequant2(q1[i][0], q1[i][1], bf_lo(z[2]), bf_hi(z[2]), bf_lo(s[2]), bf_hi(s[2]));
          o[3] = dequant2(q1[i][2], q1[i][3], bf_lo(z[3]), bf_hi(z[3]), bf_lo(s[3]), bf_hi(s[3]));
          __builtin_amdgcn_raw_buffer_store_b128(o, wsr, lane * 16, WS_WQ + ((det * 48 + 2 * dkp + k2) * 5 + (wave >> 1) + 2 * i) * 1024, 16);
        }
      WAIT_LGKM0();
      STAMP(2);
      publish(p.ws, WS_DF + (dj * 4 + wave) * 8, tag, lane);
      STAMP(3);
    } else {
      tag = load_tag(p);
    }
    if constexpr (PH & PH_W) {
      issue_v(8, 8);
      issue_v(9, 9);
    }
  }
  if constexpr (PH & PH_W) {
    STAMP(4);
    if constexpr (PH & PH_D) {
      if (wave == 0) wait_flags2(wsr, WS_DF + et * 96 * 8 + (lane < 48 ? lane : 0) * 16, tag);
    }
    asm volatile("" :: "s"(tag));
    WAIT_VM(0);
    BAR();
    STAMP(5);
    unsigned va[4];
    {
      const int g = lane >> 4, qq = (lane & 15) >> 2, sw = ((qq >> 1) & 1) | ((g & 1) << 1);
#pragma unroll
      for (int c = 0; c < 4; ++c) va[c] = kq * 40960 + 128 * (8 * g + qq) + 32 * (c ^ sw) + 8 * (lane & 3);
    }
    const unsigned red = kq * 40960 + 8192 + lane * 16;
    auto body = [&](auto nbc) {
      constexpr int NB = decltype(nbc)::value, M0 = NB == 3 ? 0 : 3, DEPTH = WD;
      const unsigned ab = WS_WQ + ((et * 48 + kq * 12) * 5 + M0) * 1024;
      u32x4 af[DEPTH + 1][NB];
      f32x4 acc[NB][4];
#pragma unroll
      for (int m = 0; m < NB; ++m)
#pragma unroll
        for (int c = 0; c < 4; ++c) acc[m][c] = (f32x4){0.f, 0.f, 0.f, 0.f};
      s16x4 bh[2][8];
      static_for<0, 12 + DEPTH>([&](auto ic) {
        constexpr int n = decltype(ic)::value;
        constexpr int i = n - DEPTH;
        if constexpr (i + 1 == 10) BAR();
        const unsigned so = ab + kstep(n < 12 ? n : 0) * 5120;
        static_for<0, NB * 4>([&](auto jc) {
          constexpr int j = decltype(jc)::value, m = j / 4, c = j % 4;
          if constexpr (i >= 0) {
            const bf16x8 bv = __builtin_bit_cast(bf16x8, __builtin_shufflevector(bh[i & 1][2 * c], bh[i & 1][2 * c + 1], 0, 1, 2, 3, 4, 5, 6, 7));
            acc[m][c] = __builtin_amdgcn_mfma_f32_16x16x32_bf16(__builtin_bit_cast(bf16x8, af[i % (DEPTH + 1)][m]), bv, acc[m][c], 0, 0, 0);
          }
          if constexpr (i + 1 >= 0 && i + 1 < 12 && j < 8)
            bh[(i + 1) & 1][j] = __builtin_amdgcn_ds_read_tr16_b64_v4i16((LAS s16x4*)(size_t)(va[j >> 1] + ((i + 1) % 10) * 4096 + (j & 1) * 512));
          if constexpr (n < 12 && c == 3) {
            af[n % (DEPTH + 1)][m] = __builtin_amdgcn_raw_buffer_load_b128(wsr, lane * 16 + m * 1024, so, AAUX);
          }
          __builtin_amdgcn_sched_barrier(0);
        });
        if constexpr (i == 1) {
          BAR();
          issue_v(10 + (NB == 3), NB == 3);
        }
      });
      STAMP(6);
      if constexpr (NB == 2) {
#pragma unroll
        for (int m = 0; m < NB; ++m)
#pragma unroll
          for (int c = 0; c < 4; ++c) lds_wr128(red + ((M0 + m) * 4 + c) * 1024, __builtin_bit_cast(u32x4, acc[m][c]));
        WAIT_LGKM0();
        BAR();
        BAR();
      } else {
        __builtin_amdgcn_s_setprio(3);
#pragma unroll
        for (int fi = 0; fi < 6; ++fi)
          if ((fi & 3) != kq) {
            lds_wr128(red + fi * 2048, __builtin_bit_cast(u32x4, acc[fi >> 1][2 * (fi & 1)]));
            lds_wr128(red + fi * 2048 + 1024, __builtin_bit_cast(u32x4, acc[fi >> 1][2 * (fi & 1) + 1]));
          }
        f32x4 sa[3], sb[3];
        sa[0] = kq == 0 ? acc[0][0] : kq == 1 ? acc[0][2] : kq == 2 ? acc[1][0] : acc[1][2];
        sb[0] = kq == 0 ? acc[0][1] : kq == 1 ? acc[0][3] : kq == 2 ? acc[1][1] : acc[1][3];
        sa[1] = kq == 0 ? acc[2][0] : acc[2][2];
        sb[1] = kq == 0 ? acc[2][1] : acc[2][3];
        WAIT_LGKM0();
        BAR();
        const unsigned rb = 8192 + lane * 16 + kq * 2048;
        const unsigned ol = ((lane & 15) + 16 * (lane >> 5) + 32 * ((lane >> 4) & 1)) * 16;
        auto rd = [&](int q, int j, int hf) { return __builtin_bit_cast(f32x4, lds_rd128(rb + q * 40960 + j * 8192 + hf * 1024)); };
        auto out = [&](int j) {
          const f32x4 a = sa[j], b = sb[j];
          auto r0 = __builtin_amdgcn_permlane32_swap(pack_bf16(a[0], a[1]), pack_bf16(b[0], b[1]), false, false);
          auto r1 = __builtin_amdgcn_permlane32_swap(pack_bf16(a[2], a[3]), pack_bf16(b[2], b[3]), false, false);
          const u32x4 v = {r0[0], r1[0], r0[1], r1[1]};
          const int fi = kq + 4 * j;
          __builtin_amdgcn_raw_buffer_store_b128(v, wsr, ol, WS_W2 + ((2 * gt + (fi & 1)) * 40 + 5 * et + (fi >> 1)) * 1024, 16);
        };
        const int q1 = (kq + 1) & 3, q2 = (kq + 2) & 3, q3 = (kq + 3) & 3;
        if (kq < 2) {
          sa[2] = rd(kq, 2, 0); sb[2] = rd(kq, 2, 1);
#pragma unroll
          for (int j = 0; j < 3; ++j) {
            sa[j] += rd(q1, j, 0) + (rd(q2, j, 0) + rd(q3, j, 0));
            sb[j] += rd(q1, j, 1) + (rd(q2, j, 1) + rd(q3, j, 1));
          }
          out(0); out(1); out(2);
        } else {
          sa[1] = rd(kq, 1, 0); sb[1] = rd(kq, 1, 1);
#pragma unroll
          for (int j = 0; j < 2; ++j) {
            sa[j] += rd(q1, j, 0) + (rd(q2, j, 0) + rd(q3, j, 0));
            sb[j] += rd(q1, j, 1) + (rd(q2, j, 1) + rd(q3, j, 1));
          }
          out(0); out(1);
        }
        BAR();
        STAMP(7);
        publish(p.ws, WS_WF + ((gt * 8 + et) * 4 + kq) * 8, tag, lane);
      }
    };
    if (wave >= 4) body(IC<3>{}); else body(IC<2>{});
  }
  return tag;
}

template <int PH>
__device__ __forceinline__ void phase_y(const Params& p, __amdgpu_buffer_rsrc_t wsr, int wave, int lane, int xcd, int idx, u64 tag) {
  const int st = 4 * (xcd >> 1) + (idx >> 3), gt = 8 * (xcd & 1) + (idx & 7);
  constexpr bool FUSED = (PH & (PH_X | PH_W)) != 0;
  STAMP(8);
  if (wave >= 4) {
    const int lw = wave - 4;
    __builtin_amdgcn_s_setprio(3);
    if constexpr (FUSED) {
      wait_flags(p.ws, WS_WF + ((3 * gt / 2) * 32 + lane) * 8, tag);
      if (blockIdx.x == 0) {
        const int w = lw * 64 + lane;
        wait_flags(p.ws, w < 64 ? WS_XF + ((w >> 2) * 16 + (w & 3)) * 8 : WS_WF + (w - 64) * 32, tag);
      }
    }
    STAMP(11);
    const char* wb = p.ws + WS_W2 + (size_t)(3 * gt * 40 + lw) * 1024;
    const unsigned vo = lane * 16;
    auto issue = [&](int c) {
#pragma unroll
      for (int t = 0; t < 3; ++t) glds<0, true>(wb + c * 4096 + t * 40960, vo, c * 12288 + lw * 1024 + t * 4096);
    };
    issue(0); issue(1); issue(2);
    WAIT_VM(6); BAR();
    if constexpr (FUSED) { if (blockIdx.x == 0 && threadIdx.x == 256) __hip_atomic_store((gu64*)(p.ws + WS_EPOCH), tag, RLX_AGENT); }
    issue(3); issue(4);
    WAIT_VM(9); BAR();
    issue(5); issue(6);
    WAIT_VM(9); BAR();
    issue(7); issue(8); issue(9);
    WAIT_VM(9); BAR();
    WAIT_VM(0); BAR();
    return;
  }
  if constexpr (FUSED) wait_flags(p.ws, WS_XF + ((st * 4 + wave) * 4 + (lane & 3)) * 8, tag);
  STAMP(9);
  u32x4 xf[40];
  const unsigned xbase = WS_XR + (st * 4 + wave) * 40 * 1024;
#pragma unroll
  for (int ks = 0; ks < 40; ++ks) xf[ks] = __builtin_amdgcn_raw_buffer_load_b128(wsr, lane * 16 + (ks & 3) * 1024, xbase + (ks >> 2) * 4096, 16);
  FENCE();
  STAMP(10);
  f32x16 acc[3];
#pragma unroll
  for (int t = 0; t < 3; ++t)
#pragma unroll
    for (int j = 0; j < 16; ++j) acc[t][j] = 0.f;
  {
    constexpr int L = YL;
    bf16x8 aw[L + 1];
    static_for<0, 120 + L>([&](auto ic) {
      constexpr int n = decltype(ic)::value, j = n - L;
      if constexpr (n < 120) {
        constexpr int c = n / 12, kk = (n % 12) / 3, t = n % 3;
        if constexpr (n % 12 == 0 && (c == 0 || c == 1 || c == 2 || c == 4 || c == 7)) {
          BAR();
          if constexpr (c == 0) STAMP(11);
        }
        aw[n % (L + 1)] = __builtin_bit_cast(bf16x8, lds_rd128(c * 12288 + t * 4096 + kk * 1024 + lane * 16));
      }
      if constexpr (j >= 0) acc[j % 3] = __builtin_amdgcn_mfma_f32_32x32x16_bf16(aw[j % (L + 1)], __builtin_bit_cast(bf16x8, xf[j / 3]), acc[j % 3], 0, 0, 0);
      __builtin_amdgcn_sched_barrier(0);
    });
  }
  STAMP(12);
  char* yb = p.y + (size_t)(st * 128 + wave * 32 + (lane & 31)) * 3072 + gt * 192 + (lane >> 5) * 16;
#pragma unroll
  for (int t = 0; t < 3; ++t)
#pragma unroll
    for (int i = 0; i < 4; i += 2) {
      auto r0 = __builtin_amdgcn_permlane32_swap(pack_bf16(acc[t][4 * i], acc[t][4 * i + 1]), pack_bf16(acc[t][4 * i + 4], acc[t][4 * i + 5]), false, false);
      auto r1 = __builtin_amdgcn_permlane32_swap(pack_bf16(acc[t][4 * i + 2], acc[t][4 * i + 3]), pack_bf16(acc[t][4 * i + 6], acc[t][4 * i + 7]), false, false);
      const u32x4 v = {r0[0], r1[0], r0[1], r1[1]};
      *(GAS u32x4*)(yb + t * 64 + i * 16) = v;
    }
}

template <int PH>
__global__ void __launch_bounds__(512, 2) quip_kernel(Params p) {
  const int wave = __builtin_amdgcn_readfirstlane(threadIdx.x >> 6), lane = threadIdx.x & 63;
  const int xcd = blockIdx.x & 7, idx = blockIdx.x >> 3;
  STAMP(0); STAMPC(14);
  const __amdgpu_buffer_rsrc_t wsr = __builtin_amdgcn_make_buffer_rsrc(p.ws, 0, WS_END, 0x00020000);
  u64 tag = 0;
  if (idx < 8) {
    if constexpr (PH & PH_X) tag = phase_x(p, wsr, wave, lane, xcd, idx);
  } else {
    if constexpr (PH & (PH_D | PH_W)) tag = phase_dw<PH>(p, wsr, wave, lane, xcd, idx - 8);
  }
  if constexpr (PH & PH_Y) phase_y<PH>(p, wsr, wave, lane, xcd, idx, tag);
  STAMP(13); STAMPC(15);
  while (__builtin_amdgcn_s_memrealtime() < g_t0 + 5000 + g_d) __builtin_amdgcn_s_sleep(1);
}

#define NLAUNCH 1

template <int PH>
static void launch(const Params& p, hipStream_t stream) {
  (void)hipFuncSetAttribute((const void*)quip_kernel<PH>, hipFuncAttributeMaxDynamicSharedMemorySize, LDS_BYTES);
  quip_kernel<PH><<<256, 512, LDS_BYTES, stream>>>(p);
}

extern "C" void kernel_launch(void* const* d_in, const int* in_sizes, int n_in, void* d_out, int out_size, void* d_ws,
                              size_t ws_size, hipStream_t stream) {
  Params p;
  p.x = (const char*)d_in[0]; p.qw = (const char*)d_in[1]; p.sc = (const char*)d_in[2]; p.zr = (const char*)d_in[3];
  p.U = (const char*)d_in[4]; p.V = (const char*)d_in[5]; p.y = (char*)d_out; p.ws = (char*)d_ws; p.t0 = 0; p.d = 0;
  launch<PH_ALL>(p, stream);
}
```

```cpp
#define PROBE 99
#define PROLE 1
#include <hip/hip_runtime.h>
#include <stdint.h>

#define LAS __attribute__((address_space(3)))
#define GAS __attribute__((address_space(1)))
typedef unsigned short u16;
typedef unsigned long long u64;
typedef short s16x4 __attribute__((ext_vector_type(4)));
typedef short s16x8 __attribute__((ext_vector_type(8)));
typedef __bf16 bf16x8 __attribute__((ext_vector_type(8)));
typedef __bf16 bf16x2 __attribute__((ext_vector_type(2)));
typedef float f32x2 __attribute__((ext_vector_type(2)));
typedef float f32x4 __attribute__((ext_vector_type(4)));
typedef float f32x16 __attribute__((ext_vector_type(16)));
typedef unsigned u32x4 __attribute__((ext_vector_type(4)));
typedef int i32x4 __attribute__((ext_vector_type(4)));
typedef GAS u64 gu64;

constexpr int S_ = 2048, D_ = 640, F_ = 1536;
constexpr unsigned WS_EPOCH = 0;
constexpr unsigned WS_DF = 4096;
constexpr unsigned WS_XF = 16384;
constexpr unsigned WS_WF = 20480;
constexpr unsigned WS_STAMP = 32768;
constexpr unsigned WS_WQ = 1u << 20;
constexpr unsigned WS_XR = WS_WQ + D_ * F_ * 2;
constexpr unsigned WS_W2 = WS_XR + S_ * D_ * 2;
constexpr unsigned WS_END = WS_W2 + D_ * F_ * 2;
constexpr int LDS_BYTES = 160 * 1024;

#define ROTC 2
#define AAUX 16
#define XL 5
#define YL 6
#define WD 2

enum { PH_D = 1, PH_X = 2, PH_W = 4, PH_Y = 8, PH_ALL = 15 };

struct Params {
  const char* x; const char* qw; const char* sc; const char* zr; const char* U; const char* V; char* y; char* ws;
  mutable unsigned long long t0, d;
};

#define g_t0 (p.t0)
#define g_d (p.d)
#define RLX_AGENT __ATOMIC_RELAXED, __HIP_MEMORY_SCOPE_AGENT
#define WAIT_VM(n) asm volatile("s_waitcnt vmcnt(%0)" :: "n"(n) : "memory")
#define WAIT_LGKM0() asm volatile("s_waitcnt lgkmcnt(0)" ::: "memory")
#define BAR() do { asm volatile("" ::: "memory"); __builtin_amdgcn_s_barrier(); asm volatile("" ::: "memory"); } while (0)
#define FENCE() asm volatile("" ::: "memory")

#define STAMP(i) do { if ((i) == 0) g_t0 = __builtin_amdgcn_s_memrealtime(); if ((i) == PROBE && (PROLE)) g_d = __builtin_amdgcn_s_memrealtime() - g_t0; } while (0)
#define STAMPC(i) do {} while (0)

template <int I> struct IC { static constexpr int value = I; };
template <int B, int E, class Fn>
__device__ __forceinline__ void static_for(Fn&& fn) {
  if constexpr (B < E) {
    if constexpr (E - B == 1) fn(IC<B>{});
    else { static_for<B, (B + E) / 2>(fn); static_for<(B + E) / 2, E>(fn); }
  }
}

template <int OFF, bool SC1>
__device__ __forceinline__ void glds(const char* sbase, unsigned voff, unsigned lds) {
  if constexpr (SC1)
    asm volatile("s_mov_b32 m0, %2\n\ts_nop 0\n\tglobal_load_lds_dwordx4 %0, %1 offset:%3 sc1" :: "v"(voff), "s"(sbase), "s"(lds), "n"(OFF) : "memory");
  else
    asm volatile("s_mov_b32 m0, %2\n\ts_nop 0\n\tglobal_load_lds_dwordx4 %0, %1 offset:%3" :: "v"(voff), "s"(sbase), "s"(lds), "n"(OFF) : "memory");
}
__device__ __forceinline__ void glds4(const char* sbase, unsigned v0, unsigned v1, unsigned v2, unsigned v3, unsigned lds) {
  asm volatile("s_mov_b32 m0, %5\n\ts_nop 0\n\tglobal_load_lds_dwordx4 %0, %4\n\tglobal_load_lds_dwordx4 %1, %4 offset:1024\n\t"
               "global_load_lds_dwordx4 %2, %4 offset:2048\n\tglobal_load_lds_dwordx4 %3, %4 offset:3072"
               :: "v"(v0), "v"(v1), "v"(v2), "v"(v3), "s"(sbase), "s"(lds) : "memory");
}
__device__ __forceinline__ void glds_v(const char* vaddr, unsigned lds) {
  asm volatile("s_mov_b32 m0, %1\n\ts_nop 0\n\tglobal_load_lds_dwordx4 %0, off" :: "v"(vaddr), "s"(lds) : "memory");
}

__device__ __forceinline__ unsigned pack_bf16(float a, float b) {
  f32x2 v = {a, b};
  return __builtin_bit_cast(unsigned, __builtin_convertvector(v, bf16x2));
}
__device__ __forceinline__ float bf_lo(unsigned v) { return __builtin_bit_cast(float, v << 16); }
__device__ __forceinline__ float bf_hi(unsigned v) { return __builtin_bit_cast(float, v & 0xffff0000u); }

__device__ __forceinline__ u32x4 lds_rd128(unsigned a) { return *(LAS u32x4*)(size_t)a; }
__device__ __forceinline__ void lds_wr128(unsigned a, u32x4 v) { *(LAS u32x4*)(size_t)a = v; }
__device__ __forceinline__ bf16x8 lds_tr2(unsigned a0, unsigned a1) {
  s16x4 lo = __builtin_amdgcn_ds_read_tr16_b64_v4i16((LAS s16x4*)(size_t)a0);
  s16x4 hi = __builtin_amdgcn_ds_read_tr16_b64_v4i16((LAS s16x4*)(size_t)a1);
  return __builtin_bit_cast(bf16x8, __builtin_shufflevector(lo, hi, 0, 1, 2, 3, 4, 5, 6, 7));
}

__device__ __forceinline__ u64 mix64(u64 z) {
  z += 0x9E3779B97F4A7C15ull;
  z = (z ^ (z >> 30)) * 0xBF58476D1CE4E5B9ull;
  z = (z ^ (z >> 27)) * 0x94D049BB133111EBull;
  return z ^ (z >> 31);
}

__device__ __forceinline__ u64 load_tag(const Params& p) {
  const char* e = p.ws + WS_EPOCH;
  asm volatile("" : "+s"(e));
  return mix64(*(const __attribute__((address_space(4))) u64*)e);
}

__device__ __forceinline__ void wait_flags(const char* ws, unsigned off, u64 tag) {
  gu64* f = (gu64*)(ws + off);
  for (unsigned spins = 0; spins < (1u << 20); ++spins) {
    u64 v = __hip_atomic_load(f, RLX_AGENT);
    if (__all(v == tag)) break;
    __builtin_amdgcn_s_sleep(2);
  }
  FENCE();
}
__device__ __forceinline__ void wait_flags2(__amdgpu_buffer_rsrc_t wsr, unsigned off, u64 tag) {
  const unsigned lo = (unsigned)tag, hi = (unsigned)(tag >> 32);
  for (unsigned spins = 0; spins < (1u << 20); ++spins) {
    FENCE();
    const u32x4 v = __builtin_amdgcn_raw_buffer_load_b128(wsr, off, 0, 16);
    if (__all(v[0] == lo && v[1] == hi && v[2] == lo && v[3] == hi)) break;
    __builtin_amdgcn_s_sleep(2);
  }
  FENCE();
}
__device__ __forceinline__ void publish(char* ws, unsigned off, u64 tag, int lane) {
  WAIT_VM(0);
  if (lane == 0) __hip_atomic_store((gu64*)(ws + off), tag, RLX_AGENT);
  FENCE();
}

__device__ __forceinline__ unsigned dequant2(int q0, int q1, float z0, float z1, float s0, float s1) {
  unsigned qb = pack_bf16((float)q0, (float)q1);
  unsigned t = pack_bf16(bf_lo(qb) - z0, bf_hi(qb) - z1);
  return pack_bf16(bf_lo(t) * s0, bf_hi(t) * s1);
}

__device__ __forceinline__ u64 phase_x(const Params& p, __amdgpu_buffer_rsrc_t wsr, int wave, int lane, int xcd, int k) {
  const int st = 2 * xcd + (k >> 2), etl = k & 3;
  constexpr unsigned STG = 36 * 1024, UOFF = 16 * 1024;
  if (wave >= 4) {
    const int lw = wave - 4;
    const int rot = (3 * etl + 5 * (k >> 2) + xcd) % 10;
    unsigned xo[4];
#pragma unroll
    for (int i = 0; i < 4; ++i) {
      const int row = 8 * i + (lane >> 3);
      xo[i] = row * 1280 + (((lane & 7) ^ ((row >> 1) & 7)) << 4) - i * 1024;
    }
    unsigned uo[5];
#pragma unroll
    for (int i = 0; i < 5; ++i) {
      const int u = i * 64 + lane, row = (u * 3277) >> 16;
      uo[i] = row * 1280 + (u - row * 20) * 16 - (i & 3) * 1024;
    }
    const char* xb = p.x + (size_t)(st * 128 + lw * 32) * 1280;
    const char* ub = p.U + etl * 320 + lw * (16 * 1280);
    auto issue = [&](int i) {
      int kt = i + rot; if (kt >= 10) kt -= 10;
      const unsigned slot = (i & 3) * STG;
      const char* us = ub + (size_t)kt * (64 * 1280);
      glds4(us, uo[0], uo[1], uo[2], uo[3], slot + UOFF + lw * 5120);
      glds<0, false>(us, uo[4], slot + UOFF + lw * 5120 + 4096);
      glds4(xb + kt * 128, xo[0], xo[1], xo[2], xo[3], slot + lw * 4096);
    };
    issue(0); issue(1);
    const u64 tag = load_tag(p);
    asm volatile("" :: "s"(tag));
    WAIT_VM(9); BAR();
    issue(2); issue(3);
    WAIT_VM(18); BAR();
    WAIT_VM(9); BAR();
#pragma unroll
    for (int i = 3; i < 10; ++i) {
      if (i + 1 < 10) { issue(i + 1); WAIT_VM(9); } else WAIT_VM(0);
      BAR();
    }
    BAR();
    return tag;
  }
  const int r = lane & 31, h = lane >> 5;
  unsigned xa[4];
#pragma unroll
  for (int kk = 0; kk < 4; ++kk) xa[kk] = wave * 4096 + r * 128 + (((2 * kk + h) ^ ((r >> 1) & 7)) << 4);
  const unsigned ua = UOFF + (8 * h + ((lane & 15) >> 2)) * 320 + ((lane >> 4) & 1) * 32 + (lane & 3) * 8;
  f32x16 acc[5];
#pragma unroll
  for (int t = 0; t < 5; ++t)
#pragma unroll
    for (int j = 0; j < 16; ++j) acc[t][j] = 0.f;
  const u64 tag = load_tag(p);
  asm volatile("" :: "s"(tag));
  {
    constexpr int L = XL;
    bf16x8 au[L + 1], bx[3];
    static_for<0, 200 + L>([&](auto ic) {
      constexpr int n = decltype(ic)::value;
      constexpr int j = n - L;
      if constexpr (n < 200) {
        constexpr int i = n / 20, kk = (n % 20) / 5, t = n % 5;
        constexpr unsigned slot = (i & 3) * STG;
        if constexpr (n % 20 == 0) {
          BAR();
          if constexpr (i == 0) STAMP(1); if constexpr (i == 5) STAMP(4);
        }
        if constexpr (t == 0) bx[(n / 5) % 3] = __builtin_bit_cast(bf16x8, lds_rd128(slot + xa[kk]));
        const unsigned ad = slot + ua + kk * 16 * 320 + t * 64;
        au[n % (L + 1)] = lds_tr2(ad, ad + 4 * 320);
      }
      if constexpr (j >= 0) acc[j % 5] = __builtin_amdgcn_mfma_f32_32x32x16_bf16(au[j % (L + 1)], bx[(j / 5) % 3], acc[j % 5], 0, 0, 0);
      __builtin_amdgcn_sched_barrier(0);
    });
  }
  BAR();
  STAMP(6);
  const unsigned ob = WS_XR + ((st * 4 + wave) * 40 + etl * 10) * 1024;
#pragma unroll
  for (int t = 0; t < 5; ++t)
#pragma unroll
    for (int s2 = 0; s2 < 2; ++s2) {
      u32x4 v;
#pragma unroll
      for (int j = 0; j < 4; ++j) v[j] = pack_bf16(acc[t][8 * s2 + 2 * j], acc[t][8 * s2 + 2 * j + 1]);
      __builtin_amdgcn_raw_buffer_store_b128(v, wsr, lane * 16, ob + (2 * t + s2) * 1024, 16);
    }
  STAMP(7);
  publish(p.ws, WS_XF + ((st * 4 + wave) * 4 + etl) * 8, tag, lane);
  return tag;
}

template <int PH>
__device__ __forceinline__ u64 phase_dw(const Params& p, __amdgpu_buffer_rsrc_t wsr, int wave, int lane, int xcd, int k) {
  const int et = 4 * (xcd >> 2) + k / 6, gt = 6 * (xcd & 3) + k % 6;
  const int dj = xcd * 24 + k, det = xcd, dkp = k;
  const int rot = ROTC * (k % 6);
  const int kq = wave & 3;
  unsigned vo[4];
#pragma unroll
  for (int pc = 0; pc < 4; ++pc) {
    const int row = 8 * pc + (lane >> 3), swz = ((row >> 1) & 1) | (((row >> 3) & 1) << 1);
    vo[pc] = row * 3072 + ((((lane & 7) >> 1) ^ swz) << 5) + ((lane & 1) << 4) - pc * 1024;
  }
  const char* vb = p.V + gt * 128 + (size_t)kq * (12 * 32 * 3072);
  auto kstep = [&](int i) { int t = i + rot; return t >= 12 ? t - 12 : t; };
  auto issue_v = [&](int i, int sl) {
    glds4(vb + (size_t)kstep(i) * (32 * 3072), vo[0], vo[1], vo[2], vo[3], (kq * 10 + sl) * 4096);
  };
  u64 tag;
  if (wave >= 4) {
    if constexpr (PH & PH_W) {
#pragma unroll
      for (int c = 0; c < 8; ++c) {
        const int n = (c + 2 * (et & 3)) & 7;
        issue_v(n, n);
      }
    }
    tag = load_tag(p);
  } else {
    const unsigned dl = (wave * 10 + 8) * 4096;
    if constexpr (PH & PH_D) {
      const int k2 = wave & 1, nfr = wave < 2 ? 3 : 2;
      {
        unsigned qo[2];
#pragma unroll
        for (int i = 0; i < 2; ++i) {
          const int row = 8 * i + (lane >> 3);
          qo[i] = row * 6144 + (((lane & 7) ^ ((row >> 1) & 7)) << 4) - i * 1024;
        }
        const char* qb = p.qw + (size_t)(80 * det + 16 * (wave >> 1)) * 6144 + (64 * dkp + 32 * k2) * 4;
#pragma unroll
        for (int i = 0; i < 3; ++i)
          if (i < nfr) {
            glds<0, false>(qb + (size_t)i * (32 * 6144), qo[0], dl + i * 2048);
            glds<1024, false>(qb + (size_t)i * (32 * 6144), qo[1], dl + i * 2048);
          }
        int i = (lane >> 3) & 3; if (i >= nfr) i = 0;
        const int grp = (5 * det + (wave >> 1) + 2 * i) >> 1;
        const char* sa = (((lane >> 2) & 1) ? p.zr : p.sc) + (size_t)grp * 3072 + (64 * dkp + 32 * k2) * 2 + (lane & 3) * 16;
        glds_v(sa, dl + 6144);
      }
      tag = load_tag(p);
      WAIT_VM(0);
      STAMP(1);
      const int r = lane & 15, q = lane >> 4;
      i32x4 q0[3], q1[3]; u32x4 sv[3], zv[3];
#pragma unroll
      for (int i = 0; i < 3; ++i) {
        const unsigned a = dl + i * 2048 + r * 128;
        q0[i] = __builtin_bit_cast(i32x4, lds_rd128(a + (((2 * q) ^ ((r >> 1) & 7)) << 4)));
        q1[i] = __builtin_bit_cast(i32x4, lds_rd128(a + (((2 * q + 1) ^ ((r >> 1) & 7)) << 4)));
        sv[i] = lds_rd128(dl + 6144 + i * 128 + q * 16);
        zv[i] = lds_rd128(dl + 6144 + i * 128 + 64 + q * 16);
      }
      FENCE();
#pragma unroll
      for (int i = 0; i < 3; ++i)
        if (i < nfr) {
          const u32x4 s = sv[i], z = zv[i];
          u32x4 o;
          o[0] = dequant2(q0[i][0], q0[i][1], bf_lo(z[0]), bf_hi(z[0]), bf_lo(s[0]), bf_hi(s[0]));
          o[1] = dequant2(q0[i][2], q0[i][3], bf_lo(z[1]), bf_hi(z[1]), bf_lo(s[1]), bf_hi(s[1]));
          o[2] = dequant2(q1[i][0], q1[i][1], bf_lo(z[2]), bf_hi(z[2]), bf_lo(s[2]), bf_hi(s[2]));
          o[3] = dequant2(q1[i][2], q1[i][3], bf_lo(z[3]), bf_hi(z[3]), bf_lo(s[3]), bf_hi(s[3]));
          __builtin_amdgcn_raw_buffer_store_b128(o, wsr, lane * 16, WS_WQ + ((det * 48 + 2 * dkp + k2) * 5 + (wave >> 1) + 2 * i) * 1024, 16);
        }
      WAIT_LGKM0();
      STAMP(2);
      publish(p.ws, WS_DF + (dj * 4 + wave) * 8, tag, lane);
      STAMP(3);
    } else {
      tag = load_tag(p);
    }
    if constexpr (PH & PH_W) {
      issue_v(8, 8);
      issue_v(9, 9);
    }
  }
  if constexpr (PH & PH_W) {
    STAMP(4);
    if constexpr (PH & PH_D) {
      if (wave == 0) wait_flags2(wsr, WS_DF + et * 96 * 8 + (lane < 48 ? lane : 0) * 16, tag);
    }
    asm volatile("" :: "s"(tag));
    WAIT_VM(0);
    BAR();
    STAMP(5);
    unsigned va[4];
    {
      const int g = lane >> 4, qq = (lane & 15) >> 2, sw = ((qq >> 1) & 1) | ((g & 1) << 1);
#pragma unroll
      for (int c = 0; c < 4; ++c) va[c] = kq * 40960 + 128 * (8 * g + qq) + 32 * (c ^ sw) + 8 * (lane & 3);
    }
    const unsigned red = kq * 40960 + 8192 + lane * 16;
    auto body = [&](auto nbc) {
      constexpr int NB = decltype(nbc)::value, M0 = NB == 3 ? 0 : 3, DEPTH = WD;
      const unsigned ab = WS_WQ + ((et * 48 + kq * 12) * 5 + M0) * 1024;
      u32x4 af[DEPTH + 1][NB];
      f32x4 acc[NB][4];
#pragma unroll
      for (int m = 0; m < NB; ++m)
#pragma unroll
        for (int c = 0; c < 4; ++c) acc[m][c] = (f32x4){0.f, 0.f, 0.f, 0.f};
      s16x4 bh[2][8];
      static_for<0, 12 + DEPTH>([&](auto ic) {
        constexpr int n = decltype(ic)::value;
        constexpr int i = n - DEPTH;
        if constexpr (i + 1 == 10) BAR();
        const unsigned so = ab + kstep(n < 12 ? n : 0) * 5120;
        static_for<0, NB * 4>([&](auto jc) {
          constexpr int j = decltype(jc)::value, m = j / 4, c = j % 4;
          if constexpr (i >= 0) {
            const bf16x8 bv = __builtin_bit_cast(bf16x8, __builtin_shufflevector(bh[i & 1][2 * c], bh[i & 1][2 * c + 1], 0, 1, 2, 3, 4, 5, 6, 7));
            acc[m][c] = __builtin_amdgcn_mfma_f32_16x16x32_bf16(__builtin_bit_cast(bf16x8, af[i % (DEPTH + 1)][m]), bv, acc[m][c], 0, 0, 0);
          }
          if constexpr (i + 1 >= 0 && i + 1 < 12 && j < 8)
            bh[(i + 1) & 1][j] = __builtin_amdgcn_ds_read_tr16_b64_v4i16((LAS s16x4*)(size_t)(va[j >> 1] + ((i + 1) % 10) * 4096 + (j & 1) * 512));
          if constexpr (n < 12 && c == 3) {
            af[n % (DEPTH + 1)][m] = __builtin_amdgcn_raw_buffer_load_b128(wsr, lane * 16 + m * 1024, so, AAUX);
          }
          __builtin_amdgcn_sched_barrier(0);
        });
        if constexpr (i == 1) {
          BAR();
          issue_v(10 + (NB == 3), NB == 3);
        }
      });
      STAMP(6);
      if constexpr (NB == 2) {
#pragma unroll
        for (int m = 0; m < NB; ++m)
#pragma unroll
          for (int c = 0; c < 4; ++c) lds_wr128(red + ((M0 + m) * 4 + c) * 1024, __builtin_bit_cast(u32x4, acc[m][c]));
        WAIT_LGKM0();
        BAR();
        BAR();
      } else {
        __builtin_amdgcn_s_setprio(3);
#pragma unroll
        for (int fi = 0; fi < 6; ++fi)
          if ((fi & 3) != kq) {
            lds_wr128(red + fi * 2048, __builtin_bit_cast(u32x4, acc[fi >> 1][2 * (fi & 1)]));
            lds_wr128(red + fi * 2048 + 1024, __builtin_bit_cast(u32x4, acc[fi >> 1][2 * (fi & 1) + 1]));
          }
        f32x4 sa[3], sb[3];
        sa[0] = kq == 0 ? acc[0][0] : kq == 1 ? acc[0][2] : kq == 2 ? acc[1][0] : acc[1][2];
        sb[0] = kq == 0 ? acc[0][1] : kq == 1 ? acc[0][3] : kq == 2 ? acc[1][1] : acc[1][3];
        sa[1] = kq == 0 ? acc[2][0] : acc[2][2];
        sb[1] = kq == 0 ? acc[2][1] : acc[2][3];
        WAIT_LGKM0();
        BAR();
        const unsigned rb = 8192 + lane * 16 + kq * 2048;
        const unsigned ol = ((lane & 15) + 16 * (lane >> 5) + 32 * ((lane >> 4) & 1)) * 16;
        auto rd = [&](int q, int j, int hf) { return __builtin_bit_cast(f32x4, lds_rd128(rb + q * 40960 + j * 8192 + hf * 1024)); };
        auto out = [&](int j) {
          const f32x4 a = sa[j], b = sb[j];
          auto r0 = __builtin_amdgcn_permlane32_swap(pack_bf16(a[0], a[1]), pack_bf16(b[0], b[1]), false, false);
          auto r1 = __builtin_amdgcn_permlane32_swap(pack_bf16(a[2], a[3]), pack_bf16(b[2], b[3]), false, false);
          const u32x4 v = {r0[0], r1[0], r0[1], r1[1]};
          const int fi = kq + 4 * j;
          __builtin_amdgcn_raw_buffer_store_b128(v, wsr, ol, WS_W2 + ((2 * gt + (fi & 1)) * 40 + 5 * et + (fi >> 1)) * 1024, 16);
        };
        const int q1 = (kq + 1) & 3, q2 = (kq + 2) & 3, q3 = (kq + 3) & 3;
        if (kq < 2) {
          sa[2] = rd(kq, 2, 0); sb[2] = rd(kq, 2, 1);
#pragma unroll
          for (int j = 0; j < 3; ++j) {
            sa[j] += rd(q1, j, 0) + (rd(q2, j, 0) + rd(q3, j, 0));
            sb[j] += rd(q1, j, 1) + (rd(q2, j, 1) + rd(q3, j, 1));
          }
          out(0); out(1); out(2);
        } else {
          sa[1] = rd(kq, 1, 0); sb[1] = rd(kq, 1, 1);
#pragma unroll
          for (int j = 0; j < 2; ++j) {
            sa[j] += rd(q1, j, 0) + (rd(q2, j, 0) + rd(q3, j, 0));
            sb[j] += rd(q1, j, 1) + (rd(q2, j, 1) + rd(q3, j, 1));
          }
          out(0); out(1);
        }
        BAR();
        STAMP(7);
        publish(p.ws, WS_WF + ((gt * 8 + et) * 4 + kq) * 8, tag, lane);
      }
    };
    if (wave >= 4) body(IC<3>{}); else body(IC<2>{});
  }
  return tag;
}

template <int PH>
__device__ __forceinline__ void phase_y(const Params& p, __amdgpu_buffer_rsrc_t wsr, int wave, int lane, int xcd, int idx, u64 tag) {
  const int st = 4 * (xcd >> 1) + (idx >> 3), gt = 8 * (xcd & 1) + (idx & 7);
  constexpr bool FUSED = (PH & (PH_X | PH_W)) != 0;
  STAMP(8);
  if (wave >= 4) {
    const int lw = wave - 4;
    __builtin_amdgcn_s_setprio(3);
    if constexpr (FUSED) {
      wait_flags(p.ws, WS_WF + ((3 * gt / 2) * 32 + lane) * 8, tag);
      if (blockIdx.x == 0) {
        const int w = lw * 64 + lane;
        wait_flags(p.ws, w < 64 ? WS_XF + ((w >> 2) * 16 + (w & 3)) * 8 : WS_WF + (w - 64) * 32, tag);
      }
    }
    STAMP(11);
    const char* wb = p.ws + WS_W2 + (size_t)(3 * gt * 40 + lw) * 1024;
    const unsigned vo = lane * 16;
    auto issue = [&](int c) {
#pragma unroll
      for (int t = 0; t < 3; ++t) glds<0, true>(wb + c * 4096 + t * 40960, vo, c * 12288 + lw * 1024 + t * 4096);
    };
    issue(0); issue(1); issue(2);
    WAIT_VM(6); BAR();
    if constexpr (FUSED) { if (blockIdx.x == 0 && threadIdx.x == 256) __hip_atomic_store((gu64*)(p.ws + WS_EPOCH), tag, RLX_AGENT); }
    issue(3); issue(4);
    WAIT_VM(9); BAR();
    issue(5); issue(6);
    WAIT_VM(9); BAR();
    issue(7); issue(8); issue(9);
    WAIT_VM(9); BAR();
    WAIT_VM(0); BAR();
    return;
  }
  if constexpr (FUSED) wait_flags(p.ws, WS_XF + ((st * 4 + wave) * 4 + (lane & 3)) * 8, tag);
  STAMP(9);
  u32x4 xf[40];
  const unsigned xbase = WS_XR + (st * 4 + wave) * 40 * 1024;
#pragma unroll
  for (int ks = 0; ks < 40; ++ks) xf[ks] = __builtin_amdgcn_raw_buffer_load_b128(wsr, lane * 16 + (ks & 3) * 1024, xbase + (ks >> 2) * 4096, 16);
  FENCE();
  STAMP(10);
  f32x16 acc[3];
#pragma unroll
  for (int t = 0; t < 3; ++t)
#pragma unroll
    for (int j = 0; j < 16; ++j) acc[t][j] = 0.f;
  {
    constexpr int L = YL;
    bf16x8 aw[L + 1];
    static_for<0, 120 + L>([&](auto ic) {
      constexpr int n = decltype(ic)::value, j = n - L;
      if constexpr (n < 120) {
        constexpr int c = n / 12, kk = (n % 12) / 3, t = n % 3;
        if constexpr (n % 12 == 0 && (c == 0 || c == 1 || c == 2 || c == 4 || c == 7)) {
          BAR();
          if constexpr (c == 0) STAMP(11);
        }
        aw[n % (L + 1)] = __builtin_bit_cast(bf16x8, lds_rd128(c * 12288 + t * 4096 + kk * 1024 + lane * 16));
      }
      if constexpr (j >= 0) acc[j % 3] = __builtin_amdgcn_mfma_f32_32x32x16_bf16(aw[j % (L + 1)], __builtin_bit_cast(bf16x8, xf[j / 3]), acc[j % 3], 0, 0, 0);
      __builtin_amdgcn_sched_barrier(0);
    });
  }
  STAMP(12);
  const __amdgpu_buffer_rsrc_t yr = __builtin_amdgcn_make_buffer_rsrc(p.y, 0, S_ * F_ * 2, 0x00020000);
  const unsigned yo = (st * 128 + wave * 32 + (lane & 31)) * 3072 + gt * 192 + (lane >> 5) * 16;
#pragma unroll
  for (int t = 0; t < 3; ++t)
#pragma unroll
    for (int i = 0; i < 4; i += 2) {
      auto r0 = __builtin_amdgcn_permlane32_swap(pack_bf16(acc[t][4 * i], acc[t][4 * i + 1]), pack_bf16(acc[t][4 * i + 4], acc[t][4 * i + 5]), false, false);
      auto r1 = __builtin_amdgcn_permlane32_swap(pack_bf16(acc[t][4 * i + 2], acc[t][4 * i + 3]), pack_bf16(acc[t][4 * i + 6], acc[t][4 * i + 7]), false, false);
      const u32x4 v = {r0[0], r1[0], r0[1], r1[1]};
      __builtin_amdgcn_raw_buffer_store_b128(v, yr, yo + t * 64 + i * 16, 0, 16);
    }
}

template <int PH>
__global__ void __launch_bounds__(512, 2) quip_kernel(Params p) {
  const int wave = __builtin_amdgcn_readfirstlane(threadIdx.x >> 6), lane = threadIdx.x & 63;
  const int xcd = blockIdx.x & 7, idx = blockIdx.x >> 3;
  STAMP(0); STAMPC(14);
  if (threadIdx.x == 0 || threadIdx.x == 256) __hip_atomic_store((gu64*)(p.ws + WS_STAMP + blockIdx.x * 16 + (threadIdx.x >> 8) * 8), g_t0, RLX_AGENT);
  const __amdgpu_buffer_rsrc_t wsr = __builtin_amdgcn_make_buffer_rsrc(p.ws, 0, WS_END, 0x00020000);
  u64 tag = 0;
  if (idx < 8) {
    if constexpr (PH & PH_X) tag = phase_x(p, wsr, wave, lane, xcd, idx);
  } else {
    if constexpr (PH & (PH_D | PH_W)) tag = phase_dw<PH>(p, wsr, wave, lane, xcd, idx - 8);
  }
  if constexpr (PH & PH_Y) phase_y<PH>(p, wsr, wave, lane, xcd, idx, tag);
  STAMP(13); STAMPC(15);
  if (blockIdx.x == 0 && wave == 0) {
    while (__builtin_amdgcn_s_memrealtime() < g_t0 + 3500) __builtin_amdgcn_s_sleep(1);
    u64 mn = ~0ull;
    for (int i = lane; i < 512; i += 64) { u64 v = __hip_atomic_load((gu64*)(p.ws + WS_STAMP + i * 8), RLX_AGENT); mn = v < mn ? v : mn; }
    for (int o = 32; o; o >>= 1) { u64 v = __shfl_xor(mn, o); mn = v < mn ? v : mn; }
    if (mn + 2000 < g_t0) mn = g_t0;
    while (__builtin_amdgcn_s_memrealtime() < mn + 5000) __builtin_amdgcn_s_sleep(1);
  }
}

#define NLAUNCH 1

template <int PH>
static void launch(const Params& p, hipStream_t stream) {
  (void)hipFuncSetAttribute((const void*)quip_kernel<PH>, hipFuncAttributeMaxDynamicSharedMemorySize, LDS_BYTES);
  quip_kernel<PH><<<256, 512, LDS_BYTES, stream>>>(p);
}

extern "C" void kernel_launch(void* const* d_in, const int* in_sizes, int n_in, void* d_out, int out_size, void* d_ws,
                              size_t ws_size, hipStream_t stream) {
  Params p;
  p.x = (const char*)d_in[0]; p.qw = (const char*)d_in[1]; p.sc = (const char*)d_in[2]; p.zr = (const char*)d_in[3];
  p.U = (const char*)d_in[4]; p.V = (const char*)d_in[5]; p.y = (char*)d_out; p.ws = (char*)d_ws; p.t0 = 0; p.d = 0;
  launch<PH_ALL>(p, stream);
}
```

```cpp
#include <hip/hip_runtime.h>
#include <stdint.h>

#define LAS __attribute__((address_space(3)))
#define GAS __attribute__((address_space(1)))
typedef unsigned short u16;
typedef unsigned long long u64;
typedef short s16x4 __attribute__((ext_vector_type(4)));
typedef short s16x8 __attribute__((ext_vector_type(8)));
typedef __bf16 bf16x8 __attribute__((ext_vector_type(8)));
typedef __bf16 bf16x2 __attribute__((ext_vector_type(2)));
typedef float f32x2 __attribute__((ext_vector_type(2)));
typedef float f32x4 __attribute__((ext_vector_type(4)));
typedef float f32x16 __attribute__((ext_vector_type(16)));
typedef unsigned u32x4 __attribute__((ext_vector_type(4)));
typedef int i32x4 __attribute__((ext_vector_type(4)));
typedef GAS u64 gu64;

constexpr int S_ = 2048, D_ = 640, F_ = 1536;
constexpr unsigned WS_EPOCH = 0;
constexpr unsigned FS = 128;
constexpr unsigned WS_DF = 4096;
constexpr unsigned WS_XF = WS_DF + 192 * FS;
constexpr unsigned WS_WF = WS_XF + 256 * FS;
constexpr unsigned WS_STAMP = 512 * 1024;
constexpr unsigned WS_WQ = 1u << 20;
constexpr unsigned WS_XR = WS_WQ + D_ * F_ * 2;
constexpr unsigned WS_W2 = WS_XR + S_ * D_ * 2;
constexpr unsigned WS_END = WS_W2 + D_ * F_ * 2;
constexpr int LDS_BYTES = 160 * 1024;

#define XSPREAD 1
#define ROTX 1
#define ROTC 0
#define ROTE 1
#define VEARLY 2
#define SEAM 0
#define POLLFIRST 1
#define PSLEEP 8
#define XPRE 5
#define YAUX 0
#define CODEKB 13

struct Params {
  const char* x; const char* qw; const char* sc; const char* zr; const char* U; const char* V; char* y; char* ws;
};

#define RLX_AGENT __ATOMIC_RELAXED, __HIP_MEMORY_SCOPE_AGENT
#define WAIT_VM(n) asm volatile("s_waitcnt vmcnt(%0)" :: "n"(n) : "memory")
#define WAIT_LGKM0() asm volatile("s_waitcnt lgkmcnt(0)" ::: "memory")
#define BAR() do { asm volatile("" ::: "memory"); __builtin_amdgcn_s_barrier(); asm volatile("" ::: "memory"); } while (0)
#define FENCE() asm volatile("" ::: "memory")

#define STAMP(i) do {} while (0)
#define STAMPC(i) do {} while (0)

#define TS(v)
#define TS2(v)
template <int I> struct IC { static constexpr int value = I; };
template <int B, int E, class Fn>
__device__ __forceinline__ void static_for(Fn&& fn) {
  if constexpr (B < E) {
    if constexpr (E - B == 1) fn(IC<B>{});
    else { static_for<B, (B + E) / 2>(fn); static_for<(B + E) / 2, E>(fn); }
  }
}

template <int OFF, bool SC1>
__device__ __forceinline__ void glds(const char* sbase, unsigned voff, unsigned lds) {
  if constexpr (SC1)
    asm volatile("s_mov_b32 m0, %2\n\ts_nop 0\n\tglobal_load_lds_dwordx4 %0, %1 offset:%3 sc1" :: "v"(voff), "s"(sbase), "s"(lds), "n"(OFF) : "memory");
  else
    asm volatile("s_mov_b32 m0, %2\n\ts_nop 0\n\tglobal_load_lds_dwordx4 %0, %1 offset:%3" :: "v"(voff), "s"(sbase), "s"(lds), "n"(OFF) : "memory");
}
__device__ __forceinline__ void glds4(const char* sbase, unsigned v0, unsigned v1, unsigned v2, unsigned v3, unsigned lds) {
  asm volatile("s_mov_b32 m0, %5\n\ts_nop 0\n\tglobal_load_lds_dwordx4 %0, %4\n\tglobal_load_lds_dwordx4 %1, %4 offset:1024\n\t"
               "global_load_lds_dwordx4 %2, %4 offset:2048\n\tglobal_load_lds_dwordx4 %3, %4 offset:3072"
               :: "v"(v0), "v"(v1), "v"(v2), "v"(v3), "s"(sbase), "s"(lds) : "memory");
}
__device__ __forceinline__ void glds4_lin_sc1(const char* sbase, unsigned v, unsigned lds) {
  asm volatile("s_mov_b32 m0, %2\n\ts_nop 0\n\tglobal_load_lds_dwordx4 %0, %1 sc1\n\tglobal_load_lds_dwordx4 %0, %1 offset:1024 sc1\n\t"
               "global_load_lds_dwordx4 %0, %1 offset:2048 sc1\n\tglobal_load_lds_dwordx4 %0, %1 offset:3072 sc1"
               :: "v"(v), "s"(sbase), "s"(lds) : "memory");
}
template <int OFF>
__device__ __forceinline__ void gload_sc1(u32x4& d, const char* sbase, unsigned voff) {
  asm volatile("global_load_dwordx4 %0, %1, %2 offset:%3 sc1" : "=v"(d) : "v"(voff), "s"(sbase), "n"(OFF) : "memory");
}
template <int N>
__device__ __forceinline__ void wait_for(u32x4& d) { asm volatile("s_waitcnt vmcnt(%1)" : "+v"(d) : "n"(N) : "memory"); }
__device__ __forceinline__ void opaque(unsigned& v) { asm volatile("" : "+v"(v)); }
__device__ __forceinline__ void glds_v(const char* vaddr, unsigned lds) {
  asm volatile("s_mov_b32 m0, %1\n\ts_nop 0\n\tglobal_load_lds_dwordx4 %0, off" :: "v"(vaddr), "s"(lds) : "memory");
}

__device__ __forceinline__ unsigned pack_bf16(float a, float b) {
  f32x2 v = {a, b};
  return __builtin_bit_cast(unsigned, __builtin_convertvector(v, bf16x2));
}
__device__ __forceinline__ float bf_lo(unsigned v) { return __builtin_bit_cast(float, v << 16); }
__device__ __forceinline__ float bf_hi(unsigned v) { return __builtin_bit_cast(float, v & 0xffff0000u); }

__device__ __forceinline__ u32x4 lds_rd128(unsigned a) { return *(LAS u32x4*)(size_t)a; }
__device__ __forceinline__ void lds_wr128(unsigned a, u32x4 v) { *(LAS u32x4*)(size_t)a = v; }
__device__ __forceinline__ bf16x8 lds_tr2(unsigned a0, unsigned a1) {
  s16x4 lo = __builtin_amdgcn_ds_read_tr16_b64_v4i16((LAS s16x4*)(size_t)a0);
  s16x4 hi = __builtin_amdgcn_ds_read_tr16_b64_v4i16((LAS s16x4*)(size_t)a1);
  return __builtin_bit_cast(bf16x8, __builtin_shufflevector(lo, hi, 0, 1, 2, 3, 4, 5, 6, 7));
}

__device__ __forceinline__ u64 mix64(u64 z) {
  z += 0x9E3779B97F4A7C15ull;
  z = (z ^ (z >> 30)) * 0xBF58476D1CE4E5B9ull;
  z = (z ^ (z >> 27)) * 0x94D049BB133111EBull;
  return z ^ (z >> 31);
}

__device__ __forceinline__ u64 load_tag(const Params& p) {
  const char* e = p.ws + WS_EPOCH;
  asm volatile("" : "+s"(e));
  return mix64(*(const __attribute__((address_space(4))) u64*)e);
}

__device__ __forceinline__ void wait_flags(const char* ws, unsigned off, u64 tag) {
  gu64* f = (gu64*)(ws + off);
#pragma unroll 1
  for (unsigned spins = 0; spins < (1u << 20); ++spins) {
    u64 v = __hip_atomic_load(f, RLX_AGENT);
    if (__all(v == tag)) break;
    __builtin_amdgcn_s_sleep(PSLEEP);
  }
  FENCE();
}
__device__ __forceinline__ void wait_flags2(__amdgpu_buffer_rsrc_t wsr, unsigned off, u64 tag) {
  const unsigned lo = (unsigned)tag, hi = (unsigned)(tag >> 32);
#pragma unroll 1
  for (unsigned spins = 0; spins < (1u << 20); ++spins) {
    FENCE();
    const u32x4 v = __builtin_amdgcn_raw_buffer_load_b128(wsr, off, 0, 16);
    if (__all(v[0] == lo && v[1] == hi && v[2] == lo && v[3] == hi)) break;
    __builtin_amdgcn_s_sleep(PSLEEP);
  }
  FENCE();
}
__device__ __forceinline__ void publish(char* ws, unsigned off, u64 tag, int lane) {
  WAIT_VM(0);
  if (lane == 0) __hip_atomic_store((gu64*)(ws + off), tag, RLX_AGENT);
  FENCE();
}

__device__ __forceinline__ unsigned dequant2(int q0, int q1, float z0, float z1, float s0, float s1) {
  unsigned qb = pack_bf16((float)q0, (float)q1);
  unsigned t = pack_bf16(bf_lo(qb) - z0, bf_hi(qb) - z1);
  return pack_bf16(bf_lo(t) * s0, bf_hi(t) * s1);
}

constexpr unsigned XSTG = 36 * 1024, XUOFF = 16 * 1024;

struct XLoader {
  unsigned xo[4], uo[5]; const char* xb; const char* ub; int rot, lw;
  __device__ __forceinline__ void init(const Params& p, int st, int etl, int lw_, int lane, int rot_) {
    lw = lw_;
    rot = rot_;
#pragma unroll
    for (int i = 0; i < 4; ++i) {
      const int row = 8 * i + (lane >> 3);
      xo[i] = row * 1280 + (((lane & 7) ^ ((row >> 1) & 7)) << 4) - i * 1024;
    }
#pragma unroll
    for (int i = 0; i < 5; ++i) {
      const int u = i * 64 + lane, row = (u * 3277) >> 16;
      uo[i] = row * 960 + u * 16 - (i & 3) * 1024;
    }
    xb = p.x + (size_t)(st * 128 + lw * 32) * 1280;
    ub = p.U + etl * 320 + lw * (16 * 1280);
  }
  __device__ __forceinline__ void issue(int i) {
    int kt = i + rot; if (kt >= 10) kt -= 10;
    const unsigned slot = (i & 3) * XSTG;
    const char* us = ub + (size_t)kt * (64 * 1280);
    glds4(us, uo[0], uo[1], uo[2], uo[3], slot + XUOFF + lw * 5120);
    glds<0, false>(us, uo[4], slot + XUOFF + lw * 5120 + 4096);
    glds4(xb + kt * 128, xo[0], xo[1], xo[2], xo[3], slot + lw * 4096);
  }
  __device__ __forceinline__ void run() {
    WAIT_VM(9); BAR();
    issue(2); issue(3);
    WAIT_VM(18); BAR();
    WAIT_VM(9); BAR();
#pragma unroll 1
    for (int i = 3; i < 9; ++i) {
      issue(i + 1); WAIT_VM(9);
      BAR();
    }
    WAIT_VM(0); BAR();
    BAR();
    BAR();
  }
};

__device__ __forceinline__ void x_compute(const Params& p, __amdgpu_buffer_rsrc_t wsr, int wave, int lane, int st, int etl, u64 tag) {
  const int r = lane & 31, h = lane >> 5;
  unsigned xa0[4];
#pragma unroll
  for (int kk = 0; kk < 4; ++kk) xa0[kk] = wave * 4096 + r * 128 + (((2 * kk + h) ^ ((r >> 1) & 7)) << 4);
  const unsigned ua0 = XUOFF + (8 * h + ((lane & 15) >> 2)) * 320 + ((lane >> 4) & 1) * 32 + (lane & 3) * 8;
  f32x16 acc[5];
#pragma unroll
  for (int t = 0; t < 5; ++t)
#pragma unroll
    for (int j = 0; j < 16; ++j) acc[t][j] = 0.f;
  constexpr int L = 4;
  bf16x8 au[5], bx[2];
#pragma unroll
  for (int t = 0; t < 5; ++t) asm volatile("" : "+v"(acc[t]));
  BAR();
  STAMP(1);
  bx[0] = __builtin_bit_cast(bf16x8, lds_rd128(xa0[0]));
#pragma unroll
  for (int n = 0; n < L; ++n) { __builtin_amdgcn_sched_barrier(0); au[n] = lds_tr2(ua0 + n * 64, ua0 + n * 64 + 4 * 320); }
  __builtin_amdgcn_sched_barrier(0);
#pragma unroll 1
  for (int i = 0; i < 10; ++i) {
    const unsigned cur = (i & 3) * XSTG, nxt = ((i + 1) & 3) * XSTG;
    const unsigned ua = ua0 + cur, un = ua0 + nxt, xn = xa0[0] + nxt;
    const unsigned xa1 = xa0[1] + cur, xa2 = xa0[2] + cur, xa3 = xa0[3] + cur;
    static_for<0, 20>([&](auto jc) {
      constexpr int j = decltype(jc)::value, n = j + L;
      if constexpr (n == 20) BAR();
      constexpr int kk = (n % 20) / 5, t = n % 5;
      if constexpr (t == 0) bx[(n / 5) & 1] = __builtin_bit_cast(bf16x8, lds_rd128(n == 20 ? xn : kk == 1 ? xa1 : kk == 2 ? xa2 : xa3));
      const unsigned ad = (n < 20 ? ua : un) + kk * 16 * 320 + t * 64;
      au[n % 5] = lds_tr2(ad, ad + 4 * 320);
      acc[j % 5] = __builtin_amdgcn_mfma_f32_32x32x16_bf16(au[j % 5], bx[(j / 5) & 1], acc[j % 5], 0, 0, 0);
      __builtin_amdgcn_sched_barrier(0);
    });
  }
  STAMP(6);
  const unsigned ob = WS_XR + ((st * 4 + wave) * 40 + etl * 10) * 1024;
#pragma unroll
  for (int t = 0; t < 5; ++t)
#pragma unroll
    for (int s2 = 0; s2 < 2; ++s2) {
      u32x4 v;
#pragma unroll
      for (int j = 0; j < 4; ++j) v[j] = pack_bf16(acc[t][8 * s2 + 2 * j], acc[t][8 * s2 + 2 * j + 1]);
      __builtin_amdgcn_raw_buffer_store_b128(v, wsr, lane * 16, ob + (2 * t + s2) * 1024, 16);
    }
  WAIT_LGKM0();
  BAR();
  STAMP(7);
  publish(p.ws, WS_XF + ((st * 4 + wave) * 4 + etl) * FS, tag, lane);
}

__device__ __forceinline__ void d_issue(const Params& p, int wave, int lane, int det, int dkp) {
  const unsigned dl = (wave * 10 + 8) * 4096;
  const int k2 = wave & 1, nfr = wave < 2 ? 3 : 2;
  unsigned qo[2];
#pragma unroll
  for (int i = 0; i < 2; ++i) {
    const int row = 8 * i + (lane >> 3);
    qo[i] = row * 6144 + (((lane & 7) ^ ((row >> 1) & 7)) << 4) - i * 1024;
  }
  const char* qb = p.qw + (size_t)(80 * det + 16 * (wave >> 1)) * 6144 + (64 * dkp + 32 * k2) * 4;
  glds4(qb, qo[0], qo[1], qo[0] + (32 * 6144 - 2048), qo[1] + (32 * 6144 - 2048), dl);
  if (wave < 2) {
    glds<0, false>(qb + 64 * 6144, qo[0], dl + 4096);
    glds<1024, false>(qb + 64 * 6144, qo[1], dl + 4096);
  }
  int i = (lane >> 3) & 3; if (i >= nfr) i = 0;
  const int grp = (5 * det + (wave >> 1) + 2 * i) >> 1;
  const char* sa = (((lane >> 2) & 1) ? p.zr : p.sc) + (size_t)grp * 3072 + (64 * dkp + 32 * k2) * 2 + (lane & 3) * 16;
  glds_v(sa, dl + 6144);
}

__device__ __forceinline__ void d_compute(const Params& p, __amdgpu_buffer_rsrc_t wsr, int wave, int lane, int det, int dkp) {
  const unsigned dl = (wave * 10 + 8) * 4096;
  const int r = lane & 15, q = lane >> 4;
  const unsigned a0 = dl + r * 128 + (((2 * q) ^ ((r >> 1) & 7)) << 4), a1 = dl + r * 128 + (((2 * q + 1) ^ ((r >> 1) & 7)) << 4);
  const unsigned as = dl + 6144 + q * 16;
  const unsigned ob = WS_WQ + ((det * 48 + 2 * dkp + (wave & 1)) * 5 + (wave >> 1)) * 1024;
  const int nfr = wave < 2 ? 3 : 2;
  i32x4 q0 = __builtin_bit_cast(i32x4, lds_rd128(a0)), q1 = __builtin_bit_cast(i32x4, lds_rd128(a1));
  u32x4 s = lds_rd128(as), z = lds_rd128(as + 64);
#pragma unroll 1
  for (int i = 0; i < nfr; ++i) {
    const i32x4 nq0 = __builtin_bit_cast(i32x4, lds_rd128(a0 + i * 2048 + 2048)), nq1 = __builtin_bit_cast(i32x4, lds_rd128(a1 + i * 2048 + 2048));
    const u32x4 ns = lds_rd128(as + i * 128 + 128), nz = lds_rd128(as + i * 128 + 192);
    u32x4 o;
    o[0] = dequant2(q0[0], q0[1], bf_lo(z[0]), bf_hi(z[0]), bf_lo(s[0]), bf_hi(s[0]));
    o[1] = dequant2(q0[2], q0[3], bf_lo(z[1]), bf_hi(z[1]), bf_lo(s[1]), bf_hi(s[1]));
    o[2] = dequant2(q1[0], q1[1], bf_lo(z[2]), bf_hi(z[2]), bf_lo(s[2]), bf_hi(s[2]));
    o[3] = dequant2(q1[2], q1[3], bf_lo(z[3]), bf_hi(z[3]), bf_lo(s[3]), bf_hi(s[3]));
    __builtin_amdgcn_raw_buffer_store_b128(o, wsr, lane * 16, ob + i * 2048, 16);
    q0 = nq0; q1 = nq1; s = ns; z = nz;
  }
}

struct VLoader {
  unsigned vo[4]; const char* vb; int rot, kq;
  __device__ __forceinline__ void init(const Params& p, int gt, int kq_, int rot_, int lane) {
    kq = kq_; rot = rot_;
#pragma unroll
    for (int pc = 0; pc < 4; ++pc) {
      const int row = 8 * pc + (lane >> 3), swz = ((row >> 1) & 1) | (((row >> 3) & 1) << 1);
      vo[pc] = row * 3072 + ((((lane & 7) >> 1) ^ swz) << 5) + ((lane & 1) << 4) - pc * 1024;
    }
    vb = p.V + gt * 128 + (size_t)kq * (12 * 32 * 3072);
  }
  __device__ __forceinline__ int kstep(int i) const { const int t = i + rot; return t >= 12 ? t - 12 : t; }
  __device__ __forceinline__ void issue_at(int i, unsigned lds) const {
    glds4(vb + (size_t)kstep(i) * (32 * 3072), vo[0], vo[1], vo[2], vo[3], lds);
  }
  __device__ __forceinline__ void issue(int i) const { issue_at(i, (kq * 10 + i) * 4096); }
};

template <int NB>
__device__ __forceinline__ void w_body(const Params& p, __amdgpu_buffer_rsrc_t wsr, const VLoader& vl, int wave, int lane, int et, int gt, u64 tag) {
  constexpr int M0 = NB == 3 ? 0 : 3, SEC = NB == 3 ? 0 : 1;
  const int kq = wave & 3;
  unsigned va0[4];
  {
    const int g = lane >> 4, qq = (lane & 15) >> 2, sw = ((qq >> 1) & 1) | ((g & 1) << 1);
#pragma unroll
    for (int c = 0; c < 4; ++c) va0[c] = kq * 40960 + 128 * (8 * g + qq) + 32 * (c ^ sw) + 8 * (lane & 3);
  }
  const unsigned ab = WS_WQ + ((et * 48 + kq * 12) * 5 + M0) * 1024;
  u32x4 af[2][NB];
  f32x4 acc[NB][4];
#pragma unroll
  for (int m = 0; m < NB; ++m)
#pragma unroll
    for (int c = 0; c < 4; ++c) acc[m][c] = (f32x4){0.f, 0.f, 0.f, 0.f};
  s16x4 bh[2][8];
  const char* wq = p.ws;
  const unsigned vl16 = lane * 16;
#pragma unroll
  for (int m = 0; m < NB; ++m)
#pragma unroll
    for (int c = 0; c < 4; ++c) asm volatile("" : "+v"(acc[m][c]));
  BAR();
  STAMP(5);
  static_for<0, 2 * NB>([&](auto ic) {
    constexpr int n = decltype(ic)::value / NB, m = decltype(ic)::value % NB;
    gload_sc1<m * 1024>(af[n][m], wq + (ab + vl.kstep(n) * 5120), vl16);
  });
#pragma unroll
  for (int j = 0; j < 8; ++j) bh[0][j] = __builtin_amdgcn_ds_read_tr16_b64_v4i16((LAS s16x4*)(size_t)(va0[j >> 1] + (j & 1) * 512));
  __builtin_amdgcn_sched_barrier(0);
  unsigned vc[4] = {va0[0], va0[1], va0[2], va0[3]};
  const unsigned vlast = (kq & 1) * 40960 + (kq >> 1) * 8192;
  auto iter = [&](int it, auto modec) {
    constexpr int MODE = decltype(modec)::value;
    const unsigned step = it == 4 ? vlast - kq * 40960 - 32768 : 8192;
    unsigned vn[4];
#pragma unroll
    for (int c = 0; c < 4; ++c) { vn[c] = vc[c] + step; opaque(vn[c]); }
    if constexpr (MODE == 1) { BAR(); vl.issue_at(10 + SEC, vlast + SEC * 4096); }
    static_for<0, 2>([&](auto hc) {
      constexpr int hf = decltype(hc)::value;
      if constexpr (hf == 1 && MODE == 0) { if (it == 4) BAR(); }
      const unsigned so = ab + vl.kstep(MODE == 2 ? 0 : 2 * it + 2 + hf) * 5120;
      static_for<0, NB * 4>([&](auto jc) {
        constexpr int j = decltype(jc)::value, m = j / 4, c = j % 4;
        if constexpr (c == 0) {
          constexpr int N = MODE == 1 ? 2 * NB + 3 : MODE == 0 ? 2 * NB - 1 : 2 * NB - 1 - (hf * NB + m);
          wait_for<N>(af[hf][m]);
        }
        const bf16x8 bv = __builtin_bit_cast(bf16x8, __builtin_shufflevector(bh[hf][2 * c], bh[hf][2 * c + 1], 0, 1, 2, 3, 4, 5, 6, 7));
        acc[m][c] = __builtin_amdgcn_mfma_f32_16x16x32_bf16(__builtin_bit_cast(bf16x8, af[hf][m]), bv, acc[m][c], 0, 0, 0);
        __builtin_amdgcn_sched_barrier(0);
        if constexpr (j < 8 && !(MODE == 2 && hf == 1))
          bh[hf ^ 1][j] = __builtin_amdgcn_ds_read_tr16_b64_v4i16((LAS s16x4*)(size_t)(hf == 0 ? vc[j >> 1] + 4096 + (j & 1) * 512 : vn[j >> 1] + (j & 1) * 512));
        if constexpr (MODE != 2 && c == 3) gload_sc1<m * 1024>(af[hf][m], wq + so, vl16);
        __builtin_amdgcn_sched_barrier(0);
      });
    });
#pragma unroll
    for (int c = 0; c < 4; ++c) vc[c] = vn[c];
  };
#pragma unroll 1
  for (int it = 0; it < 2; ++it) iter(it, IC<0>{});
  iter(2, IC<1>{});
#pragma unroll 1
  for (int it = 3; it < 5; ++it) iter(it, IC<0>{});
  iter(5, IC<2>{});
  STAMP(6);
  const unsigned red = 81920 + kq * 20480 + lane * 16;
#pragma unroll
  for (int m = 0; m < NB; ++m)
#pragma unroll
    for (int c = 0; c < 4; ++c) lds_wr128(red + ((M0 + m) * 4 + c) * 1024, __builtin_bit_cast(u32x4, acc[m][c]));
  WAIT_LGKM0();
  BAR();
  if constexpr (NB == 3) {
    STAMP(10);
    const unsigned ol = ((lane & 15) + 16 * (lane >> 5) + 32 * ((lane >> 4) & 1)) * 16;
    f32x4 t[3][2][4];
#pragma unroll
    for (int j = 0; j < 3; ++j) {
      const int fi = kq + (j == 2 && kq >= 2 ? 4 : 4 * j);
      const unsigned rb = 81920 + lane * 16 + fi * 2048;
#pragma unroll
      for (int hf = 0; hf < 2; ++hf)
#pragma unroll
        for (int q = 0; q < 4; ++q) t[j][hf][q] = __builtin_bit_cast(f32x4, lds_rd128(rb + q * 20480 + hf * 1024));
    }
#pragma unroll
    for (int j = 0; j < 3; ++j) {
      const int fi = kq + (j == 2 && kq >= 2 ? 4 : 4 * j);
      const f32x4 a = (t[j][0][0] + t[j][0][1]) + (t[j][0][2] + t[j][0][3]), b = (t[j][1][0] + t[j][1][1]) + (t[j][1][2] + t[j][1][3]);
      auto r0 = __builtin_amdgcn_permlane32_swap(pack_bf16(a[0], a[1]), pack_bf16(b[0], b[1]), false, false);
      auto r1 = __builtin_amdgcn_permlane32_swap(pack_bf16(a[2], a[3]), pack_bf16(b[2], b[3]), false, false);
      const u32x4 v = {r0[0], r1[0], r0[1], r1[1]};
      __builtin_amdgcn_raw_buffer_store_b128(v, wsr, ol, WS_W2 + ((2 * gt + (fi & 1)) * 40 + 5 * et + (fi >> 1)) * 1024, 16);
    }
    if (SEAM == 1) BAR();
    STAMP(7);
    publish(p.ws, WS_WF + ((gt * 8 + et) * 4 + kq) * FS, tag, lane);
    if (SEAM == 2) BAR();
  }
}

constexpr unsigned YW = 80 * 1024;

__device__ __forceinline__ void y_load(const Params& p, int wave, int lane, int st, int gt, u64 tag) {
  const int lw = wave - 4;
  STAMP(8);
  if (lw == 0) wait_flags(p.ws, WS_WF + ((3 * gt / 2) * 32 + lane) * FS, tag);
  if (blockIdx.x == 0) {
    const int w = lw * 64 + lane;
    wait_flags(p.ws, w < 64 ? WS_XF + ((w >> 2) * 16 + (w & 3)) * FS : WS_WF + (w - 64) * 4 * FS, tag);
  }
  BAR();
  STAMP(11);
  const char* wb = p.ws + WS_W2 + (size_t)(3 * gt * 40 + lw) * 1024;
  const char* xb = p.ws + WS_XR + (size_t)(st * 4 + lw) * 40960;
  const unsigned vo = lane * 16;
  auto issue_w = [&](int c, unsigned slot) {
#pragma unroll
    for (int t = 0; t < 3; ++t) glds<0, true>(wb + c * 4096 + t * 40960, vo, YW + slot * 12288 + lw * 1024 + t * 4096);
  };
  auto issue_x = [&](int c, unsigned slot) { glds4_lin_sc1(xb + c * 4096, vo, slot * 16384 + lw * 4096); };
  constexpr int n0 = 3 + (XPRE > 0 ? 0 : 4), n1 = 3 + (XPRE > 1 ? 0 : 4), n2 = 3 + (XPRE > 2 ? 0 : 4), n3 = 3 + (XPRE > 3 ? 0 : 4), n4 = 3 + (XPRE > 4 ? 0 : 4);
  auto issue0 = [&](int c) { issue_w(c, c); if (c >= XPRE) issue_x(c, c); };
  issue0(0); issue0(1); issue0(2);
  WAIT_VM(n1 + n2); BAR();
  if (blockIdx.x == 0 && threadIdx.x == 256) __hip_atomic_store((gu64*)(p.ws + WS_EPOCH), tag, RLX_AGENT);
  issue0(3); issue0(4);
  WAIT_VM(n2 + n3 + n4); BAR();
  WAIT_VM(n3 + n4); BAR();
  issue_w(5, 0); issue_x(5, 0);
  WAIT_VM(n4 + 7); BAR();
#pragma unroll 1
  for (int c = 6; c < 10; ++c) {
    issue_w(c, c - 5); issue_x(c, c - 5);
    WAIT_VM(14); BAR();
  }
  WAIT_VM(7); BAR();
  WAIT_VM(0); BAR();
  BAR();
  (void)n0;
}

__device__ __forceinline__ void y_compute(const Params& p, int wave, int lane, int st, int gt, u64 tag, bool b4) {
  STAMP(8);
  if (SEAM > 0 && !POLLFIRST && b4) BAR();
  wait_flags(p.ws, WS_XF + ((st * 4 + wave) * 4 + (lane & 3)) * FS, tag);
  if (SEAM > 0 && POLLFIRST && b4) BAR();
  STAMP(9);
  {
    const char* xb = p.ws + WS_XR + (size_t)(st * 4 + wave) * 40960;
#pragma unroll
    for (int c = 0; c < XPRE; ++c) glds4_lin_sc1(xb + c * 4096, lane * 16, c * 16384 + wave * 4096);
  }
  STAMP(10);
  f32x16 acc[3];
#pragma unroll
  for (int t = 0; t < 3; ++t)
#pragma unroll
    for (int j = 0; j < 16; ++j) acc[t][j] = 0.f;
  constexpr int L = 5;
  bf16x8 aw[6], bx[4];
  const unsigned a0 = YW + lane * 16, x0 = wave * 4096 + lane * 16;
#pragma unroll
  for (int t = 0; t < 3; ++t) asm volatile("" : "+v"(acc[t]));
  BAR();
  WAIT_VM(0);
  BAR();
  STAMP(11);
#pragma unroll
  for (int n = 0; n < L; ++n) {
    if (n % 3 == 0) bx[n / 3] = __builtin_bit_cast(bf16x8, lds_rd128(x0 + (n / 3) * 1024));
    __builtin_amdgcn_sched_barrier(0);
    aw[n] = __builtin_bit_cast(bf16x8, lds_rd128(a0 + (n % 3) * 4096 + (n / 3) * 1024));
    __builtin_amdgcn_sched_barrier(0);
  }
  unsigned cur = 0;
#pragma unroll 1
  for (int c = 0; c < 10; ++c) {
    const unsigned nxt = cur == 4 ? 0 : cur + 1;
    const unsigned ac = a0 + cur * 12288, an = a0 + nxt * 12288, xc = x0 + cur * 16384, xn = x0 + nxt * 16384;
    static_for<0, 12>([&](auto jc) {
      constexpr int j = decltype(jc)::value, n = j + L;
      if constexpr (n == 12) BAR();
      constexpr int kk = (n % 12) / 3, t = n % 3;
      if constexpr (t == 0) bx[kk] = __builtin_bit_cast(bf16x8, lds_rd128((n < 12 ? xc : xn) + kk * 1024));
      aw[n % 6] = __builtin_bit_cast(bf16x8, lds_rd128((n < 12 ? ac : an) + t * 4096 + kk * 1024));
      acc[j % 3] = __builtin_amdgcn_mfma_f32_32x32x16_bf16(aw[j % 6], bx[j / 3], acc[j % 3], 0, 0, 0);
      __builtin_amdgcn_sched_barrier(0);
    });
    cur = nxt;
  }
  STAMP(12);
  const __amdgpu_buffer_rsrc_t yr = __builtin_amdgcn_make_buffer_rsrc(p.y, 0, S_ * F_ * 2, 0x00020000);
  const unsigned yo = (st * 128 + wave * 32 + (lane & 31)) * 3072 + gt * 192 + (lane >> 5) * 16;
#pragma unroll
  for (int t = 0; t < 3; ++t)
#pragma unroll
    for (int i = 0; i < 4; i += 2) {
      auto r0 = __builtin_amdgcn_permlane32_swap(pack_bf16(acc[t][4 * i], acc[t][4 * i + 1]), pack_bf16(acc[t][4 * i + 4], acc[t][4 * i + 5]), false, false);
      auto r1 = __builtin_amdgcn_permlane32_swap(pack_bf16(acc[t][4 * i + 2], acc[t][4 * i + 3]), pack_bf16(acc[t][4 * i + 6], acc[t][4 * i + 7]), false, false);
      const u32x4 v = {r0[0], r1[0], r0[1], r1[1]};
      __builtin_amdgcn_raw_buffer_store_b128(v, yr, yo + t * 64 + i * 16, 0, YAUX);
    }
}

__global__ void __launch_bounds__(512, 2) quip_kernel(Params p) {
  const int wave = __builtin_amdgcn_readfirstlane(threadIdx.x >> 6), lane = threadIdx.x & 63;
  const int xcd = blockIdx.x & 7, idx = blockIdx.x >> 3;
  STAMP(0); STAMPC(14);
  unsigned pf = 0;
  {
    const unsigned ln = idx * 8 + wave;
    const GAS unsigned* code = (const GAS unsigned*)((__builtin_amdgcn_s_getpc() & ~255ull) + (ln < CODEKB * 8 ? ln : 0) * 128);
    pf = __hip_atomic_load(code, __ATOMIC_RELAXED, __HIP_MEMORY_SCOPE_WORKGROUP);
  }
  const bool isx = idx < 8;
  const int xst = 4 * (xcd >> 1) + (idx >> 1), etl = 2 * (xcd & 1) + (idx & 1);
  const int wk = idx - 8, wk6 = wk / 6;
  const int et = 4 * (xcd >> 2) + wk6, gt = 6 * (xcd & 3) + wk - 6 * wk6;
  const int dj = xcd * 24 + wk, det = xcd, dkp = wk;
  const int rotc = (ROTC * 2 * (wk - 6 * wk6) + ROTE * 3 * wk6) % 12;
  const int rotx = ROTX * ((idx >> 1) * 2 + (idx & 1) * 5) % 10;
  XLoader xl; VLoader vl;
  if (!isx && wave < 4) d_issue(p, wave, lane, det, dkp);
  if (isx && wave >= 4) { xl.init(p, xst, etl, wave - 4, lane, rotx); xl.issue(0); xl.issue(1); }
  if (!isx) {
    vl.init(p, gt, wave & 3, rotc, lane);
    if (wave >= 4) {
#pragma unroll
      for (int i = 0; i < VEARLY; ++i) vl.issue(i);
    }
  }
  const __amdgpu_buffer_rsrc_t wsr = __builtin_amdgcn_make_buffer_rsrc(p.ws, 0, WS_END, 0x00020000);
  const u64 tag = load_tag(p);
  asm volatile("" :: "s"(tag));
  const int yst = 4 * (xcd >> 1) + (idx >> 3), ygt = 8 * (xcd & 1) + (idx & 7);
  if (wave < 4) {
    if (isx) {
      x_compute(p, wsr, wave, lane, xst, etl, tag);
      asm volatile("" :: "v"(pf));
    } else {
      WAIT_VM(0);
      asm volatile("" :: "v"(pf));
      STAMP(1);
      d_compute(p, wsr, wave, lane, det, dkp);
      STAMP(2);
      WAIT_VM(0);
      BAR();
      if (wave == 0) {
        publish(p.ws, WS_DF + dj * FS, tag, lane);
        STAMP(3);
        wait_flags(p.ws, WS_DF + (et * 24 + (lane < 24 ? lane : 0)) * FS, tag);
      }
      STAMP(4);
      w_body<2>(p, wsr, vl, wave, lane, et, gt, tag);
    }
    y_compute(p, wave, lane, yst, ygt, tag, !isx);
  } else {
    if (isx) {
      xl.run();
      asm volatile("" :: "v"(pf));
      __builtin_amdgcn_s_setprio(3);
    } else {
      BAR();
      STAMP(3);
#pragma unroll 1
      for (int i = VEARLY; i < 10; ++i) vl.issue(i);
      WAIT_VM(0);
      asm volatile("" :: "v"(pf));
      STAMP(4);
      __builtin_amdgcn_s_setprio(3);
      w_body<3>(p, wsr, vl, wave, lane, et, gt, tag);
    }
    y_load(p, wave, lane, yst, ygt, tag);
  }
  STAMP(13); STAMPC(15);
}

extern "C" void kernel_launch(void* const* d_in, const int* in_sizes, int n_in, void* d_out, int out_size, void* d_ws,
                              size_t ws_size, hipStream_t stream) {
  Params p;
  p.x = (const char*)d_in[0]; p.qw = (const char*)d_in[1]; p.sc = (const char*)d_in[2]; p.zr = (const char*)d_in[3];
  p.U = (const char*)d_in[4]; p.V = (const char*)d_in[5]; p.y = (char*)d_out; p.ws = (char*)d_ws;
  (void)hipFuncSetAttribute((const void*)quip_kernel, hipFuncAttributeMaxDynamicSharedMemorySize, LDS_BYTES);
  quip_kernel<<<256, 512, LDS_BYTES, stream>>>(p);
}
```

```cpp
#define PROBE 99
#define PROLE 1
#include <hip/hip_runtime.h>
#include <stdint.h>

#define LAS __attribute__((address_space(3)))
#define GAS __attribute__((address_space(1)))
typedef unsigned short u16;
typedef unsigned long long u64;
typedef short s16x4 __attribute__((ext_vector_type(4)));
typedef short s16x8 __attribute__((ext_vector_type(8)));
typedef __bf16 bf16x8 __attribute__((ext_vector_type(8)));
typedef __bf16 bf16x2 __attribute__((ext_vector_type(2)));
typedef float f32x2 __attribute__((ext_vector_type(2)));
typedef float f32x4 __attribute__((ext_vector_type(4)));
typedef float f32x16 __attribute__((ext_vector_type(16)));
typedef unsigned u32x4 __attribute__((ext_vector_type(4)));
typedef int i32x4 __attribute__((ext_vector_type(4)));
typedef GAS u64 gu64;

constexpr int S_ = 2048, D_ = 640, F_ = 1536;
constexpr unsigned WS_EPOCH = 0;
constexpr unsigned FS = 128;
constexpr unsigned WS_DF = 4096;
constexpr unsigned WS_XF = WS_DF + 192 * FS;
constexpr unsigned WS_WF = WS_XF + 256 * FS;
constexpr unsigned WS_STAMP = 512 * 1024;
constexpr unsigned WS_WQ = 1u << 20;
constexpr unsigned WS_XR = WS_WQ + D_ * F_ * 2;
constexpr unsigned WS_W2 = WS_XR + S_ * D_ * 2;
constexpr unsigned WS_END = WS_W2 + D_ * F_ * 2;
constexpr int LDS_BYTES = 160 * 1024;

#define XSPREAD 1
#define ROTX 1
#define ROTC 0
#define ROTE 1
#define VEARLY 2
#define SEAM 0
#define POLLFIRST 1
#define PSLEEP 8
#define XPRE 5
#define YAUX 0
#define CODEKB 13

struct Params {
  const char* x; const char* qw; const char* sc; const char* zr; const char* U; const char* V; char* y; char* ws;
  mutable unsigned long long t0, d;
};

#define RLX_AGENT __ATOMIC_RELAXED, __HIP_MEMORY_SCOPE_AGENT
#define WAIT_VM(n) asm volatile("s_waitcnt vmcnt(%0)" :: "n"(n) : "memory")
#define WAIT_LGKM0() asm volatile("s_waitcnt lgkmcnt(0)" ::: "memory")
#define BAR() do { asm volatile("" ::: "memory"); __builtin_amdgcn_s_barrier(); asm volatile("" ::: "memory"); } while (0)
#define FENCE() asm volatile("" ::: "memory")

#define STAMP(i) do { if ((i) == 0) p.t0 = __builtin_amdgcn_s_memrealtime(); if ((i) == PROBE && (PROLE)) p.d = __builtin_amdgcn_s_memrealtime() - p.t0; } while (0)
#define STAMPC(i) do {} while (0)

#define TS(v)
#define TS2(v)
template <int I> struct IC { static constexpr int value = I; };
template <int B, int E, class Fn>
__device__ __forceinline__ void static_for(Fn&& fn) {
  if constexpr (B < E) {
    if constexpr (E - B == 1) fn(IC<B>{});
    else { static_for<B, (B + E) / 2>(fn); static_for<(B + E) / 2, E>(fn); }
  }
}

template <int OFF, bool SC1>
__device__ __forceinline__ void glds(const char* sbase, unsigned voff, unsigned lds) {
  if constexpr (SC1)
    asm volatile("s_mov_b32 m0, %2\n\ts_nop 0\n\tglobal_load_lds_dwordx4 %0, %1 offset:%3 sc1" :: "v"(voff), "s"(sbase), "s"(lds), "n"(OFF) : "memory");
  else
    asm volatile("s_mov_b32 m0, %2\n\ts_nop 0\n\tglobal_load_lds_dwordx4 %0, %1 offset:%3" :: "v"(voff), "s"(sbase), "s"(lds), "n"(OFF) : "memory");
}
__device__ __forceinline__ void glds4(const char* sbase, unsigned v0, unsigned v1, unsigned v2, unsigned v3, unsigned lds) {
  asm volatile("s_mov_b32 m0, %5\n\ts_nop 0\n\tglobal_load_lds_dwordx4 %0, %4\n\tglobal_load_lds_dwordx4 %1, %4 offset:1024\n\t"
               "global_load_lds_dwordx4 %2, %4 offset:2048\n\tglobal_load_lds_dwordx4 %3, %4 offset:3072"
               :: "v"(v0), "v"(v1), "v"(v2), "v"(v3), "s"(sbase), "s"(lds) : "memory");
}
__device__ __forceinline__ void glds4_lin_sc1(const char* sbase, unsigned v, unsigned lds) {
  asm volatile("s_mov_b32 m0, %2\n\ts_nop 0\n\tglobal_load_lds_dwordx4 %0, %1 sc1\n\tglobal_load_lds_dwordx4 %0, %1 offset:1024 sc1\n\t"
               "global_load_lds_dwordx4 %0, %1 offset:2048 sc1\n\tglobal_load_lds_dwordx4 %0, %1 offset:3072 sc1"
               :: "v"(v), "s"(sbase), "s"(lds) : "memory");
}
template <int OFF>
__device__ __forceinline__ void gload_sc1(u32x4& d, const char* sbase, unsigned voff) {
  asm volatile("global_load_dwordx4 %0, %1, %2 offset:%3 sc1" : "=v"(d) : "v"(voff), "s"(sbase), "n"(OFF) : "memory");
}
template <int N>
__device__ __forceinline__ void wait_for(u32x4& d) { asm volatile("s_waitcnt vmcnt(%1)" : "+v"(d) : "n"(N) : "memory"); }
__device__ __forceinline__ void opaque(unsigned& v) { asm volatile("" : "+v"(v)); }
__device__ __forceinline__ void glds_v(const char* vaddr, unsigned lds) {
  asm volatile("s_mov_b32 m0, %1\n\ts_nop 0\n\tglobal_load_lds_dwordx4 %0, off" :: "v"(vaddr), "s"(lds) : "memory");
}

__device__ __forceinline__ unsigned pack_bf16(float a, float b) {
  f32x2 v = {a, b};
  return __builtin_bit_cast(unsigned, __builtin_convertvector(v, bf16x2));
}
__device__ __forceinline__ float bf_lo(unsigned v) { return __builtin_bit_cast(float, v << 16); }
__device__ __forceinline__ float bf_hi(unsigned v) { return __builtin_bit_cast(float, v & 0xffff0000u); }

__device__ __forceinline__ u32x4 lds_rd128(unsigned a) { return *(LAS u32x4*)(size_t)a; }
__device__ __forceinline__ void lds_wr128(unsigned a, u32x4 v) { *(LAS u32x4*)(size_t)a = v; }
__device__ __forceinline__ bf16x8 lds_tr2(unsigned a0, unsigned a1) {
  s16x4 lo = __builtin_amdgcn_ds_read_tr16_b64_v4i16((LAS s16x4*)(size_t)a0);
  s16x4 hi = __builtin_amdgcn_ds_read_tr16_b64_v4i16((LAS s16x4*)(size_t)a1);
  return __builtin_bit_cast(bf16x8, __builtin_shufflevector(lo, hi, 0, 1, 2, 3, 4, 5, 6, 7));
}

__device__ __forceinline__ u64 mix64(u64 z) {
  z += 0x9E3779B97F4A7C15ull;
  z = (z ^ (z >> 30)) * 0xBF58476D1CE4E5B9ull;
  z = (z ^ (z >> 27)) * 0x94D049BB133111EBull;
  return z ^ (z >> 31);
}

__device__ __forceinline__ u64 load_tag(const Params& p) {
  const char* e = p.ws + WS_EPOCH;
  asm volatile("" : "+s"(e));
  return mix64(*(const __attribute__((address_space(4))) u64*)e);
}

__device__ __forceinline__ void wait_flags(const char* ws, unsigned off, u64 tag) {
  gu64* f = (gu64*)(ws + off);
#pragma unroll 1
  for (unsigned spins = 0; spins < (1u << 20); ++spins) {
    u64 v = __hip_atomic_load(f, RLX_AGENT);
    if (__all(v == tag)) break;
    __builtin_amdgcn_s_sleep(PSLEEP);
  }
  FENCE();
}
__device__ __forceinline__ void wait_flags2(__amdgpu_buffer_rsrc_t wsr, unsigned off, u64 tag) {
  const unsigned lo = (unsigned)tag, hi = (unsigned)(tag >> 32);
#pragma unroll 1
  for (unsigned spins = 0; spins < (1u << 20); ++spins) {
    FENCE();
    const u32x4 v = __builtin_amdgcn_raw_buffer_load_b128(wsr, off, 0, 16);
    if (__all(v[0] == lo && v[1] == hi && v[2] == lo && v[3] == hi)) break;
    __builtin_amdgcn_s_sleep(PSLEEP);
  }
  FENCE();
}
__device__ __forceinline__ void publish(char* ws, unsigned off, u64 tag, int lane) {
  WAIT_VM(0);
  if (lane == 0) __hip_atomic_store((gu64*)(ws + off), tag, RLX_AGENT);
  FENCE();
}

__device__ __forceinline__ unsigned dequant2(int q0, int q1, float z0, float z1, float s0, float s1) {
  unsigned qb = pack_bf16((float)q0, (float)q1);
  unsigned t = pack_bf16(bf_lo(qb) - z0, bf_hi(qb) - z1);
  return pack_bf16(bf_lo(t) * s0, bf_hi(t) * s1);
}

constexpr unsigned XSTG = 36 * 1024, XUOFF = 16 * 1024;

struct XLoader {
  unsigned xo[4], uo[5]; const char* xb; const char* ub; int rot, lw;
  __device__ __forceinline__ void init(const Params& p, int st, int etl, int lw_, int lane, int rot_) {
    lw = lw_;
    rot = rot_;
#pragma unroll
    for (int i = 0; i < 4; ++i) {
      const int row = 8 * i + (lane >> 3);
      xo[i] = row * 1280 + (((lane & 7) ^ ((row >> 1) & 7)) << 4) - i * 1024;
    }
#pragma unroll
    for (int i = 0; i < 5; ++i) {
      const int u = i * 64 + lane, row = (u * 3277) >> 16;
      uo[i] = row * 960 + u * 16 - (i & 3) * 1024;
    }
    xb = p.x + (size_t)(st * 128 + lw * 32) * 1280;
    ub = p.U + etl * 320 + lw * (16 * 1280);
  }
  __device__ __forceinline__ void issue(int i) {
    int kt = i + rot; if (kt >= 10) kt -= 10;
    const unsigned slot = (i & 3) * XSTG;
    const char* us = ub + (size_t)kt * (64 * 1280);
    glds4(us, uo[0], uo[1], uo[2], uo[3], slot + XUOFF + lw * 5120);
    glds<0, false>(us, uo[4], slot + XUOFF + lw * 5120 + 4096);
    glds4(xb + kt * 128, xo[0], xo[1], xo[2], xo[3], slot + lw * 4096);
  }
  __device__ __forceinline__ void run() {
    WAIT_VM(9); BAR();
    issue(2); issue(3);
    WAIT_VM(18); BAR();
    WAIT_VM(9); BAR();
#pragma unroll 1
    for (int i = 3; i < 9; ++i) {
      issue(i + 1); WAIT_VM(9);
      BAR();
    }
    WAIT_VM(0); BAR();
    BAR();
    BAR();
  }
};

__device__ __forceinline__ void x_compute(const Params& p, __amdgpu_buffer_rsrc_t wsr, int wave, int lane, int st, int etl, u64 tag) {
  const int r = lane & 31, h = lane >> 5;
  unsigned xa0[4];
#pragma unroll
  for (int kk = 0; kk < 4; ++kk) xa0[kk] = wave * 4096 + r * 128 + (((2 * kk + h) ^ ((r >> 1) & 7)) << 4);
  const unsigned ua0 = XUOFF + (8 * h + ((lane & 15) >> 2)) * 320 + ((lane >> 4) & 1) * 32 + (lane & 3) * 8;
  f32x16 acc[5];
#pragma unroll
  for (int t = 0; t < 5; ++t)
#pragma unroll
    for (int j = 0; j < 16; ++j) acc[t][j] = 0.f;
  constexpr int L = 4;
  bf16x8 au[5], bx[2];
#pragma unroll
  for (int t = 0; t < 5; ++t) asm volatile("" : "+v"(acc[t]));
  BAR();
  STAMP(1);
  bx[0] = __builtin_bit_cast(bf16x8, lds_rd128(xa0[0]));
#pragma unroll
  for (int n = 0; n < L; ++n) { __builtin_amdgcn_sched_barrier(0); au[n] = lds_tr2(ua0 + n * 64, ua0 + n * 64 + 4 * 320); }
  __builtin_amdgcn_sched_barrier(0);
#pragma unroll 1
  for (int i = 0; i < 10; ++i) {
    const unsigned cur = (i & 3) * XSTG, nxt = ((i + 1) & 3) * XSTG;
    const unsigned ua = ua0 + cur, un = ua0 + nxt, xn = xa0[0] + nxt;
    const unsigned xa1 = xa0[1] + cur, xa2 = xa0[2] + cur, xa3 = xa0[3] + cur;
    static_for<0, 20>([&](auto jc) {
      constexpr int j = decltype(jc)::value, n = j + L;
      if constexpr (n == 20) BAR();
      constexpr int kk = (n % 20) / 5, t = n % 5;
      if constexpr (t == 0) bx[(n / 5) & 1] = __builtin_bit_cast(bf16x8, lds_rd128(n == 20 ? xn : kk == 1 ? xa1 : kk == 2 ? xa2 : xa3));
      const unsigned ad = (n < 20 ? ua : un) + kk * 16 * 320 + t * 64;
      au[n % 5] = lds_tr2(ad, ad + 4 * 320);
      acc[j % 5] = __builtin_amdgcn_mfma_f32_32x32x16_bf16(au[j % 5], bx[(j / 5) & 1], acc[j % 5], 0, 0, 0);
      __builtin_amdgcn_sched_barrier(0);
    });
  }
  STAMP(6);
  const unsigned ob = WS_XR + ((st * 4 + wave) * 40 + etl * 10) * 1024;
#pragma unroll
  for (int t = 0; t < 5; ++t)
#pragma unroll
    for (int s2 = 0; s2 < 2; ++s2) {
      u32x4 v;
#pragma unroll
      for (int j = 0; j < 4; ++j) v[j] = pack_bf16(acc[t][8 * s2 + 2 * j], acc[t][8 * s2 + 2 * j + 1]);
      __builtin_amdgcn_raw_buffer_store_b128(v, wsr, lane * 16, ob + (2 * t + s2) * 1024, 16);
    }
  WAIT_LGKM0();
  BAR();
  STAMP(7);
  publish(p.ws, WS_XF + ((st * 4 + wave) * 4 + etl) * FS, tag, lane);
}

__device__ __forceinline__ void d_issue(const Params& p, int wave, int lane, int det, int dkp) {
  const unsigned dl = (wave * 10 + 8) * 4096;
  const int k2 = wave & 1, nfr = wave < 2 ? 3 : 2;
  unsigned qo[2];
#pragma unroll
  for (int i = 0; i < 2; ++i) {
    const int row = 8 * i + (lane >> 3);
    qo[i] = row * 6144 + (((lane & 7) ^ ((row >> 1) & 7)) << 4) - i * 1024;
  }
  const char* qb = p.qw + (size_t)(80 * det + 16 * (wave >> 1)) * 6144 + (64 * dkp + 32 * k2) * 4;
  glds4(qb, qo[0], qo[1], qo[0] + (32 * 6144 - 2048), qo[1] + (32 * 6144 - 2048), dl);
  if (wave < 2) {
    glds<0, false>(qb + 64 * 6144, qo[0], dl + 4096);
    glds<1024, false>(qb + 64 * 6144, qo[1], dl + 4096);
  }
  int i = (lane >> 3) & 3; if (i >= nfr) i = 0;
  const int grp = (5 * det + (wave >> 1) + 2 * i) >> 1;
  const char* sa = (((lane >> 2) & 1) ? p.zr : p.sc) + (size_t)grp * 3072 + (64 * dkp + 32 * k2) * 2 + (lane & 3) * 16;
  glds_v(sa, dl + 6144);
}

__device__ __forceinline__ void d_compute(const Params& p, __amdgpu_buffer_rsrc_t wsr, int wave, int lane, int det, int dkp) {
  const unsigned dl = (wave * 10 + 8) * 4096;
  const int r = lane & 15, q = lane >> 4;
  const unsigned a0 = dl + r * 128 + (((2 * q) ^ ((r >> 1) & 7)) << 4), a1 = dl + r * 128 + (((2 * q + 1) ^ ((r >> 1) & 7)) << 4);
  const unsigned as = dl + 6144 + q * 16;
  const unsigned ob = WS_WQ + ((det * 48 + 2 * dkp + (wave & 1)) * 5 + (wave >> 1)) * 1024;
  const int nfr = wave < 2 ? 3 : 2;
  i32x4 q0 = __builtin_bit_cast(i32x4, lds_rd128(a0)), q1 = __builtin_bit_cast(i32x4, lds_rd128(a1));
  u32x4 s = lds_rd128(as), z = lds_rd128(as + 64);
#pragma unroll 1
  for (int i = 0; i < nfr; ++i) {
    const i32x4 nq0 = __builtin_bit_cast(i32x4, lds_rd128(a0 + i * 2048 + 2048)), nq1 = __builtin_bit_cast(i32x4, lds_rd128(a1 + i * 2048 + 2048));
    const u32x4 ns = lds_rd128(as + i * 128 + 128), nz = lds_rd128(as + i * 128 + 192);
    u32x4 o;
    o[0] = dequant2(q0[0], q0[1], bf_lo(z[0]), bf_hi(z[0]), bf_lo(s[0]), bf_hi(s[0]));
    o[1] = dequant2(q0[2], q0[3], bf_lo(z[1]), bf_hi(z[1]), bf_lo(s[1]), bf_hi(s[1]));
    o[2] = dequant2(q1[0], q1[1], bf_lo(z[2]), bf_hi(z[2]), bf_lo(s[2]), bf_hi(s[2]));
    o[3] = dequant2(q1[2], q1[3], bf_lo(z[3]), bf_hi(z[3]), bf_lo(s[3]), bf_hi(s[3]));
    __builtin_amdgcn_raw_buffer_store_b128(o, wsr, lane * 16, ob + i * 2048, 16);
    q0 = nq0; q1 = nq1; s = ns; z = nz;
  }
}

struct VLoader {
  unsigned vo[4]; const char* vb; int rot, kq;
  __device__ __forceinline__ void init(const Params& p, int gt, int kq_, int rot_, int lane) {
    kq = kq_; rot = rot_;
#pragma unroll
    for (int pc = 0; pc < 4; ++pc) {
      const int row = 8 * pc + (lane >> 3), swz = ((row >> 1) & 1) | (((row >> 3) & 1) << 1);
      vo[pc] = row * 3072 + ((((lane & 7) >> 1) ^ swz) << 5) + ((lane & 1) << 4) - pc * 1024;
    }
    vb = p.V + gt * 128 + (size_t)kq * (12 * 32 * 3072);
  }
  __device__ __forceinline__ int kstep(int i) const { const int t = i + rot; return t >= 12 ? t - 12 : t; }
  __device__ __forceinline__ void issue_at(int i, unsigned lds) const {
    glds4(vb + (size_t)kstep(i) * (32 * 3072), vo[0], vo[1], vo[2], vo[3], lds);
  }
  __device__ __forceinline__ void issue(int i) const { issue_at(i, (kq * 10 + i) * 4096); }
};

template <int NB>
__device__ __forceinline__ void w_body(const Params& p, __amdgpu_buffer_rsrc_t wsr, const VLoader& vl, int wave, int lane, int et, int gt, u64 tag) {
  constexpr int M0 = NB == 3 ? 0 : 3, SEC = NB == 3 ? 0 : 1;
  const int kq = wave & 3;
  unsigned va0[4];
  {
    const int g = lane >> 4, qq = (lane & 15) >> 2, sw = ((qq >> 1) & 1) | ((g & 1) << 1);
#pragma unroll
    for (int c = 0; c < 4; ++c) va0[c] = kq * 40960 + 128 * (8 * g + qq) + 32 * (c ^ sw) + 8 * (lane & 3);
  }
  const unsigned ab = WS_WQ + ((et * 48 + kq * 12) * 5 + M0) * 1024;
  u32x4 af[2][NB];
  f32x4 acc[NB][4];
#pragma unroll
  for (int m = 0; m < NB; ++m)
#pragma unroll
    for (int c = 0; c < 4; ++c) acc[m][c] = (f32x4){0.f, 0.f, 0.f, 0.f};
  s16x4 bh[2][8];
  const char* wq = p.ws;
  const unsigned vl16 = lane * 16;
#pragma unroll
  for (int m = 0; m < NB; ++m)
#pragma unroll
    for (int c = 0; c < 4; ++c) asm volatile("" : "+v"(acc[m][c]));
  BAR();
  STAMP(5);
  static_for<0, 2 * NB>([&](auto ic) {
    constexpr int n = decltype(ic)::value / NB, m = decltype(ic)::value % NB;
    gload_sc1<m * 1024>(af[n][m], wq + (ab + vl.kstep(n) * 5120), vl16);
  });
#pragma unroll
  for (int j = 0; j < 8; ++j) bh[0][j] = __builtin_amdgcn_ds_read_tr16_b64_v4i16((LAS s16x4*)(size_t)(va0[j >> 1] + (j & 1) * 512));
  __builtin_amdgcn_sched_barrier(0);
  unsigned vc[4] = {va0[0], va0[1], va0[2], va0[3]};
  const unsigned vlast = (kq & 1) * 40960 + (kq >> 1) * 8192;
  auto iter = [&](int it, auto modec) {
    constexpr int MODE = decltype(modec)::value;
    const unsigned step = it == 4 ? vlast - kq * 40960 - 32768 : 8192;
    unsigned vn[4];
#pragma unroll
    for (int c = 0; c < 4; ++c) { vn[c] = vc[c] + step; opaque(vn[c]); }
    if constexpr (MODE == 1) { BAR(); vl.issue_at(10 + SEC, vlast + SEC * 4096); }
    static_for<0, 2>([&](auto hc) {
      constexpr int hf = decltype(hc)::value;
      if constexpr (hf == 1 && MODE == 0) { if (it == 4) BAR(); }
      const unsigned so = ab + vl.kstep(MODE == 2 ? 0 : 2 * it + 2 + hf) * 5120;
      static_for<0, NB * 4>([&](auto jc) {
        constexpr int j = decltype(jc)::value, m = j / 4, c = j % 4;
        if constexpr (c == 0) {
          constexpr int N = MODE == 1 ? 2 * NB + 3 : MODE == 0 ? 2 * NB - 1 : 2 * NB - 1 - (hf * NB + m);
          wait_for<N>(af[hf][m]);
        }
        const bf16x8 bv = __builtin_bit_cast(bf16x8, __builtin_shufflevector(bh[hf][2 * c], bh[hf][2 * c + 1], 0, 1, 2, 3, 4, 5, 6, 7));
        acc[m][c] = __builtin_amdgcn_mfma_f32_16x16x32_bf16(__builtin_bit_cast(bf16x8, af[hf][m]), bv, acc[m][c], 0, 0, 0);
        __builtin_amdgcn_sched_barrier(0);
        if constexpr (j < 8 && !(MODE == 2 && hf == 1))
          bh[hf ^ 1][j] = __builtin_amdgcn_ds_read_tr16_b64_v4i16((LAS s16x4*)(size_t)(hf == 0 ? vc[j >> 1] + 4096 + (j & 1) * 512 : vn[j >> 1] + (j & 1) * 512));
        if constexpr (MODE != 2 && c == 3) gload_sc1<m * 1024>(af[hf][m], wq + so, vl16);
        __builtin_amdgcn_sched_barrier(0);
      });
    });
#pragma unroll
    for (int c = 0; c < 4; ++c) vc[c] = vn[c];
  };
#pragma unroll 1
  for (int it = 0; it < 2; ++it) iter(it, IC<0>{});
  iter(2, IC<1>{});
#pragma unroll 1
  for (int it = 3; it < 5; ++it) iter(it, IC<0>{});
  iter(5, IC<2>{});
  STAMP(6);
  const unsigned red = 81920 + kq * 20480 + lane * 16;
#pragma unroll
  for (int m = 0; m < NB; ++m)
#pragma unroll
    for (int c = 0; c < 4; ++c) lds_wr128(red + ((M0 + m) * 4 + c) * 1024, __builtin_bit_cast(u32x4, acc[m][c]));
  WAIT_LGKM0();
  BAR();
  if constexpr (NB == 3) {
    STAMP(10);
    const unsigned ol = ((lane & 15) + 16 * (lane >> 5) + 32 * ((lane >> 4) & 1)) * 16;
    f32x4 t[3][2][4];
#pragma unroll
    for (int j = 0; j < 3; ++j) {
      const int fi = kq + (j == 2 && kq >= 2 ? 4 : 4 * j);
      const unsigned rb = 81920 + lane * 16 + fi * 2048;
#pragma unroll
      for (int hf = 0; hf < 2; ++hf)
#pragma unroll
        for (int q = 0; q < 4; ++q) t[j][hf][q] = __builtin_bit_cast(f32x4, lds_rd128(rb + q * 20480 + hf * 1024));
    }
#pragma unroll
    for (int j = 0; j < 3; ++j) {
      const int fi = kq + (j == 2 && kq >= 2 ? 4 : 4 * j);
      const f32x4 a = (t[j][0][0] + t[j][0][1]) + (t[j][0][2] + t[j][0][3]), b = (t[j][1][0] + t[j][1][1]) + (t[j][1][2] + t[j][1][3]);
      auto r0 = __builtin_amdgcn_permlane32_swap(pack_bf16(a[0], a[1]), pack_bf16(b[0], b[1]), false, false);
      auto r1 = __builtin_amdgcn_permlane32_swap(pack_bf16(a[2], a[3]), pack_bf16(b[2], b[3]), false, false);
      const u32x4 v = {r0[0], r1[0], r0[1], r1[1]};
      __builtin_amdgcn_raw_buffer_store_b128(v, wsr, ol, WS_W2 + ((2 * gt + (fi & 1)) * 40 + 5 * et + (fi >> 1)) * 1024, 16);
    }
    if (SEAM == 1) BAR();
    STAMP(7);
    publish(p.ws, WS_WF + ((gt * 8 + et) * 4 + kq) * FS, tag, lane);
    if (SEAM == 2) BAR();
  }
}

constexpr unsigned YW = 80 * 1024;

__device__ __forceinline__ void y_load(const Params& p, int wave, int lane, int st, int gt, u64 tag) {
  const int lw = wave - 4;
  STAMP(8);
  if (lw == 0) wait_flags(p.ws, WS_WF + ((3 * gt / 2) * 32 + lane) * FS, tag);
  if (blockIdx.x == 0) {
    const int w = lw * 64 + lane;
    wait_flags(p.ws, w < 64 ? WS_XF + ((w >> 2) * 16 + (w & 3)) * FS : WS_WF + (w - 64) * 4 * FS, tag);
  }
  BAR();
  STAMP(11);
  const char* wb = p.ws + WS_W2 + (size_t)(3 * gt * 40 + lw) * 1024;
  const char* xb = p.ws + WS_XR + (size_t)(st * 4 + lw) * 40960;
  const unsigned vo = lane * 16;
  auto issue_w = [&](int c, unsigned slot) {
#pragma unroll
    for (int t = 0; t < 3; ++t) glds<0, true>(wb + c * 4096 + t * 40960, vo, YW + slot * 12288 + lw * 1024 + t * 4096);
  };
  auto issue_x = [&](int c, unsigned slot) { glds4_lin_sc1(xb + c * 4096, vo, slot * 16384 + lw * 4096); };
  constexpr int n0 = 3 + (XPRE > 0 ? 0 : 4), n1 = 3 + (XPRE > 1 ? 0 : 4), n2 = 3 + (XPRE > 2 ? 0 : 4), n3 = 3 + (XPRE > 3 ? 0 : 4), n4 = 3 + (XPRE > 4 ? 0 : 4);
  auto issue0 = [&](int c) { issue_w(c, c); if (c >= XPRE) issue_x(c, c); };
  issue0(0); issue0(1); issue0(2);
  WAIT_VM(n1 + n2); BAR();
  if (blockIdx.x == 0 && threadIdx.x == 256) __hip_atomic_store((gu64*)(p.ws + WS_EPOCH), tag, RLX_AGENT);
  issue0(3); issue0(4);
  WAIT_VM(n2 + n3 + n4); BAR();
  WAIT_VM(n3 + n4); BAR();
  issue_w(5, 0); issue_x(5, 0);
  WAIT_VM(n4 + 7); BAR();
#pragma unroll 1
  for (int c = 6; c < 10; ++c) {
    issue_w(c, c - 5); issue_x(c, c - 5);
    WAIT_VM(14); BAR();
  }
  WAIT_VM(7); BAR();
  WAIT_VM(0); BAR();
  BAR();
  (void)n0;
}

__device__ __forceinline__ void y_compute(const Params& p, int wave, int lane, int st, int gt, u64 tag, bool b4) {
  STAMP(8);
  if (SEAM > 0 && !POLLFIRST && b4) BAR();
  wait_flags(p.ws, WS_XF + ((st * 4 + wave) * 4 + (lane & 3)) * FS, tag);
  if (SEAM > 0 && POLLFIRST && b4) BAR();
  STAMP(9);
  {
    const char* xb = p.ws + WS_XR + (size_t)(st * 4 + wave) * 40960;
#pragma unroll
    for (int c = 0; c < XPRE; ++c) glds4_lin_sc1(xb + c * 4096, lane * 16, c * 16384 + wave * 4096);
  }
  STAMP(10);
  f32x16 acc[3];
#pragma unroll
  for (int t = 0; t < 3; ++t)
#pragma unroll
    for (int j = 0; j < 16; ++j) acc[t][j] = 0.f;
  constexpr int L = 5;
  bf16x8 aw[6], bx[4];
  const unsigned a0 = YW + lane * 16, x0 = wave * 4096 + lane * 16;
#pragma unroll
  for (int t = 0; t < 3; ++t) asm volatile("" : "+v"(acc[t]));
  BAR();
  WAIT_VM(0);
  BAR();
  STAMP(11);
#pragma unroll
  for (int n = 0; n < L; ++n) {
    if (n % 3 == 0) bx[n / 3] = __builtin_bit_cast(bf16x8, lds_rd128(x0 + (n / 3) * 1024));
    __builtin_amdgcn_sched_barrier(0);
    aw[n] = __builtin_bit_cast(bf16x8, lds_rd128(a0 + (n % 3) * 4096 + (n / 3) * 1024));
    __builtin_amdgcn_sched_barrier(0);
  }
  unsigned cur = 0;
#pragma unroll 1
  for (int c = 0; c < 10; ++c) {
    const unsigned nxt = cur == 4 ? 0 : cur + 1;
    const unsigned ac = a0 + cur * 12288, an = a0 + nxt * 12288, xc = x0 + cur * 16384, xn = x0 + nxt * 16384;
    static_for<0, 12>([&](auto jc) {
      constexpr int j = decltype(jc)::value, n = j + L;
      if constexpr (n == 12) BAR();
      constexpr int kk = (n % 12) / 3, t = n % 3;
      if constexpr (t == 0) bx[kk] = __builtin_bit_cast(bf16x8, lds_rd128((n < 12 ? xc : xn) + kk * 1024));
      aw[n % 6] = __builtin_bit_cast(bf16x8, lds_rd128((n < 12 ? ac : an) + t * 4096 + kk * 1024));
      acc[j % 3] = __builtin_amdgcn_mfma_f32_32x32x16_bf16(aw[j % 6], bx[j / 3], acc[j % 3], 0, 0, 0);
      __builtin_amdgcn_sched_barrier(0);
    });
    cur = nxt;
  }
  STAMP(12);
  const __amdgpu_buffer_rsrc_t yr = __builtin_amdgcn_make_buffer_rsrc(p.y, 0, S_ * F_ * 2, 0x00020000);
  const unsigned yo = (st * 128 + wave * 32 + (lane & 31)) * 3072 + gt * 192 + (lane >> 5) * 16;
#pragma unroll
  for (int t = 0; t < 3; ++t)
#pragma unroll
    for (int i = 0; i < 4; i += 2) {
      auto r0 = __builtin_amdgcn_permlane32_swap(pack_bf16(acc[t][4 * i], acc[t][4 * i + 1]), pack_bf16(acc[t][4 * i + 4], acc[t][4 * i + 5]), false, false);
      auto r1 = __builtin_amdgcn_permlane32_swap(pack_bf16(acc[t][4 * i + 2], acc[t][4 * i + 3]), pack_bf16(acc[t][4 * i + 6], acc[t][4 * i + 7]), false, false);
      const u32x4 v = {r0[0], r1[0], r0[1], r1[1]};
      __builtin_amdgcn_raw_buffer_store_b128(v, yr, yo + t * 64 + i * 16, 0, YAUX);
    }
}

__global__ void __launch_bounds__(512, 2) quip_kernel(Params p) {
  const int wave = __builtin_amdgcn_readfirstlane(threadIdx.x >> 6), lane = threadIdx.x & 63;
  const int xcd = blockIdx.x & 7, idx = blockIdx.x >> 3;
  STAMP(0); STAMPC(14);
  unsigned pf = 0;
  {
    const unsigned ln = idx * 8 + wave;
    const GAS unsigned* code = (const GAS unsigned*)((__builtin_amdgcn_s_getpc() & ~255ull) + (ln < CODEKB * 8 ? ln : 0) * 128);
    pf = __hip_atomic_load(code, __ATOMIC_RELAXED, __HIP_MEMORY_SCOPE_WORKGROUP);
  }
  const bool isx = idx < 8;
  const int xst = 4 * (xcd >> 1) + (idx >> 1), etl = 2 * (xcd & 1) + (idx & 1);
  const int wk = idx - 8, wk6 = wk / 6;
  const int et = 4 * (xcd >> 2) + wk6, gt = 6 * (xcd & 3) + wk - 6 * wk6;
  const int dj = xcd * 24 + wk, det = xcd, dkp = wk;
  const int rotc = (ROTC * 2 * (wk - 6 * wk6) + ROTE * 3 * wk6) % 12;
  const int rotx = ROTX * ((idx >> 1) * 2 + (idx & 1) * 5) % 10;
  XLoader xl; VLoader vl;
  if (!isx && wave < 4) d_issue(p, wave, lane, det, dkp);
  if (isx && wave >= 4) { xl.init(p, xst, etl, wave - 4, lane, rotx); xl.issue(0); xl.issue(1); }
  if (!isx) {
    vl.init(p, gt, wave & 3, rotc, lane);
    if (wave >= 4) {
#pragma unroll
      for (int i = 0; i < VEARLY; ++i) vl.issue(i);
    }
  }
  const __amdgpu_buffer_rsrc_t wsr = __builtin_amdgcn_make_buffer_rsrc(p.ws, 0, WS_END, 0x00020000);
  const u64 tag = load_tag(p);
  asm volatile("" :: "s"(tag));
  const int yst = 4 * (xcd >> 1) + (idx >> 3), ygt = 8 * (xcd & 1) + (idx & 7);
  if (wave < 4) {
    if (isx) {
      x_compute(p, wsr, wave, lane, xst, etl, tag);
      asm volatile("" :: "v"(pf));
    } else {
      WAIT_VM(0);
      asm volatile("" :: "v"(pf));
      STAMP(1);
      d_compute(p, wsr, wave, lane, det, dkp);
      STAMP(2);
      WAIT_VM(0);
      BAR();
      if (wave == 0) {
        publish(p.ws, WS_DF + dj * FS, tag, lane);
        STAMP(3);
        wait_flags(p.ws, WS_DF + (et * 24 + (lane < 24 ? lane : 0)) * FS, tag);
      }
      STAMP(4);
      w_body<2>(p, wsr, vl, wave, lane, et, gt, tag);
    }
    y_compute(p, wave, lane, yst, ygt, tag, !isx);
  } else {
    if (isx) {
      xl.run();
      asm volatile("" :: "v"(pf));
      __builtin_amdgcn_s_setprio(3);
    } else {
      BAR();
      STAMP(3);
#pragma unroll 1
      for (int i = VEARLY; i < 10; ++i) vl.issue(i);
      WAIT_VM(0);
      asm volatile("" :: "v"(pf));
      STAMP(4);
      __builtin_amdgcn_s_setprio(3);
      w_body<3>(p, wsr, vl, wave, lane, et, gt, tag);
    }
    y_load(p, wave, lane, yst, ygt, tag);
  }
  STAMP(13); STAMPC(15);
  while (__builtin_amdgcn_s_memrealtime() < p.t0 + 5000 + p.d) __builtin_amdgcn_s_sleep(1);
}

extern "C" void kernel_launch(void* const* d_in, const int* in_sizes, int n_in, void* d_out, int out_size, void* d_ws,
                              size_t ws_size, hipStream_t stream) {
  Params p;
  p.x = (const char*)d_in[0]; p.qw = (const char*)d_in[1]; p.sc = (const char*)d_in[2]; p.zr = (const char*)d_in[3];
  p.U = (const char*)d_in[4]; p.V = (const char*)d_in[5]; p.y = (char*)d_out; p.ws = (char*)d_ws; p.t0 = 0; p.d = 0;
  (void)hipFuncSetAttribute((const void*)quip_kernel, hipFuncAttributeMaxDynamicSharedMemorySize, LDS_BYTES);
  quip_kernel<<<256, 512, LDS_BYTES, stream>>>(p);
}
```

```cpp
#include <hip/hip_runtime.h>
#include <stdint.h>

#define LAS __attribute__((address_space(3)))
#define GAS __attribute__((address_space(1)))
typedef unsigned short u16;
typedef unsigned long long u64;
typedef short s16x4 __attribute__((ext_vector_type(4)));
typedef short s16x8 __attribute__((ext_vector_type(8)));
typedef __bf16 bf16x8 __attribute__((ext_vector_type(8)));
typedef __bf16 bf16x2 __attribute__((ext_vector_type(2)));
typedef float f32x2 __attribute__((ext_vector_type(2)));
typedef float f32x4 __attribute__((ext_vector_type(4)));
typedef float f32x16 __attribute__((ext_vector_type(16)));
typedef unsigned u32x4 __attribute__((ext_vector_type(4)));
typedef int i32x4 __attribute__((ext_vector_type(4)));
typedef GAS u64 gu64;

constexpr int S_ = 2048, D_ = 640, F_ = 1536;
constexpr unsigned WS_EPOCH = 0;
constexpr unsigned FS = 128;
constexpr unsigned WS_DF = 4096;
constexpr unsigned WS_XF = WS_DF + 192 * FS;
constexpr unsigned WS_WF = WS_XF + 256 * FS;
constexpr unsigned WS_STAMP = 512 * 1024;
constexpr unsigned WS_WQ = 1u << 20;
constexpr unsigned WS_XR = WS_WQ + D_ * F_ * 2;
constexpr unsigned WS_W2 = WS_XR + S_ * D_ * 2;
constexpr unsigned WS_END = WS_W2 + D_ * F_ * 2;
constexpr int LDS_BYTES = 160 * 1024;

#define XSPREAD 1
#define ROTX 1
#define ROTC 0
#define WMAP 1
#define DPOLL 1
#define PFMAP 0
#define VDELAY 0
#define FPACK 1
#define FTOUCH 0
#define ROTY 0
#define YDELAY 0
#define YMAP 1
#define VMID 0
#define VPAUSE 0
#define DDRY 0
#define EPI 1
#define XLA 6
#define VEARLY 2
#define SEAM 2
#define POLLFIRST 1
#define PSLEEP 8
#define XPRE 5
#define YAUX 2
#define CODEKB 16

__device__ __forceinline__ unsigned xf_off(int st, int etl) { return WS_XF + (st * 4 + etl) * FS; }
__device__ __forceinline__ unsigned wf_off(int i) { return WS_WF + i * FS; }
constexpr unsigned LW = 79 * 1024;

struct Params {
  const char* x; const char* qw; const char* sc; const char* zr; const char* U; const char* V; char* y; char* ws;
};

#define RLX_AGENT __ATOMIC_RELAXED, __HIP_MEMORY_SCOPE_AGENT
#define WAIT_VM(n) asm volatile("s_waitcnt vmcnt(%0)" :: "n"(n) : "memory")
#define WAIT_LGKM0() asm volatile("s_waitcnt lgkmcnt(0)" ::: "memory")
#define BAR() do { asm volatile("" ::: "memory"); __builtin_amdgcn_s_barrier(); asm volatile("" ::: "memory"); } while (0)
#define FENCE() asm volatile("" ::: "memory")

#define STAMP(i) do {} while (0)
#define STAMPC(i) do {} while (0)

#define TS(v)
#define TS2(v)
template <int I> struct IC { static constexpr int value = I; };
template <int B, int E, class Fn>
__device__ __forceinline__ void static_for(Fn&& fn) {
  if constexpr (B < E) {
    if constexpr (E - B == 1) fn(IC<B>{});
    else { static_for<B, (B + E) / 2>(fn); static_for<(B + E) / 2, E>(fn); }
  }
}

template <int OFF, bool SC1>
__device__ __forceinline__ void glds(const char* sbase, unsigned voff, unsigned lds) {
  if constexpr (SC1)
    asm volatile("s_mov_b32 m0, %2\n\ts_nop 0\n\tglobal_load_lds_dwordx4 %0, %1 offset:%3 sc1" :: "v"(voff), "s"(sbase), "s"(lds), "n"(OFF) : "memory");
  else
    asm volatile("s_mov_b32 m0, %2\n\ts_nop 0\n\tglobal_load_lds_dwordx4 %0, %1 offset:%3" :: "v"(voff), "s"(sbase), "s"(lds), "n"(OFF) : "memory");
}
__device__ __forceinline__ void glds4(const char* sbase, unsigned v0, unsigned v1, unsigned v2, unsigned v3, unsigned lds) {
  asm volatile("s_mov_b32 m0, %5\n\ts_nop 0\n\tglobal_load_lds_dwordx4 %0, %4\n\tglobal_load_lds_dwordx4 %1, %4 offset:1024\n\t"
               "global_load_lds_dwordx4 %2, %4 offset:2048\n\tglobal_load_lds_dwordx4 %3, %4 offset:3072"
               :: "v"(v0), "v"(v1), "v"(v2), "v"(v3), "s"(sbase), "s"(lds) : "memory");
}
__device__ __forceinline__ void glds4_lin_sc1(const char* sbase, unsigned v, unsigned lds) {
  asm volatile("s_mov_b32 m0, %2\n\ts_nop 0\n\tglobal_load_lds_dwordx4 %0, %1 sc1\n\tglobal_load_lds_dwordx4 %0, %1 offset:1024 sc1\n\t"
               "global_load_lds_dwordx4 %0, %1 offset:2048 sc1\n\tglobal_load_lds_dwordx4 %0, %1 offset:3072 sc1"
               :: "v"(v), "s"(sbase), "s"(lds) : "memory");
}
template <int OFF>
__device__ __forceinline__ void gload_sc1(u32x4& d, const char* sbase, unsigned voff) {
  asm volatile("global_load_dwordx4 %0, %1, %2 offset:%3 sc1" : "=v"(d) : "v"(voff), "s"(sbase), "n"(OFF) : "memory");
}
template <int N>
__device__ __forceinline__ void wait_for(u32x4& d) { asm volatile("s_waitcnt vmcnt(%1)" : "+v"(d) : "n"(N) : "memory"); }
__device__ __forceinline__ void opaque(unsigned& v) { asm volatile("" : "+v"(v)); }
__device__ __forceinline__ void glds_v(const char* vaddr, unsigned lds) {
  asm volatile("s_mov_b32 m0, %1\n\ts_nop 0\n\tglobal_load_lds_dwordx4 %0, off" :: "v"(vaddr), "s"(lds) : "memory");
}

__device__ __forceinline__ unsigned pack_bf16(float a, float b) {
  f32x2 v = {a, b};
  return __builtin_bit_cast(unsigned, __builtin_convertvector(v, bf16x2));
}
__device__ __forceinline__ float bf_lo(unsigned v) { return __builtin_bit_cast(float, v << 16); }
__device__ __forceinline__ float bf_hi(unsigned v) { return __builtin_bit_cast(float, v & 0xffff0000u); }

__device__ __forceinline__ u32x4 lds_rd128(unsigned a) { return *(LAS u32x4*)(size_t)a; }
__device__ __forceinline__ void lds_wr128(unsigned a, u32x4 v) { *(LAS u32x4*)(size_t)a = v; }
__device__ __forceinline__ bf16x8 lds_tr2(unsigned a0, unsigned a1) {
  s16x4 lo = __builtin_amdgcn_ds_read_tr16_b64_v4i16((LAS s16x4*)(size_t)a0);
  s16x4 hi = __builtin_amdgcn_ds_read_tr16_b64_v4i16((LAS s16x4*)(size_t)a1);
  return __builtin_bit_cast(bf16x8, __builtin_shufflevector(lo, hi, 0, 1, 2, 3, 4, 5, 6, 7));
}

__device__ __forceinline__ u64 load_tag(const Params& p) {
  const char* e = p.ws + WS_EPOCH;
  asm volatile("" : "+s"(e));
  return *(const __attribute__((address_space(4))) u64*)e;
}
__device__ __forceinline__ void make_tag(u64& e) {
  asm volatile("" : "+s"(e));
  e += 0x9E3779B97F4A7C15ull;
  asm volatile("" : "+s"(e));
}

__device__ __forceinline__ void wait_flags(const char* ws, unsigned off, u64 tag) {
  gu64* f = (gu64*)(ws + off);
#pragma unroll 1
  for (unsigned spins = 0; spins < (1u << 20); ++spins) {
    u64 v = __hip_atomic_load(f, RLX_AGENT);
    if (__all(v == tag)) break;
    __builtin_amdgcn_s_sleep(PSLEEP);
  }
  FENCE();
}
typedef LAS unsigned lu32;
__device__ __forceinline__ void lds_set(unsigned a) { __hip_atomic_store((lu32*)(size_t)a, 1u, __ATOMIC_RELAXED, __HIP_MEMORY_SCOPE_WORKGROUP); FENCE(); }
__device__ __forceinline__ void lds_wait(unsigned a) {
#pragma unroll 1
  for (unsigned spins = 0; spins < (1u << 24); ++spins) {
    if (__builtin_amdgcn_readfirstlane(__hip_atomic_load((lu32*)(size_t)a, __ATOMIC_RELAXED, __HIP_MEMORY_SCOPE_WORKGROUP))) break;
    __builtin_amdgcn_s_sleep(1);
  }
  FENCE();
}
__device__ __forceinline__ void wait_flags2(__amdgpu_buffer_rsrc_t wsr, unsigned off, u64 tag) {
  const unsigned lo = (unsigned)tag, hi = (unsigned)(tag >> 32);
#pragma unroll 1
  for (unsigned spins = 0; spins < (1u << 20); ++spins) {
    FENCE();
    const u32x4 v = __builtin_amdgcn_raw_buffer_load_b128(wsr, off, 0, 16);
    if (__all(v[0] == lo && v[1] == hi && v[2] == lo && v[3] == hi)) break;
    __builtin_amdgcn_s_sleep(PSLEEP);
  }
  FENCE();
}
__device__ __forceinline__ void publish(char* ws, unsigned off, u64 tag, int lane) {
  WAIT_VM(0);
  if (lane == 0) __hip_atomic_store((gu64*)(ws + off), tag, RLX_AGENT);
  FENCE();
}

__device__ __forceinline__ unsigned dequant2(int q0, int q1, float z0, float z1, float s0, float s1) {
  unsigned qb = pack_bf16((float)q0, (float)q1);
  unsigned t = pack_bf16(bf_lo(qb) - z0, bf_hi(qb) - z1);
  return pack_bf16(bf_lo(t) * s0, bf_hi(t) * s1);
}

constexpr unsigned XSTG = 36 * 1024, XUOFF = 16 * 1024;

struct XLoader {
  unsigned xo[4], uo[5]; const char* xb; const char* ub; int rot, lw;
  __device__ __forceinline__ void init(const Params& p, int st, int etl, int lw_, int lane, int rot_) {
    lw = lw_;
    rot = rot_;
#pragma unroll
    for (int i = 0; i < 4; ++i) {
      const int row = 8 * i + (lane >> 3);
      xo[i] = row * 1280 + (((lane & 7) ^ ((row >> 1) & 7)) << 4) - i * 1024;
    }
#pragma unroll
    for (int i = 0; i < 5; ++i) {
      const int u = i * 64 + lane, row = (u * 3277) >> 16;
      uo[i] = row * 960 + u * 16 - (i & 3) * 1024;
    }
    xb = p.x + (size_t)(st * 128 + lw * 32) * 1280;
    ub = p.U + etl * 320 + lw * (16 * 1280);
  }
  __device__ __forceinline__ void issue(int i) {
    int kt = i + rot; if (kt >= 10) kt -= 10;
    const unsigned slot = (i & 3) * XSTG;
    const char* us = ub + (size_t)kt * (64 * 1280);
    glds4(us, uo[0], uo[1], uo[2], uo[3], slot + XUOFF + lw * 5120);
    glds<0, false>(us, uo[4], slot + XUOFF + lw * 5120 + 4096);
    glds4(xb + kt * 128, xo[0], xo[1], xo[2], xo[3], slot + lw * 4096);
  }
  __device__ __forceinline__ void run(u64& tag, u64 (&ts)[10]) {
    WAIT_VM(9); TS2(ts[0]); BAR();
#pragma unroll 1
    for (int i = 1; i < 9; ++i) {
      issue(i + 1); WAIT_VM(9);
      if (i == 1) TS2(ts[1]); if (i == 2) TS2(ts[2]); if (i == 4) TS2(ts[4]); if (i == 7) TS2(ts[6]);
      BAR();
      if (i == 2) TS2(ts[3]); if (i == 4) TS2(ts[5]); if (i == 7) TS2(ts[7]);
    }
    make_tag(tag);
    WAIT_VM(0); BAR();
    lds_wr128(LW, (u32x4){0u, 0u, 0u, 0u});
    WAIT_LGKM0();
    BAR();
    BAR();
  }
};

__device__ __forceinline__ void x_compute(const Params& p, __amdgpu_buffer_rsrc_t wsr, int wave, int lane, int st, int etl, u64& tag) {
  const int r = lane & 31, h = lane >> 5;
  unsigned xa0[4];
#pragma unroll
  for (int kk = 0; kk < 4; ++kk) xa0[kk] = wave * 4096 + r * 128 + (((2 * kk + h) ^ ((r >> 1) & 7)) << 4);
  const unsigned ua0 = XUOFF + (8 * h + ((lane & 15) >> 2)) * 320 + ((lane >> 4) & 1) * 32 + (lane & 3) * 8;
  f32x16 acc[5];
#pragma unroll
  for (int t = 0; t < 5; ++t)
#pragma unroll
    for (int j = 0; j < 16; ++j) acc[t][j] = 0.f;
  constexpr int L = XLA;
  bf16x8 au[10], bx[4];
#pragma unroll
  for (int t = 0; t < 5; ++t) asm volatile("" : "+v"(acc[t]));
  make_tag(tag);
  BAR();
  STAMP(1);
#pragma unroll
  for (int n = 0; n < L; ++n) {
    if (n % 5 == 0) bx[n / 5] = __builtin_bit_cast(bf16x8, lds_rd128(xa0[n / 5]));
    __builtin_amdgcn_sched_barrier(0);
    au[n] = lds_tr2(ua0 + (n / 5) * 16 * 320 + (n % 5) * 64, ua0 + (n / 5) * 16 * 320 + (n % 5) * 64 + 4 * 320);
    __builtin_amdgcn_sched_barrier(0);
  }
#pragma unroll 1
  for (int i = 0; i < 10; ++i) {
    const unsigned cur = (i & 3) * XSTG, nxt = ((i + 1) & 3) * XSTG;
    const unsigned ua = ua0 + cur, un = ua0 + nxt;
    static_for<0, 20>([&](auto jc) {
      constexpr int j = decltype(jc)::value, n = j + L;
      if constexpr (n == 20) BAR();
      constexpr int kk = (n % 20) / 5, t = n % 5;
      if constexpr (t == 0) { bx[kk] = __builtin_bit_cast(bf16x8, lds_rd128(xa0[kk] + (n < 20 ? cur : nxt))); __builtin_amdgcn_sched_barrier(0); }
      const unsigned ad = (n < 20 ? ua : un) + kk * 16 * 320 + t * 64;
      au[n % 10] = lds_tr2(ad, ad + 4 * 320);
      acc[j % 5] = __builtin_amdgcn_mfma_f32_32x32x16_bf16(au[j % 10], bx[j / 5], acc[j % 5], 0, 0, 0);
      __builtin_amdgcn_sched_barrier(0);
    });
  }
  STAMP(6);
  const unsigned ob = WS_XR + ((st * 4 + wave) * 40 + etl * 10) * 1024;
#pragma unroll
  for (int t = 0; t < 5; ++t)
#pragma unroll
    for (int s2 = 0; s2 < 2; ++s2) {
      u32x4 v;
#pragma unroll
      for (int j = 0; j < 4; ++j) v[j] = pack_bf16(acc[t][8 * s2 + 2 * j], acc[t][8 * s2 + 2 * j + 1]);
      __builtin_amdgcn_raw_buffer_store_b128(v, wsr, lane * 16, ob + (2 * t + s2) * 1024, 16);
    }
  STAMP(7);
  WAIT_LGKM0();
  WAIT_VM(0);
  BAR();
  if (wave == 0) publish(p.ws, xf_off(st, etl), tag, lane);
}

__device__ __forceinline__ void d_issue(const Params& p, int wave, int lane, int det, int dkp) {
  const unsigned dl = (wave * 10 + 8) * 4096;
  const int k2 = wave & 1, nfr = wave < 2 ? 3 : 2;
  unsigned qo[2];
#pragma unroll
  for (int i = 0; i < 2; ++i) {
    const int row = 8 * i + (lane >> 3);
    qo[i] = row * 6144 + (((lane & 7) ^ ((row >> 1) & 7)) << 4) - i * 1024;
  }
  const char* qb = p.qw + (size_t)(80 * det + 16 * (wave >> 1)) * 6144 + (64 * dkp + 32 * k2) * 4;
  glds4(qb, qo[0], qo[1], qo[0] + (32 * 6144 - 2048), qo[1] + (32 * 6144 - 2048), dl);
  if (wave < 2) {
    glds<0, false>(qb + 64 * 6144, qo[0], dl + 4096);
    glds<1024, false>(qb + 64 * 6144, qo[1], dl + 4096);
  }
  int i = (lane >> 3) & 3; if (i >= nfr) i = 0;
  const int grp = (5 * det + (wave >> 1) + 2 * i) >> 1;
  const char* sa = (((lane >> 2) & 1) ? p.zr : p.sc) + (size_t)grp * 3072 + (64 * dkp + 32 * k2) * 2 + (lane & 3) * 16;
  glds_v(sa, dl + 6144);
}

__device__ __forceinline__ void d_compute(const Params& p, __amdgpu_buffer_rsrc_t wsr, int wave, int lane, int det, int dkp) {
  const unsigned dl = (wave * 10 + 8) * 4096;
  const int r = lane & 15, q = lane >> 4;
  const unsigned a0 = dl + r * 128 + (((2 * q) ^ ((r >> 1) & 7)) << 4), a1 = dl + r * 128 + (((2 * q + 1) ^ ((r >> 1) & 7)) << 4);
  const unsigned as = dl + 6144 + q * 16;
  const unsigned ob = WS_WQ + ((det * 48 + 2 * dkp + (wave & 1)) * 5 + (wave >> 1)) * 1024;
  const int nfr = wave < 2 ? 3 : 2;
  i32x4 q0, q1; u32x4 s, z;
  if (!DDRY) {
    WAIT_VM(0);
    q0 = __builtin_bit_cast(i32x4, lds_rd128(a0)); q1 = __builtin_bit_cast(i32x4, lds_rd128(a1));
    s = lds_rd128(as); z = lds_rd128(as + 64);
  } else {
    q0 = q1 = (i32x4){0, 0, 0, 0}; s = z = (u32x4){0u, 0u, 0u, 0u};
  }
#pragma unroll 1
  for (int i = DDRY ? -1 : 0; i < nfr; ++i) {
    u32x4 o;
    if (DDRY) {
      o[0] = dequant2(q0[0], q0[1], bf_lo(z[0]), bf_hi(z[0]), bf_lo(s[0]), bf_hi(s[0]));
      o[1] = dequant2(q0[2], q0[3], bf_lo(z[1]), bf_hi(z[1]), bf_lo(s[1]), bf_hi(s[1]));
      o[2] = dequant2(q1[0], q1[1], bf_lo(z[2]), bf_hi(z[2]), bf_lo(s[2]), bf_hi(s[2]));
      o[3] = dequant2(q1[2], q1[3], bf_lo(z[3]), bf_hi(z[3]), bf_lo(s[3]), bf_hi(s[3]));
      asm volatile("" : "+v"(o));
      if (i < 0) WAIT_VM(0);
      else __builtin_amdgcn_raw_buffer_store_b128(o, wsr, lane * 16, ob + i * 2048, 16);
      q0 = __builtin_bit_cast(i32x4, lds_rd128(a0 + i * 2048 + 2048)); q1 = __builtin_bit_cast(i32x4, lds_rd128(a1 + i * 2048 + 2048));
      s = lds_rd128(as + i * 128 + 128); z = lds_rd128(as + i * 128 + 192);
    } else {
      const i32x4 nq0 = __builtin_bit_cast(i32x4, lds_rd128(a0 + i * 2048 + 2048)), nq1 = __builtin_bit_cast(i32x4, lds_rd128(a1 + i * 2048 + 2048));
      const u32x4 ns = lds_rd128(as + i * 128 + 128), nz = lds_rd128(as + i * 128 + 192);
      o[0] = dequant2(q0[0], q0[1], bf_lo(z[0]), bf_hi(z[0]), bf_lo(s[0]), bf_hi(s[0]));
      o[1] = dequant2(q0[2], q0[3], bf_lo(z[1]), bf_hi(z[1]), bf_lo(s[1]), bf_hi(s[1]));
      o[2] = dequant2(q1[0], q1[1], bf_lo(z[2]), bf_hi(z[2]), bf_lo(s[2]), bf_hi(s[2]));
      o[3] = dequant2(q1[2], q1[3], bf_lo(z[3]), bf_hi(z[3]), bf_lo(s[3]), bf_hi(s[3]));
      __builtin_amdgcn_raw_buffer_store_b128(o, wsr, lane * 16, ob + i * 2048, 16);
      q0 = nq0; q1 = nq1; s = ns; z = nz;
    }
  }
}

struct VLoader {
  unsigned vo[4]; const char* vb; int rot, kq;
  __device__ __forceinline__ void init(const Params& p, int gt, int kq_, int rot_, int lane) {
    kq = kq_; rot = rot_;
#pragma unroll
    for (int pc = 0; pc < 4; ++pc) {
      const int row = 8 * pc + (lane >> 3), swz = ((row >> 1) & 1) | (((row >> 3) & 1) << 1);
      vo[pc] = row * 3072 + ((((lane & 7) >> 1) ^ swz) << 5) + ((lane & 1) << 4) - pc * 1024;
    }
    vb = p.V + gt * 128 + (size_t)kq * (12 * 32 * 3072);
  }
  __device__ __forceinline__ int kstep(int i) const { const int t = i + rot; return t >= 12 ? t - 12 : t; }
  __device__ __forceinline__ void issue_at(int i, unsigned lds) const {
    glds4(vb + (size_t)kstep(i) * (32 * 3072), vo[0], vo[1], vo[2], vo[3], lds);
  }
  __device__ __forceinline__ void issue(int i) const { issue_at(i, (kq * 10 + i) * 4096); }
};

template <int NB>
__device__ __forceinline__ void w_body(const Params& p, __amdgpu_buffer_rsrc_t wsr, const VLoader& vl, int wave, int lane, int et, int gt, u64 tag) {
  constexpr int M0 = NB == 3 ? 0 : 3, SEC = NB == 3 ? 0 : 1;
  const int kq = wave & 3;
  unsigned va0[4];
  {
    const int g = lane >> 4, qq = (lane & 15) >> 2, sw = ((qq >> 1) & 1) | ((g & 1) << 1);
#pragma unroll
    for (int c = 0; c < 4; ++c) va0[c] = kq * 40960 + 128 * (8 * g + qq) + 32 * (c ^ sw) + 8 * (lane & 3);
  }
  const unsigned ab = WS_WQ + ((et * 48 + kq * 12) * 5 + M0) * 1024;
  u32x4 af[2][NB];
  f32x4 acc[NB][4];
#pragma unroll
  for (int m = 0; m < NB; ++m)
#pragma unroll
    for (int c = 0; c < 4; ++c) acc[m][c] = (f32x4){0.f, 0.f, 0.f, 0.f};
  s16x4 bh[2][8];
  const char* wq = p.ws;
  const unsigned vl16 = lane * 16;
#pragma unroll
  for (int m = 0; m < NB; ++m)
#pragma unroll
    for (int c = 0; c < 4; ++c) asm volatile("" : "+v"(acc[m][c]));
  BAR();
  STAMP(5);
  static_for<0, 2 * NB>([&](auto ic) {
    constexpr int n = decltype(ic)::value / NB, m = decltype(ic)::value % NB;
    gload_sc1<m * 1024>(af[n][m], wq + (ab + vl.kstep(n) * 5120), vl16);
  });
#pragma unroll
  for (int j = 0; j < 8; ++j) bh[0][j] = __builtin_amdgcn_ds_read_tr16_b64_v4i16((LAS s16x4*)(size_t)(va0[j >> 1] + (j & 1) * 512));
  __builtin_amdgcn_sched_barrier(0);
  unsigned vc[4] = {va0[0], va0[1], va0[2], va0[3]};
  const unsigned vlast = (kq & 1) * 40960 + (kq >> 1) * 8192;
  auto iter = [&](int it, auto modec) {
    constexpr int MODE = decltype(modec)::value;
    const unsigned step = it == 4 ? vlast - kq * 40960 - 32768 : 8192;
    unsigned vn[4];
#pragma unroll
    for (int c = 0; c < 4; ++c) { vn[c] = vc[c] + step; opaque(vn[c]); }
    if constexpr (MODE == 1) { BAR(); vl.issue_at(10 + SEC, vlast + SEC * 4096); }
    static_for<0, 2>([&](auto hc) {
      constexpr int hf = decltype(hc)::value;
      if constexpr (hf == 1 && MODE == 0) { if (it == 4) BAR(); }
      const unsigned so = ab + vl.kstep(MODE == 2 ? 0 : 2 * it + 2 + hf) * 5120;
      static_for<0, NB * 4>([&](auto jc) {
        constexpr int j = decltype(jc)::value, m = j / 4, c = j % 4;
        if constexpr (c == 0) {
          constexpr int N = MODE == 1 ? 2 * NB + 3 : MODE == 0 ? 2 * NB - 1 : 2 * NB - 1 - (hf * NB + m);
          wait_for<N>(af[hf][m]);
        }
        const bf16x8 bv = __builtin_bit_cast(bf16x8, __builtin_shufflevector(bh[hf][2 * c], bh[hf][2 * c + 1], 0, 1, 2, 3, 4, 5, 6, 7));
        acc[m][c] = __builtin_amdgcn_mfma_f32_16x16x32_bf16(__builtin_bit_cast(bf16x8, af[hf][m]), bv, acc[m][c], 0, 0, 0);
        __builtin_amdgcn_sched_barrier(0);
        if constexpr (j < 8 && !(MODE == 2 && hf == 1))
          bh[hf ^ 1][j] = __builtin_amdgcn_ds_read_tr16_b64_v4i16((LAS s16x4*)(size_t)(hf == 0 ? vc[j >> 1] + 4096 + (j & 1) * 512 : vn[j >> 1] + (j & 1) * 512));
        if constexpr (MODE != 2 && c == 3) gload_sc1<m * 1024>(af[hf][m], wq + so, vl16);
        __builtin_amdgcn_sched_barrier(0);
      });
    });
#pragma unroll
    for (int c = 0; c < 4; ++c) vc[c] = vn[c];
  };
#pragma unroll 1
  for (int it = 0; it < 2; ++it) iter(it, IC<0>{});
  iter(2, IC<1>{});
#pragma unroll 1
  for (int it = 3; it < 5; ++it) iter(it, IC<0>{});
  iter(5, IC<2>{});
  STAMP(6);
  const unsigned red = 81920 + kq * 20480 + lane * 16;
#pragma unroll
  for (int m = 0; m < NB; ++m)
#pragma unroll
    for (int c = 0; c < 4; ++c) lds_wr128(red + ((M0 + m) * 4 + c) * 1024, __builtin_bit_cast(u32x4, acc[m][c]));
  if constexpr (NB == 2) lds_wr128(LW, (u32x4){0u, 0u, 0u, 0u});
  WAIT_LGKM0();
  BAR();
  if constexpr (NB == 3) {
    STAMP(10);
    const unsigned ol = ((lane & 15) + 16 * (lane >> 5) + 32 * ((lane >> 4) & 1)) * 16;
    f32x4 t[3][2][4];
#pragma unroll
    for (int j = 0; j < 3; ++j) {
      const int fi = kq + (j == 2 && kq >= 2 ? 4 : 4 * j);
      const unsigned rb = 81920 + lane * 16 + fi * 2048;
#pragma unroll
      for (int hf = 0; hf < 2; ++hf)
#pragma unroll
        for (int q = 0; q < 4; ++q) t[j][hf][q] = __builtin_bit_cast(f32x4, lds_rd128(rb + q * 20480 + hf * 1024));
    }
#pragma unroll
    for (int j = 0; j < 3; ++j) {
      const int fi = kq + (j == 2 && kq >= 2 ? 4 : 4 * j);
      const f32x4 a = (t[j][0][0] + t[j][0][1]) + (t[j][0][2] + t[j][0][3]), b = (t[j][1][0] + t[j][1][1]) + (t[j][1][2] + t[j][1][3]);
      auto r0 = __builtin_amdgcn_permlane32_swap(pack_bf16(a[0], a[1]), pack_bf16(b[0], b[1]), false, false);
      auto r1 = __builtin_amdgcn_permlane32_swap(pack_bf16(a[2], a[3]), pack_bf16(b[2], b[3]), false, false);
      const u32x4 v = {r0[0], r1[0], r0[1], r1[1]};
      __builtin_amdgcn_raw_buffer_store_b128(v, wsr, ol, WS_W2 + ((2 * gt + (fi & 1)) * 40 + 5 * et + (fi >> 1)) * 1024, 16);
    }
    if (SEAM == 1) BAR();
    STAMP(7);
    WAIT_VM(0);
    if (lane == 0) {
      if (__hip_atomic_fetch_add((lu32*)(size_t)LW, 1u, __ATOMIC_RELAXED, __HIP_MEMORY_SCOPE_WORKGROUP) == 3u)
        __hip_atomic_store((gu64*)(p.ws + wf_off(gt * 8 + et)), tag, RLX_AGENT);
    }
    FENCE();
    if (SEAM == 2) BAR();
  }
}

constexpr int XREG = 30;
__device__ __forceinline__ void y_load(const Params& p, int wave, int lane, int st, int gt, int rot, u64 tag) {
  const int lw = wave - 4;
  STAMP(8);
  if (lw == 0) {
    if (YDELAY > 0) __builtin_amdgcn_s_sleep(YDELAY);
    wait_flags(p.ws, wf_off((3 * gt / 2) * 8 + (lane & 15)), tag);
    lds_set(LW + 4);
  } else lds_wait(LW + 4);
  if (blockIdx.x == 0) {
    const int w = lw * 64 + lane;
    wait_flags(p.ws, w < 64 ? xf_off(w >> 2, w & 3) : wf_off(w - 64), tag);
  }
  STAMP(11);
  const char* wb = p.ws + WS_W2 + (size_t)(3 * gt * 40 + lw) * 1024;
  const char* xb = p.ws + WS_XR + (size_t)(st * 4 + lw) * 40960;
  const unsigned vo = lane * 16;
  auto kc = [&](int c) { const int t = c + rot; return (t >= 10 ? t - 10 : t) * 4096; };
  auto issue = [&](int c) {
    const char* src = wb + kc(c);
#pragma unroll
    for (int t = 0; t < 3; ++t) glds<0, true>(src + t * 40960, vo, c * 12288 + lw * 1024 + t * 4096);
  };
  issue(0); issue(1); issue(2);
  WAIT_VM(6); BAR();
  if (blockIdx.x == 0 && threadIdx.x == 256) __hip_atomic_store((gu64*)(p.ws + WS_EPOCH), tag, RLX_AGENT);
  issue(3); issue(4);
  WAIT_VM(9); BAR();
  issue(5); issue(6);
  WAIT_VM(9); BAR();
  issue(7);
  const unsigned xl = 122880 + lw * 10240;
  glds<2048, true>(xb + kc(7), vo, xl - 2048); glds<3072, true>(xb + kc(7), vo, xl - 2048);
  issue(8);
  glds4_lin_sc1(xb + kc(8), vo, xl + 2048);
  WAIT_VM(12); BAR();
  issue(9);
  glds4_lin_sc1(xb + kc(9), vo, xl + 6144);
  WAIT_VM(7); BAR();
  WAIT_VM(0); BAR();
}

__device__ __forceinline__ void y_compute(const Params& p, __amdgpu_buffer_rsrc_t wsr, int wave, int lane, int st, int gt, int rot, u64 tag, bool b4) {
  STAMP(8);
  if (wave == 0) { wait_flags(p.ws, xf_off(st, lane & 3), tag); lds_set(LW + 8); } else lds_wait(LW + 8);
  if (SEAM > 0 && b4) BAR();
  STAMP(9);
  u32x4 xf[XREG];
  const unsigned xbase = WS_XR + (st * 4 + wave) * 40 * 1024;
#pragma unroll
  for (int ks = 0; ks < XREG; ++ks) {
    const int t = (ks >> 2) + rot;
    xf[ks] = __builtin_amdgcn_raw_buffer_load_b128(wsr, lane * 16 + (ks & 3) * 1024, xbase + (t >= 10 ? t - 10 : t) * 4096, 16);
  }
  FENCE();
  STAMP(10);
  f32x16 acc[3];
#pragma unroll
  for (int t = 0; t < 3; ++t)
#pragma unroll
    for (int j = 0; j < 16; ++j) acc[t][j] = 0.f;
  {
    constexpr int L = 6;
    bf16x8 aw[L + 1], bx[4];
    const unsigned a0 = lane * 16, a1 = lane * 16 + 61440, x0 = 122880 + wave * 10240 + lane * 16;
    static_for<0, 120 + L>([&](auto ic) {
      constexpr int n = decltype(ic)::value, j = n - L;
      if constexpr (n < 120) {
        constexpr int c = n / 12, kk = (n % 12) / 3, t = n % 3;
        if constexpr (n % 12 == 0 && (c == 0 || c == 1 || c == 2 || c == 4 || c == 7 || c == 9)) {
          BAR();
          if constexpr (c == 0) STAMP(11);
        }
        if constexpr (t == 0 && n / 3 >= XREG) bx[(n / 3) & 3] = __builtin_bit_cast(bf16x8, lds_rd128(x0 + (n / 3 - XREG) * 1024));
        aw[n % (L + 1)] = __builtin_bit_cast(bf16x8, lds_rd128((c < 5 ? a0 + c * 12288 : a1 + (c - 5) * 12288) + t * 4096 + kk * 1024));
      }
      if constexpr (j >= 0) {
        if constexpr (j / 3 < XREG) acc[j % 3] = __builtin_amdgcn_mfma_f32_32x32x16_bf16(aw[j % (L + 1)], __builtin_bit_cast(bf16x8, xf[j / 3]), acc[j % 3], 0, 0, 0);
        else acc[j % 3] = __builtin_amdgcn_mfma_f32_32x32x16_bf16(aw[j % (L + 1)], bx[(j / 3) & 3], acc[j % 3], 0, 0, 0);
      }
      __builtin_amdgcn_sched_barrier(0);
    });
  }
  STAMP(12);
  const __amdgpu_buffer_rsrc_t yr = __builtin_amdgcn_make_buffer_rsrc(p.y, 0, S_ * F_ * 2, 0x00020000);
  {
    typedef unsigned u32x2 __attribute__((ext_vector_type(2)));
    const unsigned eb = wave * 8192, wa = eb + (lane & 31) * 208 + (lane >> 5) * 8;
#pragma unroll
    for (int t = 0; t < 3; ++t)
#pragma unroll
      for (int i = 0; i < 4; ++i) {
        const u32x2 v = {pack_bf16(acc[t][4 * i], acc[t][4 * i + 1]), pack_bf16(acc[t][4 * i + 2], acc[t][4 * i + 3])};
        *(LAS u32x2*)(size_t)(wa + t * 64 + i * 16) = v;
      }
    u32x4 o[6]; unsigned yo[6];
#pragma unroll
    for (int k = 0; k < 6; ++k) {
      const int u = k * 64 + lane, row = (u * 5462) >> 16, col = u - row * 12;
      o[k] = lds_rd128(eb + row * 208 + col * 16);
      yo[k] = row * 3072 + col * 16;
    }
    const unsigned ys = (st * 128 + wave * 32) * 3072 + gt * 192;
#pragma unroll
    for (int k = 0; k < 6; ++k) __builtin_amdgcn_raw_buffer_store_b128(o[k], yr, yo[k], ys, YAUX);
  }
}

__global__ void __launch_bounds__(512, 2) quip_kernel(Params p) {
  const int wave = __builtin_amdgcn_readfirstlane(threadIdx.x >> 6), lane = threadIdx.x & 63;
  const int xcd = blockIdx.x & 7, idx = blockIdx.x >> 3;
  STAMP(0); STAMPC(14);
  unsigned pf = 0;
  if (CODEKB > 0) {
    const unsigned ln = PFMAP ? (31 - idx) * 8 + wave : idx * 8 + wave;
    const GAS unsigned* code = (const GAS unsigned*)((__builtin_amdgcn_s_getpc() & ~255ull) + (ln < CODEKB * 8 ? ln : 0) * 128);
    pf = __hip_atomic_load(code, __ATOMIC_RELAXED, __HIP_MEMORY_SCOPE_WORKGROUP);
  }
  const bool isx = idx < 8;
  const int xst = 4 * (xcd >> 1) + (idx >> 1), etl = 2 * (xcd & 1) + (idx & 1);
  const int wk = idx - 8;
  const int et = wk & 7, gt = 3 * xcd + (wk >> 3);
  const int rotc = (3 * et >> 1) + ROTC * 4 * (wk >> 3);
  const int dj = xcd * 24 + wk, det = xcd, dkp = wk;
  const int rotx = ROTX * ((idx >> 1) * 2 + (idx & 1) * 5) % 10;
  XLoader xl; VLoader vl;
  if (!isx && wave < 4) d_issue(p, wave, lane, det, dkp);
  if (isx && wave >= 4) { xl.init(p, xst, etl, wave - 4, lane, rotx); xl.issue(0); xl.issue(1); }
  if (!isx) {
    vl.init(p, gt, wave & 3, rotc >= 12 ? rotc - 12 : rotc, lane);
    if (wave >= 4) {
      if (VDELAY > 0) __builtin_amdgcn_s_sleep(VDELAY);
#pragma unroll
      for (int i = 0; i < VEARLY; ++i) vl.issue(i);
    }
  }
  const __amdgpu_buffer_rsrc_t wsr = __builtin_amdgcn_make_buffer_rsrc(p.ws, 0, WS_END, 0x00020000);
  u64 tag = load_tag(p);
  const int yst = 4 * (xcd >> 1) + (idx & 3), ygt = 8 * (xcd & 1) + (idx >> 2);
  const int roty = ROTY * ((idx & 7) + (5 * (idx >> 3) >> 1)) % 10;
  if (wave < 4) {
    if (isx) {
      x_compute(p, wsr, wave, lane, xst, etl, tag);
      asm volatile("" :: "v"(pf));
    } else {
      d_compute(p, wsr, wave, lane, det, dkp);
      STAMP(2);
      make_tag(tag);
      WAIT_VM(0);
      asm volatile("" :: "v"(pf));
      BAR();
      if (wave == 0) {
        publish(p.ws, WS_DF + dj * FS, tag, lane);
        STAMP(3);
        if (!DPOLL) wait_flags(p.ws, WS_DF + (et * 24 + (lane < 24 ? lane : 0)) * FS, tag);
      }
      STAMP(4);
      w_body<2>(p, wsr, vl, wave, lane, et, gt, tag);
    }
    y_compute(p, wsr, wave, lane, yst, ygt, roty, tag, !isx);
  } else {
    if (isx) {
      u64 ts[10];
      xl.run(tag, ts);
      asm volatile("" :: "v"(pf));
      __builtin_amdgcn_s_setprio(3);
    } else {
      make_tag(tag);
      if (VMID > VEARLY) {
        WAIT_VM(0);
        if (VPAUSE > 0) __builtin_amdgcn_s_sleep(VPAUSE);
#pragma unroll 1
        for (int i = VEARLY; i < VMID; ++i) vl.issue(i);
      }
      BAR();
      STAMP(3);
#pragma unroll 1
      for (int i = VMID > VEARLY ? VMID : VEARLY; i < 10; ++i) vl.issue(i);
      STAMP(2);
      if (DPOLL && wave == 4) wait_flags(p.ws, WS_DF + (et * 24 + (lane < 24 ? lane : 0)) * FS, tag);
      WAIT_VM(0);
      asm volatile("" :: "v"(pf));
      STAMP(4);
      __builtin_amdgcn_s_setprio(3);
      w_body<3>(p, wsr, vl, wave, lane, et, gt, tag);
    }
    y_load(p, wave, lane, yst, ygt, roty, tag);
  }
  STAMP(13); STAMPC(15);
}

extern "C" void kernel_launch(void* const* d_in, const int* in_sizes, int n_in, void* d_out, int out_size, void* d_ws,
                              size_t ws_size, hipStream_t stream) {
  Params p;
  p.x = (const char*)d_in[0]; p.qw = (const char*)d_in[1]; p.sc = (const char*)d_in[2]; p.zr = (const char*)d_in[3];
  p.U = (const char*)d_in[4]; p.V = (const char*)d_in[5]; p.y = (char*)d_out; p.ws = (char*)d_ws;
  (void)hipFuncSetAttribute((const void*)quip_kernel, hipFuncAttributeMaxDynamicSharedMemorySize, LDS_BYTES);
  quip_kernel<<<256, 512, LDS_BYTES, stream>>>(p);
}
```

```cpp
#define PROBE 5
#define PROLE ((blockIdx.x >> 3) >= 8)
#include <hip/hip_runtime.h>
#include <stdint.h>

#define LAS __attribute__((address_space(3)))
#define GAS __attribute__((address_space(1)))
typedef unsigned short u16;
typedef unsigned long long u64;
typedef short s16x4 __attribute__((ext_vector_type(4)));
typedef short s16x8 __attribute__((ext_vector_type(8)));
typedef __bf16 bf16x8 __attribute__((ext_vector_type(8)));
typedef __bf16 bf16x2 __attribute__((ext_vector_type(2)));
typedef float f32x2 __attribute__((ext_vector_type(2)));
typedef float f32x4 __attribute__((ext_vector_type(4)));
typedef float f32x16 __attribute__((ext_vector_type(16)));
typedef unsigned u32x4 __attribute__((ext_vector_type(4)));
typedef int i32x4 __attribute__((ext_vector_type(4)));
typedef GAS u64 gu64;

constexpr int S_ = 2048, D_ = 640, F_ = 1536;
constexpr unsigned WS_EPOCH = 0;
constexpr unsigned FS = 128;
constexpr unsigned WS_DF = 4096;
constexpr unsigned WS_XF = WS_DF + 192 * FS;
constexpr unsigned WS_WF = WS_XF + 256 * FS;
constexpr unsigned WS_STAMP = 512 * 1024;
constexpr unsigned WS_WQ = 1u << 20;
constexpr unsigned WS_XR = WS_WQ + D_ * F_ * 2;
constexpr unsigned WS_W2 = WS_XR + S_ * D_ * 2;
constexpr unsigned WS_END = WS_W2 + D_ * F_ * 2;
constexpr int LDS_BYTES = 160 * 1024;

#define XSPREAD 1
#define ROTX 0
#define ROTC 0
#define WMAP 1
#define DPOLL 1
#define PFMAP 0
#define VDELAY 0
#define FPACK 1
#define FTOUCH 0
#define ROTY 0
#define YDELAY 12
#define YMAP 1
#define VMID 0
#define VPAUSE 0
#define DDRY 0
#define EPI 1
#define PFX 3
#define VMODE 1
#define PDELAY 0
#define XWAIT 0
#define XDELAY 0
#define FLANES 16
#define XLA 6
#define VEARLY 2
#define SEAM 2
#define POLLFIRST 1
#define PSLEEP 2
#define XPRE 5
#define YAUX 0
#define CODEKB 16

__device__ __forceinline__ unsigned xf_off(int st, int etl) { return WS_XF + (st * 4 + etl) * FS; }
__device__ __forceinline__ unsigned wf_off(int i) { return WS_WF + i * FS; }
constexpr unsigned LW = 79 * 1024;

struct Params {
  const char* x; const char* qw; const char* sc; const char* zr; const char* U; const char* V; char* y; char* ws;
  mutable unsigned long long t0, d;
};

#define RLX_AGENT __ATOMIC_RELAXED, __HIP_MEMORY_SCOPE_AGENT
#define WAIT_VM(n) asm volatile("s_waitcnt vmcnt(%0)" :: "n"(n) : "memory")
#define WAIT_LGKM0() asm volatile("s_waitcnt lgkmcnt(0)" ::: "memory")
#define BAR() do { asm volatile("" ::: "memory"); __builtin_amdgcn_s_barrier(); asm volatile("" ::: "memory"); } while (0)
#define FENCE() asm volatile("" ::: "memory")

#define STAMP(i) do { if ((i) == 0) p.t0 = __builtin_amdgcn_s_memrealtime(); if ((i) == PROBE && (PROLE)) p.d = __builtin_amdgcn_s_memrealtime() - p.t0; } while (0)
#define STAMPC(i) do {} while (0)

#define TS(v)
#define TS2(v)
template <int I> struct IC { static constexpr int value = I; };
template <int B, int E, class Fn>
__device__ __forceinline__ void static_for(Fn&& fn) {
  if constexpr (B < E) {
    if constexpr (E - B == 1) fn(IC<B>{});
    else { static_for<B, (B + E) / 2>(fn); static_for<(B + E) / 2, E>(fn); }
  }
}

template <int OFF, bool SC1>
__device__ __forceinline__ void glds(const char* sbase, unsigned voff, unsigned lds) {
  if constexpr (SC1)
    asm volatile("s_mov_b32 m0, %2\n\ts_nop 0\n\tglobal_load_lds_dwordx4 %0, %1 offset:%3 sc1" :: "v"(voff), "s"(sbase), "s"(lds), "n"(OFF) : "memory");
  else
    asm volatile("s_mov_b32 m0, %2\n\ts_nop 0\n\tglobal_load_lds_dwordx4 %0, %1 offset:%3" :: "v"(voff), "s"(sbase), "s"(lds), "n"(OFF) : "memory");
}
__device__ __forceinline__ void glds4(const char* sbase, unsigned v0, unsigned v1, unsigned v2, unsigned v3, unsigned lds) {
  asm volatile("s_mov_b32 m0, %5\n\ts_nop 0\n\tglobal_load_lds_dwordx4 %0, %4\n\tglobal_load_lds_dwordx4 %1, %4 offset:1024\n\t"
               "global_load_lds_dwordx4 %2, %4 offset:2048\n\tglobal_load_lds_dwordx4 %3, %4 offset:3072"
               :: "v"(v0), "v"(v1), "v"(v2), "v"(v3), "s"(sbase), "s"(lds) : "memory");
}
__device__ __forceinline__ void glds4_lin_sc1(const char* sbase, unsigned v, unsigned lds) {
  asm volatile("s_mov_b32 m0, %2\n\ts_nop 0\n\tglobal_load_lds_dwordx4 %0, %1 sc1\n\tglobal_load_lds_dwordx4 %0, %1 offset:1024 sc1\n\t"
               "global_load_lds_dwordx4 %0, %1 offset:2048 sc1\n\tglobal_load_lds_dwordx4 %0, %1 offset:3072 sc1"
               :: "v"(v), "s"(sbase), "s"(lds) : "memory");
}
template <int OFF>
__device__ __forceinline__ void gload_sc1(u32x4& d, const char* sbase, unsigned voff) {
  asm volatile("global_load_dwordx4 %0, %1, %2 offset:%3 sc1" : "=v"(d) : "v"(voff), "s"(sbase), "n"(OFF) : "memory");
}
template <int N>
__device__ __forceinline__ void wait_for(u32x4& d) { asm volatile("s_waitcnt vmcnt(%1)" : "+v"(d) : "n"(N) : "memory"); }
__device__ __forceinline__ void opaque(unsigned& v) { asm volatile("" : "+v"(v)); }
__device__ __forceinline__ void glds_v(const char* vaddr, unsigned lds) {
  asm volatile("s_mov_b32 m0, %1\n\ts_nop 0\n\tglobal_load_lds_dwordx4 %0, off" :: "v"(vaddr), "s"(lds) : "memory");
}

__device__ __forceinline__ unsigned pack_bf16(float a, float b) {
  f32x2 v = {a, b};
  return __builtin_bit_cast(unsigned, __builtin_convertvector(v, bf16x2));
}
__device__ __forceinline__ float bf_lo(unsigned v) { return __builtin_bit_cast(float, v << 16); }
__device__ __forceinline__ float bf_hi(unsigned v) { return __builtin_bit_cast(float, v & 0xffff0000u); }

__device__ __forceinline__ u32x4 lds_rd128(unsigned a) { return *(LAS u32x4*)(size_t)a; }
__device__ __forceinline__ void lds_wr128(unsigned a, u32x4 v) { *(LAS u32x4*)(size_t)a = v; }
__device__ __forceinline__ bf16x8 lds_tr2(unsigned a0, unsigned a1) {
  s16x4 lo = __builtin_amdgcn_ds_read_tr16_b64_v4i16((LAS s16x4*)(size_t)a0);
  s16x4 hi = __builtin_amdgcn_ds_read_tr16_b64_v4i16((LAS s16x4*)(size_t)a1);
  return __builtin_bit_cast(bf16x8, __builtin_shufflevector(lo, hi, 0, 1, 2, 3, 4, 5, 6, 7));
}

__device__ __forceinline__ u64 load_tag(const Params& p) {
  const char* e = p.ws + WS_EPOCH;
  asm volatile("" : "+s"(e));
  return *(const __attribute__((address_space(4))) u64*)e;
}
__device__ __forceinline__ u64 load_tag_v(const Params& p) { FENCE(); const u64 e = __hip_atomic_load((gu64*)(p.ws + WS_EPOCH), RLX_AGENT); FENCE(); return e; }
__device__ __forceinline__ u64 make_tag_v(u64 e) {
  asm volatile("" : "+v"(e));
  const u64 u = ((u64)(unsigned)__builtin_amdgcn_readfirstlane((unsigned)(e >> 32)) << 32) | (unsigned)__builtin_amdgcn_readfirstlane((unsigned)e);
  return u + 0x9E3779B97F4A7C15ull;
}
__device__ __forceinline__ void make_tag(u64& e) {
  asm volatile("" : "+s"(e));
  e += 0x9E3779B97F4A7C15ull;
  asm volatile("" : "+s"(e));
}

__device__ __forceinline__ void wait_flags(const char* ws, unsigned off, u64 tag) {
  gu64* f = (gu64*)(ws + off);
#pragma unroll 1
  for (unsigned spins = 0; spins < (1u << 20); ++spins) {
    u64 v = __hip_atomic_load(f, RLX_AGENT);
    if (__all(v == tag)) break;
    __builtin_amdgcn_s_sleep(PSLEEP);
  }
  FENCE();
}
typedef LAS unsigned lu32;
__device__ __forceinline__ void lds_set(unsigned a) { __hip_atomic_store((lu32*)(size_t)a, 1u, __ATOMIC_RELAXED, __HIP_MEMORY_SCOPE_WORKGROUP); FENCE(); }
__device__ __forceinline__ void lds_wait(unsigned a) {
#pragma unroll 1
  for (unsigned spins = 0; spins < (1u << 24); ++spins) {
    if (__builtin_amdgcn_readfirstlane(__hip_atomic_load((lu32*)(size_t)a, __ATOMIC_RELAXED, __HIP_MEMORY_SCOPE_WORKGROUP))) break;
    __builtin_amdgcn_s_sleep(1);
  }
  FENCE();
}
__device__ __forceinline__ void wait_flags2(__amdgpu_buffer_rsrc_t wsr, unsigned off, u64 tag) {
  const unsigned lo = (unsigned)tag, hi = (unsigned)(tag >> 32);
#pragma unroll 1
  for (unsigned spins = 0; spins < (1u << 20); ++spins) {
    FENCE();
    const u32x4 v = __builtin_amdgcn_raw_buffer_load_b128(wsr, off, 0, 16);
    if (__all(v[0] == lo && v[1] == hi && v[2] == lo && v[3] == hi)) break;
    __builtin_amdgcn_s_sleep(PSLEEP);
  }
  FENCE();
}
__device__ __forceinline__ void publish(char* ws, unsigned off, u64 tag, int lane) {
  WAIT_VM(0);
  if (lane < FLANES) __hip_atomic_store((gu64*)(ws + off + lane * 8), tag, RLX_AGENT);
  FENCE();
}

__device__ __forceinline__ unsigned dequant2(int q0, int q1, float z0, float z1, float s0, float s1) {
  unsigned qb = pack_bf16((float)q0, (float)q1);
  unsigned t = pack_bf16(bf_lo(qb) - z0, bf_hi(qb) - z1);
  return pack_bf16(bf_lo(t) * s0, bf_hi(t) * s1);
}

constexpr unsigned XSTG = 36 * 1024, XUOFF = 16 * 1024;

struct XLoader {
  unsigned xo[4], uo[5]; const char* xb; const char* ub; int rot, lw;
  __device__ __forceinline__ void init(const Params& p, int st, int etl, int lw_, int lane, int rot_) {
    lw = lw_;
    rot = rot_;
#pragma unroll
    for (int i = 0; i < 4; ++i) {
      const int row = 8 * i + (lane >> 3);
      xo[i] = row * 1280 + (((lane & 7) ^ ((row >> 1) & 7)) << 4) - i * 1024;
    }
#pragma unroll
    for (int i = 0; i < 5; ++i) {
      const int u = i * 64 + lane, row = (u * 3277) >> 16;
      uo[i] = row * 960 + u * 16 - (i & 3) * 1024;
    }
    xb = p.x + (size_t)(st * 128 + lw * 32) * 1280;
    ub = p.U + etl * 320 + lw * (16 * 1280);
  }
  __device__ __forceinline__ void issue(int i) {
    int kt = i + rot; if (kt >= 10) kt -= 10;
    const unsigned slot = (i & 3) * XSTG;
    const char* us = ub + (size_t)kt * (64 * 1280);
    glds4(us, uo[0], uo[1], uo[2], uo[3], slot + XUOFF + lw * 5120);
    glds<0, false>(us, uo[4], slot + XUOFF + lw * 5120 + 4096);
    glds4(xb + kt * 128, xo[0], xo[1], xo[2], xo[3], slot + lw * 4096);
  }
  __device__ __forceinline__ void run(u64& tag, u64 (&ts)[10]) {
    WAIT_VM(9); TS2(ts[0]); BAR();
#pragma unroll 1
    for (int i = 1; i < 9; ++i) {
      issue(i + 1); WAIT_VM(9);
      if (i == 1) TS2(ts[1]); if (i == 2) TS2(ts[2]); if (i == 4) TS2(ts[4]); if (i == 7) TS2(ts[6]);
      BAR();
      if (i == 2) TS2(ts[3]); if (i == 4) TS2(ts[5]); if (i == 7) TS2(ts[7]);
    }
    make_tag(tag);
    WAIT_VM(0); BAR();
    lds_wr128(LW, (u32x4){0u, 0u, 0u, 0u});
    WAIT_LGKM0();
    BAR();
    BAR();
  }
};

__device__ __forceinline__ void x_compute(const Params& p, __amdgpu_buffer_rsrc_t wsr, int wave, int lane, int st, int etl, u64& tag) {
  const int r = lane & 31, h = lane >> 5;
  unsigned xa0[4];
#pragma unroll
  for (int kk = 0; kk < 4; ++kk) xa0[kk] = wave * 4096 + r * 128 + (((2 * kk + h) ^ ((r >> 1) & 7)) << 4);
  const unsigned ua0 = XUOFF + (8 * h + ((lane & 15) >> 2)) * 320 + ((lane >> 4) & 1) * 32 + (lane & 3) * 8;
  f32x16 acc[5];
#pragma unroll
  for (int t = 0; t < 5; ++t)
#pragma unroll
    for (int j = 0; j < 16; ++j) acc[t][j] = 0.f;
  constexpr int L = XLA;
  bf16x8 au[10], bx[4];
#pragma unroll
  for (int t = 0; t < 5; ++t) asm volatile("" : "+v"(acc[t]));
  const u64 ev = load_tag_v(p);
  BAR();
  STAMP(1);
#pragma unroll
  for (int n = 0; n < L; ++n) {
    if (n % 5 == 0) bx[n / 5] = __builtin_bit_cast(bf16x8, lds_rd128(xa0[n / 5]));
    __builtin_amdgcn_sched_barrier(0);
    au[n] = lds_tr2(ua0 + (n / 5) * 16 * 320 + (n % 5) * 64, ua0 + (n / 5) * 16 * 320 + (n % 5) * 64 + 4 * 320);
    __builtin_amdgcn_sched_barrier(0);
  }
#pragma unroll 1
  for (int i = 0; i < 10; ++i) {
    const unsigned cur = (i & 3) * XSTG, nxt = ((i + 1) & 3) * XSTG;
    const unsigned ua = ua0 + cur, un = ua0 + nxt;
    static_for<0, 20>([&](auto jc) {
      constexpr int j = decltype(jc)::value, n = j + L;
      if constexpr (n == 20) BAR();
      constexpr int kk = (n % 20) / 5, t = n % 5;
      if constexpr (t == 0) { bx[kk] = __builtin_bit_cast(bf16x8, lds_rd128(xa0[kk] + (n < 20 ? cur : nxt))); __builtin_amdgcn_sched_barrier(0); }
      const unsigned ad = (n < 20 ? ua : un) + kk * 16 * 320 + t * 64;
      au[n % 10] = lds_tr2(ad, ad + 4 * 320);
      acc[j % 5] = __builtin_amdgcn_mfma_f32_32x32x16_bf16(au[j % 10], bx[j / 5], acc[j % 5], 0, 0, 0);
      __builtin_amdgcn_sched_barrier(0);
    });
  }
  STAMP(6);
  const unsigned ob = WS_XR + ((st * 4 + wave) * 40 + etl * 10) * 1024;
#pragma unroll
  for (int t = 0; t < 5; ++t)
#pragma unroll
    for (int s2 = 0; s2 < 2; ++s2) {
      u32x4 v;
#pragma unroll
      for (int j = 0; j < 4; ++j) v[j] = pack_bf16(acc[t][8 * s2 + 2 * j], acc[t][8 * s2 + 2 * j + 1]);
      __builtin_amdgcn_raw_buffer_store_b128(v, wsr, lane * 16, ob + (2 * t + s2) * 1024, 16);
    }
  STAMP(7);
  WAIT_LGKM0();
  WAIT_VM(0);
  tag = make_tag_v(ev);
  asm volatile("" :: "s"(tag));
  BAR();
  if (wave == 0) publish(p.ws, xf_off(st, etl), tag, lane);
}

__device__ __forceinline__ void d_issue(const Params& p, int wave, int lane, int det, int dkp) {
  const unsigned dl = (wave * 10 + 8) * 4096;
  const int k2 = wave & 1, nfr = wave < 2 ? 3 : 2;
  unsigned qo[2];
#pragma unroll
  for (int i = 0; i < 2; ++i) {
    const int row = 8 * i + (lane >> 3);
    qo[i] = row * 6144 + (((lane & 7) ^ ((row >> 1) & 7)) << 4) - i * 1024;
  }
  const char* qb = p.qw + (size_t)(80 * det + 16 * (wave >> 1)) * 6144 + (64 * dkp + 32 * k2) * 4;
  glds4(qb, qo[0], qo[1], qo[0] + (32 * 6144 - 2048), qo[1] + (32 * 6144 - 2048), dl);
  if (wave < 2) {
    glds<0, false>(qb + 64 * 6144, qo[0], dl + 4096);
    glds<1024, false>(qb + 64 * 6144, qo[1], dl + 4096);
  }
  int i = (lane >> 3) & 3; if (i >= nfr) i = 0;
  const int grp = (5 * det + (wave >> 1) + 2 * i) >> 1;
  const char* sa = (((lane >> 2) & 1) ? p.zr : p.sc) + (size_t)grp * 3072 + (64 * dkp + 32 * k2) * 2 + (lane & 3) * 16;
  glds_v(sa, dl + 6144);
}

__device__ __forceinline__ void d_compute(const Params& p, __amdgpu_buffer_rsrc_t wsr, int wave, int lane, int det, int dkp) {
  const unsigned dl = (wave * 10 + 8) * 4096;
  const int r = lane & 15, q = lane >> 4;
  const unsigned a0 = dl + r * 128 + (((2 * q) ^ ((r >> 1) & 7)) << 4), a1 = dl + r * 128 + (((2 * q + 1) ^ ((r >> 1) & 7)) << 4);
  const unsigned as = dl + 6144 + q * 16;
  const unsigned ob = WS_WQ + ((det * 48 + 2 * dkp + (wave & 1)) * 5 + (wave >> 1)) * 1024;
  const int nfr = wave < 2 ? 3 : 2;
  i32x4 q0 = __builtin_bit_cast(i32x4, lds_rd128(a0)), q1 = __builtin_bit_cast(i32x4, lds_rd128(a1));
  u32x4 s = lds_rd128(as), z = lds_rd128(as + 64);
#pragma unroll 1
  for (int i = 0; i < nfr; ++i) {
    const i32x4 nq0 = __builtin_bit_cast(i32x4, lds_rd128(a0 + i * 2048 + 2048)), nq1 = __builtin_bit_cast(i32x4, lds_rd128(a1 + i * 2048 + 2048));
    const u32x4 ns = lds_rd128(as + i * 128 + 128), nz = lds_rd128(as + i * 128 + 192);
    u32x4 o;
    o[0] = dequant2(q0[0], q0[1], bf_lo(z[0]), bf_hi(z[0]), bf_lo(s[0]), bf_hi(s[0]));
    o[1] = dequant2(q0[2], q0[3], bf_lo(z[1]), bf_hi(z[1]), bf_lo(s[1]), bf_hi(s[1]));
    o[2] = dequant2(q1[0], q1[1], bf_lo(z[2]), bf_hi(z[2]), bf_lo(s[2]), bf_hi(s[2]));
    o[3] = dequant2(q1[2], q1[3], bf_lo(z[3]), bf_hi(z[3]), bf_lo(s[3]), bf_hi(s[3]));
    __builtin_amdgcn_raw_buffer_store_b128(o, wsr, lane * 16, ob + i * 2048, 16);
    q0 = nq0; q1 = nq1; s = ns; z = nz;
  }
}

struct VLoader {
  unsigned vo[4]; const char* vb; int rot, kq;
  __device__ __forceinline__ void init(const Params& p, int gt, int kq_, int rot_, int lane) {
    kq = kq_; rot = rot_;
#pragma unroll
    for (int pc = 0; pc < 4; ++pc) {
      const int row = 8 * pc + (lane >> 3), swz = ((row >> 1) & 1) | (((row >> 3) & 1) << 1);
      vo[pc] = row * 3072 + ((((lane & 7) >> 1) ^ swz) << 5) + ((lane & 1) << 4) - pc * 1024;
    }
    vb = p.V + gt * 128 + (size_t)kq * (12 * 32 * 3072);
  }
  __device__ __forceinline__ int kstep(int i) const { const int t = i + rot; return t >= 12 ? t - 12 : t; }
  __device__ __forceinline__ void issue_at(int i, unsigned lds) const {
    glds4(vb + (size_t)kstep(i) * (32 * 3072), vo[0], vo[1], vo[2], vo[3], lds);
  }
  __device__ __forceinline__ void issue(int i) const { issue_at(i, (kq * 10 + i) * 4096); }
};

template <int NB>
__device__ __forceinline__ void w_body(const Params& p, __amdgpu_buffer_rsrc_t wsr, const VLoader& vl, int wave, int lane, int et, int gt, u64 tag) {
  constexpr int M0 = NB == 3 ? 0 : 3, SEC = NB == 3 ? 0 : 1;
  const int kq = wave & 3;
  unsigned va0[4];
  {
    const int g = lane >> 4, qq = (lane & 15) >> 2, sw = ((qq >> 1) & 1) | ((g & 1) << 1);
#pragma unroll
    for (int c = 0; c < 4; ++c) va0[c] = kq * 40960 + 128 * (8 * g + qq) + 32 * (c ^ sw) + 8 * (lane & 3);
  }
  const unsigned ab = WS_WQ + ((et * 48 + kq * 12) * 5 + M0) * 1024;
  u32x4 af[2][NB];
  f32x4 acc[NB][4];
#pragma unroll
  for (int m = 0; m < NB; ++m)
#pragma unroll
    for (int c = 0; c < 4; ++c) acc[m][c] = (f32x4){0.f, 0.f, 0.f, 0.f};
  s16x4 bh[2][8];
  const char* wq = p.ws;
  const unsigned vl16 = lane * 16;
#pragma unroll
  for (int m = 0; m < NB; ++m)
#pragma unroll
    for (int c = 0; c < 4; ++c) asm volatile("" : "+v"(acc[m][c]));
  BAR();
  STAMP(5);
  static_for<0, 2 * NB>([&](auto ic) {
    constexpr int n = decltype(ic)::value / NB, m = decltype(ic)::value % NB;
    gload_sc1<m * 1024>(af[n][m], wq + (ab + vl.kstep(n) * 5120), vl16);
  });
#pragma unroll
  for (int j = 0; j < 8; ++j) bh[0][j] = __builtin_amdgcn_ds_read_tr16_b64_v4i16((LAS s16x4*)(size_t)(va0[j >> 1] + (j & 1) * 512));
  __builtin_amdgcn_sched_barrier(0);
  unsigned vc[4] = {va0[0], va0[1], va0[2], va0[3]};
  const unsigned vlast = (kq & 1) * 40960 + (kq >> 1) * 8192;
  auto iter = [&](int it, auto modec) {
    constexpr int MODE = decltype(modec)::value;
    const unsigned step = it == 4 ? vlast - kq * 40960 - 32768 : 8192;
    unsigned vn[4];
#pragma unroll
    for (int c = 0; c < 4; ++c) { vn[c] = vc[c] + step; opaque(vn[c]); }
    if constexpr (MODE == 1) { BAR(); vl.issue_at(10 + SEC, vlast + SEC * 4096); }
    static_for<0, 2>([&](auto hc) {
      constexpr int hf = decltype(hc)::value;
      if constexpr (hf == 1 && MODE == 0) { if (it == 4) BAR(); }
      const unsigned so = ab + vl.kstep(MODE == 2 ? 0 : 2 * it + 2 + hf) * 5120;
      static_for<0, NB * 4>([&](auto jc) {
        constexpr int j = decltype(jc)::value, m = j / 4, c = j % 4;
        if constexpr (c == 0) {
          constexpr int N = MODE == 1 ? 2 * NB + 3 : MODE == 0 ? 2 * NB - 1 : 2 * NB - 1 - (hf * NB + m);
          wait_for<N>(af[hf][m]);
        }
        const bf16x8 bv = __builtin_bit_cast(bf16x8, __builtin_shufflevector(bh[hf][2 * c], bh[hf][2 * c + 1], 0, 1, 2, 3, 4, 5, 6, 7));
        acc[m][c] = __builtin_amdgcn_mfma_f32_16x16x32_bf16(__builtin_bit_cast(bf16x8, af[hf][m]), bv, acc[m][c], 0, 0, 0);
        __builtin_amdgcn_sched_barrier(0);
        if constexpr (j < 8 && !(MODE == 2 && hf == 1))
          bh[hf ^ 1][j] = __builtin_amdgcn_ds_read_tr16_b64_v4i16((LAS s16x4*)(size_t)(hf == 0 ? vc[j >> 1] + 4096 + (j & 1) * 512 : vn[j >> 1] + (j & 1) * 512));
        if constexpr (MODE != 2 && c == 3) gload_sc1<m * 1024>(af[hf][m], wq + so, vl16);
        __builtin_amdgcn_sched_barrier(0);
      });
    });
#pragma unroll
    for (int c = 0; c < 4; ++c) vc[c] = vn[c];
  };
#pragma unroll 1
  for (int it = 0; it < 2; ++it) iter(it, IC<0>{});
  iter(2, IC<1>{});
#pragma unroll 1
  for (int it = 3; it < 5; ++it) iter(it, IC<0>{});
  iter(5, IC<2>{});
  STAMP(6);
  const unsigned red = 81920 + kq * 20480 + lane * 16;
#pragma unroll
  for (int m = 0; m < NB; ++m)
#pragma unroll
    for (int c = 0; c < 4; ++c) lds_wr128(red + ((M0 + m) * 4 + c) * 1024, __builtin_bit_cast(u32x4, acc[m][c]));
  if constexpr (NB == 2) lds_wr128(LW, (u32x4){0u, 0u, 0u, 0u});
  WAIT_LGKM0();
  BAR();
  if constexpr (NB == 3) {
    STAMP(10);
    const unsigned ol = ((lane & 15) + 16 * (lane >> 5) + 32 * ((lane >> 4) & 1)) * 16;
    f32x4 t[3][2][4];
#pragma unroll
    for (int j = 0; j < 3; ++j) {
      const int fi = kq + (j == 2 && kq >= 2 ? 4 : 4 * j);
      const unsigned rb = 81920 + lane * 16 + fi * 2048;
#pragma unroll
      for (int hf = 0; hf < 2; ++hf)
#pragma unroll
        for (int q = 0; q < 4; ++q) t[j][hf][q] = __builtin_bit_cast(f32x4, lds_rd128(rb + q * 20480 + hf * 1024));
    }
#pragma unroll
    for (int j = 0; j < 3; ++j) {
      const int fi = kq + (j == 2 && kq >= 2 ? 4 : 4 * j);
      const f32x4 a = (t[j][0][0] + t[j][0][1]) + (t[j][0][2] + t[j][0][3]), b = (t[j][1][0] + t[j][1][1]) + (t[j][1][2] + t[j][1][3]);
      auto r0 = __builtin_amdgcn_permlane32_swap(pack_bf16(a[0], a[1]), pack_bf16(b[0], b[1]), false, false);
      auto r1 = __builtin_amdgcn_permlane32_swap(pack_bf16(a[2], a[3]), pack_bf16(b[2], b[3]), false, false);
      const u32x4 v = {r0[0], r1[0], r0[1], r1[1]};
      __builtin_amdgcn_raw_buffer_store_b128(v, wsr, ol, WS_W2 + ((2 * gt + (fi & 1)) * 40 + 5 * et + (fi >> 1)) * 1024, 16);
    }
    if (SEAM == 1) BAR();
    STAMP(7);
    WAIT_VM(0);
    unsigned old = 0;
    if (lane == 0) old = __hip_atomic_fetch_add((lu32*)(size_t)LW, 1u, __ATOMIC_RELAXED, __HIP_MEMORY_SCOPE_WORKGROUP);
    if (__builtin_amdgcn_readfirstlane(old) == 3) publish(p.ws, wf_off(gt * 8 + et), tag, lane);
    FENCE();
    if (SEAM == 2) BAR();
  }
}

constexpr int XREG = 30;
__device__ __forceinline__ void y_load(const Params& p, int wave, int lane, int st, int gt, int rot, u64 tag, bool isx) {
  const int lw = wave - 4;
  STAMP(8);
  if (lw == 0) {
    if (XWAIT && isx) lds_wait(LW + 12);
    else if (YDELAY > 0) __builtin_amdgcn_s_sleep(YDELAY);
    wait_flags(p.ws, wf_off((3 * gt / 2) * 8 + (lane & 15)), tag);
    lds_set(LW + 4);
  } else lds_wait(LW + 4);
  if (blockIdx.x == 0) {
    const int w = lw * 64 + lane;
    wait_flags(p.ws, w < 64 ? xf_off(w >> 2, w & 3) : wf_off(w - 64), tag);
  }
  STAMP(11);
  const char* wb = p.ws + WS_W2 + (size_t)(3 * gt * 40 + lw) * 1024;
  const char* xb = p.ws + WS_XR + (size_t)(st * 4 + lw) * 40960;
  const unsigned vo = lane * 16;
  auto kc = [&](int c) { const int t = c + rot; return (t >= 10 ? t - 10 : t) * 4096; };
  auto issue = [&](int c) {
    const char* src = wb + kc(c);
#pragma unroll
    for (int t = 0; t < 3; ++t) glds<0, true>(src + t * 40960, vo, c * 12288 + lw * 1024 + t * 4096);
  };
  issue(0); issue(1); issue(2);
  WAIT_VM(6); BAR();
  if (blockIdx.x == 0 && threadIdx.x == 256) __hip_atomic_store((gu64*)(p.ws + WS_EPOCH), tag, RLX_AGENT);
  issue(3); issue(4);
  WAIT_VM(9); BAR();
  issue(5); issue(6);
  WAIT_VM(9); BAR();
  issue(7);
  const unsigned xl = 122880 + lw * 10240;
  glds<2048, true>(xb + kc(7), vo, xl - 2048); glds<3072, true>(xb + kc(7), vo, xl - 2048);
  issue(8);
  glds4_lin_sc1(xb + kc(8), vo, xl + 2048);
  WAIT_VM(12); BAR();
  issue(9);
  glds4_lin_sc1(xb + kc(9), vo, xl + 6144);
  WAIT_VM(7); BAR();
  WAIT_VM(0); BAR();
}

__device__ __forceinline__ void y_compute(const Params& p, __amdgpu_buffer_rsrc_t wsr, int wave, int lane, int st, int gt, int rot, u64 tag, bool b4) {
  STAMP(8);
  if (wave == 0) { if (XDELAY > 0 && !b4) __builtin_amdgcn_s_sleep(XDELAY); wait_flags(p.ws, xf_off(st, lane & 3), tag); lds_set(LW + 8); } else lds_wait(LW + 8);
  if (SEAM > 0 && b4) BAR();
  STAMP(9);
  u32x4 xf[XREG];
  const unsigned xbase = WS_XR + (st * 4 + wave) * 40 * 1024;
#pragma unroll
  for (int ks = 0; ks < XREG; ++ks) {
    const int t = (ks >> 2) + rot;
    xf[ks] = __builtin_amdgcn_raw_buffer_load_b128(wsr, lane * 16 + (ks & 3) * 1024, xbase + (t >= 10 ? t - 10 : t) * 4096, 16);
  }
  FENCE();
  if (XWAIT && wave == 3) lds_set(LW + 12);
  STAMP(10);
  f32x16 acc[3];
#pragma unroll
  for (int t = 0; t < 3; ++t)
#pragma unroll
    for (int j = 0; j < 16; ++j) acc[t][j] = 0.f;
  {
    constexpr int L = 6;
    bf16x8 aw[L + 1], bx[4];
    const unsigned a0 = lane * 16, a1 = lane * 16 + 61440, x0 = 122880 + wave * 10240 + lane * 16;
    static_for<0, 120 + L>([&](auto ic) {
      constexpr int n = decltype(ic)::value, j = n - L;
      if constexpr (n < 120) {
        constexpr int c = n / 12, kk = (n % 12) / 3, t = n % 3;
        if constexpr (n % 12 == 0 && (c == 0 || c == 1 || c == 2 || c == 4 || c == 7 || c == 9)) {
          BAR();
          if constexpr (c == 0) STAMP(11);
        }
        if constexpr (t == 0 && n / 3 >= XREG) bx[(n / 3) & 3] = __builtin_bit_cast(bf16x8, lds_rd128(x0 + (n / 3 - XREG) * 1024));
        aw[n % (L + 1)] = __builtin_bit_cast(bf16x8, lds_rd128((c < 5 ? a0 + c * 12288 : a1 + (c - 5) * 12288) + t * 4096 + kk * 1024));
      }
      if constexpr (j >= 0) {
        if constexpr (j / 3 < XREG) acc[j % 3] = __builtin_amdgcn_mfma_f32_32x32x16_bf16(aw[j % (L + 1)], __builtin_bit_cast(bf16x8, xf[j / 3]), acc[j % 3], 0, 0, 0);
        else acc[j % 3] = __builtin_amdgcn_mfma_f32_32x32x16_bf16(aw[j % (L + 1)], bx[(j / 3) & 3], acc[j % 3], 0, 0, 0);
      }
      __builtin_amdgcn_sched_barrier(0);
    });
  }
  STAMP(12);
  const __amdgpu_buffer_rsrc_t yr = __builtin_amdgcn_make_buffer_rsrc(p.y, 0, S_ * F_ * 2, 0x00020000);
  {
    typedef unsigned u32x2 __attribute__((ext_vector_type(2)));
    const unsigned eb = wave * 8192, wa = eb + (lane & 31) * 208 + (lane >> 5) * 8;
#pragma unroll
    for (int t = 0; t < 3; ++t)
#pragma unroll
      for (int i = 0; i < 4; ++i) {
        const u32x2 v = {pack_bf16(acc[t][4 * i], acc[t][4 * i + 1]), pack_bf16(acc[t][4 * i + 2], acc[t][4 * i + 3])};
        *(LAS u32x2*)(size_t)(wa + t * 64 + i * 16) = v;
      }
    u32x4 o[6]; unsigned yo[6];
#pragma unroll
    for (int k = 0; k < 6; ++k) {
      const int u = k * 64 + lane, row = (u * 5462) >> 16, col = u - row * 12;
      o[k] = lds_rd128(eb + row * 208 + col * 16);
      yo[k] = row * 3072 + col * 16;
    }
    const unsigned ys = (st * 128 + wave * 32) * 3072 + gt * 192;
#pragma unroll
    for (int k = 0; k < 6; ++k) __builtin_amdgcn_raw_buffer_store_b128(o[k], yr, yo[k], ys, YAUX);
  }
}

__global__ void __launch_bounds__(512, 2) quip_kernel(Params p) {
  const int wave = __builtin_amdgcn_readfirstlane(threadIdx.x >> 6), lane = threadIdx.x & 63;
  const int xcd = blockIdx.x & 7, idx = blockIdx.x >> 3;
  STAMP(0); STAMPC(14);
  unsigned pf = 0;
  if (CODEKB > 0) {
    const unsigned ln = PFMAP ? (31 - idx) * 8 + wave : idx * 8 + wave;
    const GAS unsigned* code = (const GAS unsigned*)((__builtin_amdgcn_s_getpc() & ~255ull) + (ln < CODEKB * 8 ? ln : 0) * 128);
    pf = __hip_atomic_load(code, __ATOMIC_RELAXED, __HIP_MEMORY_SCOPE_WORKGROUP);
  }
  const bool isx = idx < 8;
  const int xst = 4 * (xcd >> 1) + (idx >> 1), etl = 2 * (xcd & 1) + (idx & 1);
  const int wk = idx - 8;
  const int et = wk & 7, gt = 3 * xcd + (wk >> 3);
  const int rotc = (3 * et >> 1) + ROTC * 4 * (wk >> 3);
  const int dj = xcd * 24 + wk, det = xcd, dkp = wk;
  const int rotx = ROTX * ((idx >> 1) * 2 + (idx & 1) * 5) % 10;
  XLoader xl; VLoader vl;
  unsigned pf2 = 0, pf3 = 0;
  if (!isx && wave < 4) d_issue(p, wave, lane, det, dkp);
  if (isx && wave >= 4) { xl.init(p, xst, etl, wave - 4, lane, rotx); xl.issue(0); xl.issue(1); }
  vl.init(p, gt, wave & 3, rotc >= 12 ? rotc - 12 : rotc, lane);
  if (!isx && wave >= 4) {
    const unsigned w96 = wk * 4 + wave - 4;
    if (PFX == 1 || PFX == 3) {
      const unsigned n = w96 * 35 + (lane < 35 ? lane : 0), lu = n - 2560, ur = (lu * 13108) >> 16;
      const char* a = n < 2560 ? p.x + (size_t)xcd * (256 * 1280) + n * 128
                               : p.U + (xcd & 1) * 640 + ((xcd >> 1) * 160 + ur) * 1280 + (lu - ur * 5) * 128;
      if (PFX == 3) { vl.issue(0); vl.issue(1); }
      FENCE();
      pf2 = __hip_atomic_load((const GAS unsigned*)a, __ATOMIC_RELAXED, __HIP_MEMORY_SCOPE_WORKGROUP);
      FENCE();
    } else if (PFX == 2) {
      const unsigned n = w96 * 54 + (lane < 54 ? lane : 0), lu = w96 * 34 + (lane < 34 ? lane : 0), ur = (lu * 13108) >> 16;
      FENCE();
      pf2 = __hip_atomic_load((const GAS unsigned*)(p.x + (size_t)(xcd >> 1) * (512 * 1280) + (n < 5120 ? n : 0) * 128), __ATOMIC_RELAXED, __HIP_MEMORY_SCOPE_WORKGROUP);
      pf3 = __hip_atomic_load((const GAS unsigned*)(p.U + (xcd & 1) * 640 + (ur < 640 ? ur : 0) * 1280 + (lu - ur * 5) * 128), __ATOMIC_RELAXED, __HIP_MEMORY_SCOPE_WORKGROUP);
      FENCE();
    }
    if (PFX != 3) { vl.issue(0); vl.issue(1); }
  }
  const __amdgpu_buffer_rsrc_t wsr = __builtin_amdgcn_make_buffer_rsrc(p.ws, 0, WS_END, 0x00020000);
  u64 tag = 0;
  const int yst = 4 * (xcd >> 1) + (idx & 3), ygt = 8 * (xcd & 1) + (idx >> 2);
  if (wave < 4) {
    if (isx) {
      x_compute(p, wsr, wave, lane, xst, etl, tag);
      asm volatile("" :: "v"(pf));
    } else {
      const u64 ev = load_tag_v(p);
      WAIT_VM(1);
      STAMP(1);
      d_compute(p, wsr, wave, lane, det, dkp);
      STAMP(2);
      WAIT_VM(0);
      tag = make_tag_v(ev);
      asm volatile("" :: "v"(pf), "s"(tag));
      BAR();
      if (wave == 0) publish(p.ws, WS_DF + dj * FS, tag, lane);
      STAMP(3);
      w_body<2>(p, wsr, vl, wave, lane, et, gt, tag);
    }
    y_compute(p, wsr, wave, lane, yst, ygt, 0, tag, !isx);
  } else {
    tag = load_tag(p);
    if (isx) {
      u64 ts[10];
      xl.run(tag, ts);
      asm volatile("" :: "v"(pf));
      __builtin_amdgcn_s_setprio(3);
    } else {
      make_tag(tag);
      BAR();
      STAMP(3);
#pragma unroll 1
      for (int i = 2; i < 10; ++i) vl.issue(i);
      if (wave == 4) wait_flags(p.ws, WS_DF + (et * 24 + (lane < 24 ? lane : 0)) * FS, tag);
      WAIT_VM(0);
      asm volatile("" :: "v"(pf), "v"(pf2), "v"(pf3));
      STAMP(4);
      __builtin_amdgcn_s_setprio(3);
      w_body<3>(p, wsr, vl, wave, lane, et, gt, tag);
    }
    y_load(p, wave, lane, yst, ygt, 0, tag, isx);
  }
  STAMP(13); STAMPC(15);
  while (__builtin_amdgcn_s_memrealtime() < p.t0 + 5000 + p.d) __builtin_amdgcn_s_sleep(1);
}

extern "C" void kernel_launch(void* const* d_in, const int* in_sizes, int n_in, void* d_out, int out_size, void* d_ws,
                              size_t ws_size, hipStream_t stream) {
  Params p;
  p.x = (const char*)d_in[0]; p.qw = (const char*)d_in[1]; p.sc = (const char*)d_in[2]; p.zr = (const char*)d_in[3];
  p.U = (const char*)d_in[4]; p.V = (const char*)d_in[5]; p.y = (char*)d_out; p.ws = (char*)d_ws; p.t0 = 0; p.d = 0;
  (void)hipFuncSetAttribute((const void*)quip_kernel, hipFuncAttributeMaxDynamicSharedMemorySize, LDS_BYTES);
  quip_kernel<<<256, 512, LDS_BYTES, stream>>>(p);
}
```

```cpp
#include <hip/hip_runtime.h>
#include <stdint.h>

#define LAS __attribute__((address_space(3)))
#define GAS __attribute__((address_space(1)))
typedef unsigned short u16;
typedef unsigned long long u64;
typedef short s16x4 __attribute__((ext_vector_type(4)));
typedef short s16x8 __attribute__((ext_vector_type(8)));
typedef __bf16 bf16x8 __attribute__((ext_vector_type(8)));
typedef __bf16 bf16x2 __attribute__((ext_vector_type(2)));
typedef float f32x2 __attribute__((ext_vector_type(2)));
typedef float f32x4 __attribute__((ext_vector_type(4)));
typedef float f32x16 __attribute__((ext_vector_type(16)));
typedef unsigned u32x4 __attribute__((ext_vector_type(4)));
typedef int i32x4 __attribute__((ext_vector_type(4)));
typedef GAS u64 gu64;

constexpr int S_ = 2048, D_ = 640, F_ = 1536;
constexpr unsigned WS_EPOCH = 0;
constexpr unsigned FS = 128;
constexpr unsigned WS_DF = 4096;
constexpr unsigned WS_XF = WS_DF + 192 * FS;
constexpr unsigned WS_WF = WS_XF + 256 * FS;
constexpr unsigned WS_STAMP = 512 * 1024;
constexpr unsigned WS_WQ = 1u << 20;
constexpr unsigned WS_XR = WS_WQ + D_ * F_ * 2;
constexpr unsigned WS_W2 = WS_XR + S_ * D_ * 2;
constexpr unsigned WS_END = WS_W2 + D_ * F_ * 2;
constexpr int LDS_BYTES = 160 * 1024;

#define XSPREAD 1
#define ROTX 0
#define ROTC 0
#define WMAP 1
#define DPOLL 1
#define PFMAP 0
#define VDELAY 0
#define FPACK 1
#define FTOUCH 0
#define ROTY 0
#define YDELAY 12
#define YMAP 1
#define VMID 0
#define VPAUSE 0
#define DDRY 0
#define EPI 1
#define PFX 3
#define VMODE 1
#define PDELAY 0
#define XWAIT 0
#define XDELAY 0
#define FLANES 16
#define EDUMP 0
#define DPRIO 0
#define WPRIO 3
#define XLA 6
#define VEARLY 2
#define SEAM 2
#define POLLFIRST 1
#define PSLEEP 2
#define XPRE 5
#define YAUX 0
#define CODEKB 16

__device__ __forceinline__ unsigned xf_off(int st, int etl) { return WS_XF + (st * 4 + etl) * FS; }
__device__ __forceinline__ unsigned wf_off(int i) { return WS_WF + i * FS; }
constexpr unsigned LW = 79 * 1024;

struct Params {
  const char* x; const char* qw; const char* sc; const char* zr; const char* U; const char* V; char* y; char* ws;
};

#define RLX_AGENT __ATOMIC_RELAXED, __HIP_MEMORY_SCOPE_AGENT
#define WAIT_VM(n) asm volatile("s_waitcnt vmcnt(%0)" :: "n"(n) : "memory")
#define WAIT_LGKM0() asm volatile("s_waitcnt lgkmcnt(0)" ::: "memory")
#define BAR() do { asm volatile("" ::: "memory"); __builtin_amdgcn_s_barrier(); asm volatile("" ::: "memory"); } while (0)
#define FENCE() asm volatile("" ::: "memory")

#define STAMP(i) do {} while (0)
#define STAMPC(i) do {} while (0)

#define TS(v)
#define TS2(v)
template <int I> struct IC { static constexpr int value = I; };
template <int B, int E, class Fn>
__device__ __forceinline__ void static_for(Fn&& fn) {
  if constexpr (B < E) {
    if constexpr (E - B == 1) fn(IC<B>{});
    else { static_for<B, (B + E) / 2>(fn); static_for<(B + E) / 2, E>(fn); }
  }
}

template <int OFF, bool SC1>
__device__ __forceinline__ void glds(const char* sbase, unsigned voff, unsigned lds) {
  if constexpr (SC1)
    asm volatile("s_mov_b32 m0, %2\n\ts_nop 0\n\tglobal_load_lds_dwordx4 %0, %1 offset:%3 sc1" :: "v"(voff), "s"(sbase), "s"(lds), "n"(OFF) : "memory");
  else
    asm volatile("s_mov_b32 m0, %2\n\ts_nop 0\n\tglobal_load_lds_dwordx4 %0, %1 offset:%3" :: "v"(voff), "s"(sbase), "s"(lds), "n"(OFF) : "memory");
}
__device__ __forceinline__ void glds4(const char* sbase, unsigned v0, unsigned v1, unsigned v2, unsigned v3, unsigned lds) {
  asm volatile("s_mov_b32 m0, %5\n\ts_nop 0\n\tglobal_load_lds_dwordx4 %0, %4\n\tglobal_load_lds_dwordx4 %1, %4 offset:1024\n\t"
               "global_load_lds_dwordx4 %2, %4 offset:2048\n\tglobal_load_lds_dwordx4 %3, %4 offset:3072"
               :: "v"(v0), "v"(v1), "v"(v2), "v"(v3), "s"(sbase), "s"(lds) : "memory");
}
__device__ __forceinline__ void glds4_lin_sc1(const char* sbase, unsigned v, unsigned lds) {
  asm volatile("s_mov_b32 m0, %2\n\ts_nop 0\n\tglobal_load_lds_dwordx4 %0, %1 sc1\n\tglobal_load_lds_dwordx4 %0, %1 offset:1024 sc1\n\t"
               "global_load_lds_dwordx4 %0, %1 offset:2048 sc1\n\tglobal_load_lds_dwordx4 %0, %1 offset:3072 sc1"
               :: "v"(v), "s"(sbase), "s"(lds) : "memory");
}
template <int OFF>
__device__ __forceinline__ void gload_sc1(u32x4& d, const char* sbase, unsigned voff) {
  asm volatile("global_load_dwordx4 %0, %1, %2 offset:%3 sc1" : "=v"(d) : "v"(voff), "s"(sbase), "n"(OFF) : "memory");
}
template <int N>
__device__ __forceinline__ void wait_for(u32x4& d) { asm volatile("s_waitcnt vmcnt(%1)" : "+v"(d) : "n"(N) : "memory"); }
__device__ __forceinline__ void opaque(unsigned& v) { asm volatile("" : "+v"(v)); }
__device__ __forceinline__ void glds_v(const char* vaddr, unsigned lds) {
  asm volatile("s_mov_b32 m0, %1\n\ts_nop 0\n\tglobal_load_lds_dwordx4 %0, off" :: "v"(vaddr), "s"(lds) : "memory");
}

__device__ __forceinline__ unsigned pack_bf16(float a, float b) {
  f32x2 v = {a, b};
  return __builtin_bit_cast(unsigned, __builtin_convertvector(v, bf16x2));
}
__device__ __forceinline__ float bf_lo(unsigned v) { return __builtin_bit_cast(float, v << 16); }
__device__ __forceinline__ float bf_hi(unsigned v) { return __builtin_bit_cast(float, v & 0xffff0000u); }

__device__ __forceinline__ u32x4 lds_rd128(unsigned a) { return *(LAS u32x4*)(size_t)a; }
__device__ __forceinline__ void lds_wr128(unsigned a, u32x4 v) { *(LAS u32x4*)(size_t)a = v; }
__device__ __forceinline__ bf16x8 lds_tr2(unsigned a0, unsigned a1) {
  s16x4 lo = __builtin_amdgcn_ds_read_tr16_b64_v4i16((LAS s16x4*)(size_t)a0);
  s16x4 hi = __builtin_amdgcn_ds_read_tr16_b64_v4i16((LAS s16x4*)(size_t)a1);
  return __builtin_bit_cast(bf16x8, __builtin_shufflevector(lo, hi, 0, 1, 2, 3, 4, 5, 6, 7));
}

__device__ __forceinline__ u64 load_tag(const Params& p) {
  const char* e = p.ws + WS_EPOCH;
  asm volatile("" : "+s"(e));
  return *(const __attribute__((address_space(4))) u64*)e;
}
__device__ __forceinline__ u64 load_tag_v(const Params& p) { FENCE(); const u64 e = __hip_atomic_load((gu64*)(p.ws + WS_EPOCH), RLX_AGENT); FENCE(); return e; }
__device__ __forceinline__ u64 make_tag_v(u64 e) {
  asm volatile("" : "+v"(e));
  const u64 u = ((u64)(unsigned)__builtin_amdgcn_readfirstlane((unsigned)(e >> 32)) << 32) | (unsigned)__builtin_amdgcn_readfirstlane((unsigned)e);
  return u + 0x9E3779B97F4A7C15ull;
}
__device__ __forceinline__ void make_tag(u64& e) {
  asm volatile("" : "+s"(e));
  e += 0x9E3779B97F4A7C15ull;
  asm volatile("" : "+s"(e));
}

__device__ __forceinline__ void wait_flags(const char* ws, unsigned off, u64 tag) {
  gu64* f = (gu64*)(ws + off);
#pragma unroll 1
  for (unsigned spins = 0; spins < (1u << 20); ++spins) {
    u64 v = __hip_atomic_load(f, RLX_AGENT);
    if (__all(v == tag)) break;
    __builtin_amdgcn_s_sleep(PSLEEP);
  }
  FENCE();
}
typedef LAS unsigned lu32;
__device__ __forceinline__ void lds_set(unsigned a) { __hip_atomic_store((lu32*)(size_t)a, 1u, __ATOMIC_RELAXED, __HIP_MEMORY_SCOPE_WORKGROUP); FENCE(); }
__device__ __forceinline__ void lds_wait(unsigned a) {
#pragma unroll 1
  for (unsigned spins = 0; spins < (1u << 24); ++spins) {
    if (__builtin_amdgcn_readfirstlane(__hip_atomic_load((lu32*)(size_t)a, __ATOMIC_RELAXED, __HIP_MEMORY_SCOPE_WORKGROUP))) break;
    __builtin_amdgcn_s_sleep(1);
  }
  FENCE();
}
__device__ __forceinline__ void wait_flags2(__amdgpu_buffer_rsrc_t wsr, unsigned off, u64 tag) {
  const unsigned lo = (unsigned)tag, hi = (unsigned)(tag >> 32);
#pragma unroll 1
  for (unsigned spins = 0; spins < (1u << 20); ++spins) {
    FENCE();
    const u32x4 v = __builtin_amdgcn_raw_buffer_load_b128(wsr, off, 0, 16);
    if (__all(v[0] == lo && v[1] == hi && v[2] == lo && v[3] == hi)) break;
    __builtin_amdgcn_s_sleep(PSLEEP);
  }
  FENCE();
}
__device__ __forceinline__ void publish(char* ws, unsigned off, u64 tag, int lane) {
  WAIT_VM(0);
  if (lane < FLANES) __hip_atomic_store((gu64*)(ws + off + lane * 8), tag, RLX_AGENT);
  FENCE();
}

__device__ __forceinline__ unsigned dequant2(int q0, int q1, float z0, float z1, float s0, float s1) {
  unsigned qb = pack_bf16((float)q0, (float)q1);
  unsigned t = pack_bf16(bf_lo(qb) - z0, bf_hi(qb) - z1);
  return pack_bf16(bf_lo(t) * s0, bf_hi(t) * s1);
}

constexpr unsigned XSTG = 36 * 1024, XUOFF = 16 * 1024;

struct XLoader {
  unsigned xo[4], uo[5]; const char* xb; const char* ub; int rot, lw;
  __device__ __forceinline__ void init(const Params& p, int st, int etl, int lw_, int lane, int rot_) {
    lw = lw_;
    rot = rot_;
#pragma unroll
    for (int i = 0; i < 4; ++i) {
      const int row = 8 * i + (lane >> 3);
      xo[i] = row * 1280 + (((lane & 7) ^ ((row >> 1) & 7)) << 4) - i * 1024;
    }
#pragma unroll
    for (int i = 0; i < 5; ++i) {
      const int u = i * 64 + lane, row = (u * 3277) >> 16;
      uo[i] = row * 960 + u * 16 - (i & 3) * 1024;
    }
    xb = p.x + (size_t)(st * 128 + lw * 32) * 1280;
    ub = p.U + etl * 320 + lw * (16 * 1280);
  }
  __device__ __forceinline__ void issue(int i) {
    int kt = i + rot; if (kt >= 10) kt -= 10;
    const unsigned slot = (i & 3) * XSTG;
    const char* us = ub + (size_t)kt * (64 * 1280);
    glds4(us, uo[0], uo[1], uo[2], uo[3], slot + XUOFF + lw * 5120);
    glds<0, false>(us, uo[4], slot + XUOFF + lw * 5120 + 4096);
    glds4(xb + kt * 128, xo[0], xo[1], xo[2], xo[3], slot + lw * 4096);
  }
  __device__ __forceinline__ void run(const Params& p, u64& tag, u64 (&ts)[10]) {
    WAIT_VM(9); TS2(ts[0]); BAR();
    tag = load_tag(p);
#pragma unroll 1
    for (int i = 1; i < 9; ++i) {
      issue(i + 1); WAIT_VM(9);
      if (i == 1) TS2(ts[1]); if (i == 2) TS2(ts[2]); if (i == 4) TS2(ts[4]); if (i == 7) TS2(ts[6]);
      BAR();
      if (i == 2) TS2(ts[3]); if (i == 4) TS2(ts[5]); if (i == 7) TS2(ts[7]);
    }
    make_tag(tag);
    WAIT_VM(0); BAR();
    lds_wr128(LW, (u32x4){0u, 0u, 0u, 0u});
    WAIT_LGKM0();
    BAR();
    BAR();
  }
};

__device__ __forceinline__ void x_compute(const Params& p, __amdgpu_buffer_rsrc_t wsr, int wave, int lane, int st, int etl, u64& tag, bool first) {
  const int r = lane & 31, h = lane >> 5;
  unsigned xa0[4];
#pragma unroll
  for (int kk = 0; kk < 4; ++kk) xa0[kk] = wave * 4096 + r * 128 + (((2 * kk + h) ^ ((r >> 1) & 7)) << 4);
  const unsigned ua0 = XUOFF + (8 * h + ((lane & 15) >> 2)) * 320 + ((lane >> 4) & 1) * 32 + (lane & 3) * 8;
  f32x16 acc[5];
#pragma unroll
  for (int t = 0; t < 5; ++t)
#pragma unroll
    for (int j = 0; j < 16; ++j) acc[t][j] = 0.f;
  constexpr int L = XLA;
  bf16x8 au[10], bx[4];
#pragma unroll
  for (int t = 0; t < 5; ++t) asm volatile("" : "+v"(acc[t]));
  u64 ev = 0;
  if (first) ev = load_tag_v(p);
  BAR();
  if (!first) ev = load_tag_v(p);
  STAMP(1);
#pragma unroll
  for (int n = 0; n < L; ++n) {
    if (n % 5 == 0) bx[n / 5] = __builtin_bit_cast(bf16x8, lds_rd128(xa0[n / 5]));
    __builtin_amdgcn_sched_barrier(0);
    au[n] = lds_tr2(ua0 + (n / 5) * 16 * 320 + (n % 5) * 64, ua0 + (n / 5) * 16 * 320 + (n % 5) * 64 + 4 * 320);
    __builtin_amdgcn_sched_barrier(0);
  }
#pragma unroll 1
  for (int i = 0; i < 10; ++i) {
    const unsigned cur = (i & 3) * XSTG, nxt = ((i + 1) & 3) * XSTG;
    const unsigned ua = ua0 + cur, un = ua0 + nxt;
    static_for<0, 20>([&](auto jc) {
      constexpr int j = decltype(jc)::value, n = j + L;
      if constexpr (n == 20) BAR();
      constexpr int kk = (n % 20) / 5, t = n % 5;
      if constexpr (t == 0) { bx[kk] = __builtin_bit_cast(bf16x8, lds_rd128(xa0[kk] + (n < 20 ? cur : nxt))); __builtin_amdgcn_sched_barrier(0); }
      const unsigned ad = (n < 20 ? ua : un) + kk * 16 * 320 + t * 64;
      au[n % 10] = lds_tr2(ad, ad + 4 * 320);
      acc[j % 5] = __builtin_amdgcn_mfma_f32_32x32x16_bf16(au[j % 10], bx[j / 5], acc[j % 5], 0, 0, 0);
      __builtin_amdgcn_sched_barrier(0);
    });
  }
  STAMP(6);
  const unsigned ob = WS_XR + ((st * 4 + wave) * 40 + etl * 10) * 1024;
#pragma unroll
  for (int t = 0; t < 5; ++t)
#pragma unroll
    for (int s2 = 0; s2 < 2; ++s2) {
      u32x4 v;
#pragma unroll
      for (int j = 0; j < 4; ++j) v[j] = pack_bf16(acc[t][8 * s2 + 2 * j], acc[t][8 * s2 + 2 * j + 1]);
      __builtin_amdgcn_raw_buffer_store_b128(v, wsr, lane * 16, ob + (2 * t + s2) * 1024, 16);
    }
  STAMP(7);
  WAIT_LGKM0();
  WAIT_VM(0);
  tag = make_tag_v(ev);
  asm volatile("" :: "s"(tag));
  BAR();
  if (wave == 0) publish(p.ws, xf_off(st, etl), tag, lane);
}

__device__ __forceinline__ void d_issue(const Params& p, int wave, int lane, int det, int dkp) {
  const unsigned dl = (wave * 10 + 8) * 4096;
  const int k2 = wave & 1, nfr = wave < 2 ? 3 : 2;
  unsigned qo[2];
#pragma unroll
  for (int i = 0; i < 2; ++i) {
    const int row = 8 * i + (lane >> 3);
    qo[i] = row * 6144 + (((lane & 7) ^ ((row >> 1) & 7)) << 4) - i * 1024;
  }
  const char* qb = p.qw + (size_t)(80 * det + 16 * (wave >> 1)) * 6144 + (64 * dkp + 32 * k2) * 4;
  glds4(qb, qo[0], qo[1], qo[0] + (32 * 6144 - 2048), qo[1] + (32 * 6144 - 2048), dl);
  if (wave < 2) {
    glds<0, false>(qb + 64 * 6144, qo[0], dl + 4096);
    glds<1024, false>(qb + 64 * 6144, qo[1], dl + 4096);
  }
  int i = (lane >> 3) & 3; if (i >= nfr) i = 0;
  const int grp = (5 * det + (wave >> 1) + 2 * i) >> 1;
  const char* sa = (((lane >> 2) & 1) ? p.zr : p.sc) + (size_t)grp * 3072 + (64 * dkp + 32 * k2) * 2 + (lane & 3) * 16;
  glds_v(sa, dl + 6144);
}

__device__ __forceinline__ void d_compute(const Params& p, __amdgpu_buffer_rsrc_t wsr, int wave, int lane, int det, int dkp) {
  const unsigned dl = (wave * 10 + 8) * 4096;
  const int r = lane & 15, q = lane >> 4;
  const unsigned a0 = dl + r * 128 + (((2 * q) ^ ((r >> 1) & 7)) << 4), a1 = dl + r * 128 + (((2 * q + 1) ^ ((r >> 1) & 7)) << 4);
  const unsigned as = dl + 6144 + q * 16;
  const unsigned ob = WS_WQ + ((det * 48 + 2 * dkp + (wave & 1)) * 5 + (wave >> 1)) * 1024;
  const int nfr = wave < 2 ? 3 : 2;
  i32x4 q0 = __builtin_bit_cast(i32x4, lds_rd128(a0)), q1 = __builtin_bit_cast(i32x4, lds_rd128(a1));
  u32x4 s = lds_rd128(as), z = lds_rd128(as + 64);
#pragma unroll 1
  for (int i = 0; i < nfr; ++i) {
    const i32x4 nq0 = __builtin_bit_cast(i32x4, lds_rd128(a0 + i * 2048 + 2048)), nq1 = __builtin_bit_cast(i32x4, lds_rd128(a1 + i * 2048 + 2048));
    const u32x4 ns = lds_rd128(as + i * 128 + 128), nz = lds_rd128(as + i * 128 + 192);
    u32x4 o;
    o[0] = dequant2(q0[0], q0[1], bf_lo(z[0]), bf_hi(z[0]), bf_lo(s[0]), bf_hi(s[0]));
    o[1] = dequant2(q0[2], q0[3], bf_lo(z[1]), bf_hi(z[1]), bf_lo(s[1]), bf_hi(s[1]));
    o[2] = dequant2(q1[0], q1[1], bf_lo(z[2]), bf_hi(z[2]), bf_lo(s[2]), bf_hi(s[2]));
    o[3] = dequant2(q1[2], q1[3], bf_lo(z[3]), bf_hi(z[3]), bf_lo(s[3]), bf_hi(s[3]));
    __builtin_amdgcn_raw_buffer_store_b128(o, wsr, lane * 16, ob + i * 2048, 16);
    q0 = nq0; q1 = nq1; s = ns; z = nz;
  }
}

struct VLoader {
  unsigned vo[4]; const char* vb; int rot, kq;
  __device__ __forceinline__ void init(const Params& p, int gt, int kq_, int rot_, int lane) {
    kq = kq_; rot = rot_;
#pragma unroll
    for (int pc = 0; pc < 4; ++pc) {
      const int row = 8 * pc + (lane >> 3), swz = ((row >> 1) & 1) | (((row >> 3) & 1) << 1);
      vo[pc] = row * 3072 + ((((lane & 7) >> 1) ^ swz) << 5) + ((lane & 1) << 4) - pc * 1024;
    }
    vb = p.V + gt * 128 + (size_t)kq * (12 * 32 * 3072);
  }
  __device__ __forceinline__ int kstep(int i) const { const int t = i + rot; return t >= 12 ? t - 12 : t; }
  __device__ __forceinline__ void issue_at(int i, unsigned lds) const {
    glds4(vb + (size_t)kstep(i) * (32 * 3072), vo[0], vo[1], vo[2], vo[3], lds);
  }
  __device__ __forceinline__ void issue(int i) const { issue_at(i, (kq * 10 + i) * 4096); }
};

template <int NB>
__device__ __forceinline__ void w_body(const Params& p, __amdgpu_buffer_rsrc_t wsr, const VLoader& vl, int wave, int lane, int et, int gt, u64 tag) {
  constexpr int M0 = NB == 3 ? 0 : 3, SEC = NB == 3 ? 0 : 1;
  const int kq = wave & 3;
  unsigned va0[4];
  {
    const int g = lane >> 4, qq = (lane & 15) >> 2, sw = ((qq >> 1) & 1) | ((g & 1) << 1);
#pragma unroll
    for (int c = 0; c < 4; ++c) va0[c] = kq * 40960 + 128 * (8 * g + qq) + 32 * (c ^ sw) + 8 * (lane & 3);
  }
  const unsigned ab = WS_WQ + ((et * 48 + kq * 12) * 5 + M0) * 1024;
  u32x4 af[2][NB];
  f32x4 acc[NB][4];
#pragma unroll
  for (int m = 0; m < NB; ++m)
#pragma unroll
    for (int c = 0; c < 4; ++c) acc[m][c] = (f32x4){0.f, 0.f, 0.f, 0.f};
  s16x4 bh[2][8];
  const char* wq = p.ws;
  const unsigned vl16 = lane * 16;
#pragma unroll
  for (int m = 0; m < NB; ++m)
#pragma unroll
    for (int c = 0; c < 4; ++c) asm volatile("" : "+v"(acc[m][c]));
  BAR();
  STAMP(5);
  static_for<0, 2 * NB>([&](auto ic) {
    constexpr int n = decltype(ic)::value / NB, m = decltype(ic)::value % NB;
    gload_sc1<m * 1024>(af[n][m], wq + (ab + vl.kstep(n) * 5120), vl16);
  });
#pragma unroll
  for (int j = 0; j < 8; ++j) bh[0][j] = __builtin_amdgcn_ds_read_tr16_b64_v4i16((LAS s16x4*)(size_t)(va0[j >> 1] + (j & 1) * 512));
  __builtin_amdgcn_sched_barrier(0);
  unsigned vc[4] = {va0[0], va0[1], va0[2], va0[3]};
  const unsigned red = 81920 + kq * 20480 + lane * 16;
  const unsigned vlast = (kq & 1) * 40960 + (kq >> 1) * 8192;
  auto iter = [&](int it, auto modec) {
    constexpr int MODE = decltype(modec)::value;
    const unsigned step = it == 4 ? vlast - kq * 40960 - 32768 : 8192;
    unsigned vn[4];
#pragma unroll
    for (int c = 0; c < 4; ++c) { vn[c] = vc[c] + step; opaque(vn[c]); }
    if constexpr (MODE == 1) { BAR(); vl.issue_at(10 + SEC, vlast + SEC * 4096); }
    static_for<0, 2>([&](auto hc) {
      constexpr int hf = decltype(hc)::value;
      if constexpr (hf == 1 && MODE == 0) { if (it == 4) BAR(); }
      const unsigned so = ab + vl.kstep(MODE == 2 ? 0 : 2 * it + 2 + hf) * 5120;
      static_for<0, NB * 4>([&](auto jc) {
        constexpr int j = decltype(jc)::value, m = j / 4, c = j % 4;
        if constexpr (c == 0) {
          constexpr int N = MODE == 1 ? 2 * NB + 3 : MODE == 0 ? 2 * NB - 1 : 2 * NB - 1 - (hf * NB + m);
          wait_for<N>(af[hf][m]);
        }
        const bf16x8 bv = __builtin_bit_cast(bf16x8, __builtin_shufflevector(bh[hf][2 * c], bh[hf][2 * c + 1], 0, 1, 2, 3, 4, 5, 6, 7));
        acc[m][c] = __builtin_amdgcn_mfma_f32_16x16x32_bf16(__builtin_bit_cast(bf16x8, af[hf][m]), bv, acc[m][c], 0, 0, 0);
        __builtin_amdgcn_sched_barrier(0);
        if constexpr (j < 8 && !(MODE == 2 && hf == 1))
          bh[hf ^ 1][j] = __builtin_amdgcn_ds_read_tr16_b64_v4i16((LAS s16x4*)(size_t)(hf == 0 ? vc[j >> 1] + 4096 + (j & 1) * 512 : vn[j >> 1] + (j & 1) * 512));
        if constexpr (EDUMP && MODE == 2 && hf == 1 && m > 0) lds_wr128(red + ((M0 + m - 1) * 4 + c) * 1024, __builtin_bit_cast(u32x4, acc[m - 1][c]));
        if constexpr (MODE != 2 && c == 3) gload_sc1<m * 1024>(af[hf][m], wq + so, vl16);
        __builtin_amdgcn_sched_barrier(0);
      });
    });
#pragma unroll
    for (int c = 0; c < 4; ++c) vc[c] = vn[c];
  };
#pragma unroll 1
  for (int it = 0; it < 2; ++it) iter(it, IC<0>{});
  iter(2, IC<1>{});
#pragma unroll 1
  for (int it = 3; it < 5; ++it) iter(it, IC<0>{});
  iter(5, IC<2>{});
  STAMP(6);
#pragma unroll
  for (int m = EDUMP ? NB - 1 : 0; m < NB; ++m)
#pragma unroll
    for (int c = 0; c < 4; ++c) lds_wr128(red + ((M0 + m) * 4 + c) * 1024, __builtin_bit_cast(u32x4, acc[m][c]));
  if constexpr (NB == 2) lds_wr128(LW, (u32x4){0u, 0u, 0u, 0u});
  WAIT_LGKM0();
  BAR();
  if constexpr (NB == 3) {
    __builtin_amdgcn_s_setprio(3);
    STAMP(10);
    const unsigned ol = ((lane & 15) + 16 * (lane >> 5) + 32 * ((lane >> 4) & 1)) * 16;
    f32x4 t[3][2][4];
#pragma unroll
    for (int j = 0; j < 3; ++j) {
      const int fi = kq + (j == 2 && kq >= 2 ? 4 : 4 * j);
      const unsigned rb = 81920 + lane * 16 + fi * 2048;
#pragma unroll
      for (int hf = 0; hf < 2; ++hf)
#pragma unroll
        for (int q = 0; q < 4; ++q) t[j][hf][q] = __builtin_bit_cast(f32x4, lds_rd128(rb + q * 20480 + hf * 1024));
    }
#pragma unroll
    for (int j = 0; j < 3; ++j) {
      const int fi = kq + (j == 2 && kq >= 2 ? 4 : 4 * j);
      const f32x4 a = (t[j][0][0] + t[j][0][1]) + (t[j][0][2] + t[j][0][3]), b = (t[j][1][0] + t[j][1][1]) + (t[j][1][2] + t[j][1][3]);
      auto r0 = __builtin_amdgcn_permlane32_swap(pack_bf16(a[0], a[1]), pack_bf16(b[0], b[1]), false, false);
      auto r1 = __builtin_amdgcn_permlane32_swap(pack_bf16(a[2], a[3]), pack_bf16(b[2], b[3]), false, false);
      const u32x4 v = {r0[0], r1[0], r0[1], r1[1]};
      __builtin_amdgcn_raw_buffer_store_b128(v, wsr, ol, WS_W2 + ((2 * gt + (fi & 1)) * 40 + 5 * et + (fi >> 1)) * 1024, 16);
    }
    if (SEAM == 1) BAR();
    STAMP(7);
    WAIT_VM(0);
    unsigned old = 0;
    if (lane == 0) old = __hip_atomic_fetch_add((lu32*)(size_t)LW, 1u, __ATOMIC_RELAXED, __HIP_MEMORY_SCOPE_WORKGROUP);
    if (__builtin_amdgcn_readfirstlane(old) == 3) publish(p.ws, wf_off(gt * 8 + et), tag, lane);
    FENCE();
    if (SEAM == 2) BAR();
  }
}

constexpr int XREG = 30;
__device__ __forceinline__ void y_load(const Params& p, int wave, int lane, int st, int gt, int rot, u64 tag, bool isx) {
  const int lw = wave - 4;
  STAMP(8);
  if (lw == 0) {
    if (XWAIT && isx) lds_wait(LW + 12);
    else if (YDELAY > 0) __builtin_amdgcn_s_sleep(YDELAY);
    wait_flags(p.ws, wf_off((3 * gt / 2) * 8 + (lane & 15)), tag);
    lds_set(LW + 4);
  } else lds_wait(LW + 4);
  if (blockIdx.x == 0) {
    const int w = lw * 64 + lane;
    wait_flags(p.ws, w < 64 ? xf_off(w >> 2, w & 3) : wf_off(w - 64), tag);
  }
  STAMP(11);
  const char* wb = p.ws + WS_W2 + (size_t)(3 * gt * 40 + lw) * 1024;
  const char* xb = p.ws + WS_XR + (size_t)(st * 4 + lw) * 40960;
  const unsigned vo = lane * 16;
  auto kc = [&](int c) { const int t = c + rot; return (t >= 10 ? t - 10 : t) * 4096; };
  auto issue = [&](int c) {
    const char* src = wb + kc(c);
#pragma unroll
    for (int t = 0; t < 3; ++t) glds<0, true>(src + t * 40960, vo, c * 12288 + lw * 1024 + t * 4096);
  };
  issue(0); issue(1); issue(2);
  WAIT_VM(6); BAR();
  if (blockIdx.x == 0 && threadIdx.x == 256) __hip_atomic_store((gu64*)(p.ws + WS_EPOCH), tag, RLX_AGENT);
  issue(3); issue(4);
  WAIT_VM(9); BAR();
  issue(5); issue(6);
  WAIT_VM(9); BAR();
  issue(7);
  const unsigned xl = 122880 + lw * 10240;
  glds<2048, true>(xb + kc(7), vo, xl - 2048); glds<3072, true>(xb + kc(7), vo, xl - 2048);
  issue(8);
  glds4_lin_sc1(xb + kc(8), vo, xl + 2048);
  WAIT_VM(12); BAR();
  issue(9);
  glds4_lin_sc1(xb + kc(9), vo, xl + 6144);
  WAIT_VM(7); BAR();
  WAIT_VM(0); BAR();
}

__device__ __forceinline__ void y_compute(const Params& p, __amdgpu_buffer_rsrc_t wsr, int wave, int lane, int st, int gt, int rot, u64 tag, bool b4) {
  STAMP(8);
  if (wave == 0) { if (XDELAY > 0 && !b4) __builtin_amdgcn_s_sleep(XDELAY); wait_flags(p.ws, xf_off(st, lane & 3), tag); lds_set(LW + 8); } else lds_wait(LW + 8);
  if (SEAM > 0 && b4) BAR();
  STAMP(9);
  u32x4 xf[XREG];
  const unsigned xbase = WS_XR + (st * 4 + wave) * 40 * 1024;
#pragma unroll
  for (int ks = 0; ks < XREG; ++ks) {
    const int t = (ks >> 2) + rot;
    xf[ks] = __builtin_amdgcn_raw_buffer_load_b128(wsr, lane * 16 + (ks & 3) * 1024, xbase + (t >= 10 ? t - 10 : t) * 4096, 16);
  }
  FENCE();
  if (XWAIT && wave == 3) lds_set(LW + 12);
  STAMP(10);
  f32x16 acc[3];
#pragma unroll
  for (int t = 0; t < 3; ++t)
#pragma unroll
    for (int j = 0; j < 16; ++j) acc[t][j] = 0.f;
  {
    constexpr int L = 6;
    bf16x8 aw[L + 1], bx[4];
    const unsigned a0 = lane * 16, a1 = lane * 16 + 61440, x0 = 122880 + wave * 10240 + lane * 16;
    static_for<0, 120 + L>([&](auto ic) {
      constexpr int n = decltype(ic)::value, j = n - L;
      if constexpr (n < 120) {
        constexpr int c = n / 12, kk = (n % 12) / 3, t = n % 3;
        if constexpr (n % 12 == 0 && (c == 0 || c == 1 || c == 2 || c == 4 || c == 7 || c == 9)) {
          BAR();
          if constexpr (c == 0) STAMP(11);
        }
        if constexpr (t == 0 && n / 3 >= XREG) bx[(n / 3) & 3] = __builtin_bit_cast(bf16x8, lds_rd128(x0 + (n / 3 - XREG) * 1024));
        aw[n % (L + 1)] = __builtin_bit_cast(bf16x8, lds_rd128((c < 5 ? a0 + c * 12288 : a1 + (c - 5) * 12288) + t * 4096 + kk * 1024));
      }
      if constexpr (j >= 0) {
        if constexpr (j / 3 < XREG) acc[j % 3] = __builtin_amdgcn_mfma_f32_32x32x16_bf16(aw[j % (L + 1)], __builtin_bit_cast(bf16x8, xf[j / 3]), acc[j % 3], 0, 0, 0);
        else acc[j % 3] = __builtin_amdgcn_mfma_f32_32x32x16_bf16(aw[j % (L + 1)], bx[(j / 3) & 3], acc[j % 3], 0, 0, 0);
      }
      __builtin_amdgcn_sched_barrier(0);
    });
  }
  STAMP(12);
  const __amdgpu_buffer_rsrc_t yr = __builtin_amdgcn_make_buffer_rsrc(p.y, 0, S_ * F_ * 2, 0x00020000);
  {
    typedef unsigned u32x2 __attribute__((ext_vector_type(2)));
    const unsigned eb = wave * 8192, wa = eb + (lane & 31) * 208 + (lane >> 5) * 8;
#pragma unroll
    for (int t = 0; t < 3; ++t)
#pragma unroll
      for (int i = 0; i < 4; ++i) {
        const u32x2 v = {pack_bf16(acc[t][4 * i], acc[t][4 * i + 1]), pack_bf16(acc[t][4 * i + 2], acc[t][4 * i + 3])};
        *(LAS u32x2*)(size_t)(wa + t * 64 + i * 16) = v;
      }
    u32x4 o[6]; unsigned yo[6];
#pragma unroll
    for (int k = 0; k < 6; ++k) {
      const int u = k * 64 + lane, row = (u * 5462) >> 16, col = u - row * 12;
      o[k] = lds_rd128(eb + row * 208 + col * 16);
      yo[k] = row * 3072 + col * 16;
    }
    const unsigned ys = (st * 128 + wave * 32) * 3072 + gt * 192;
#pragma unroll
    for (int k = 0; k < 6; ++k) __builtin_amdgcn_raw_buffer_store_b128(o[k], yr, yo[k], ys, YAUX);
  }
}

__global__ void __launch_bounds__(512, 2) quip_kernel(Params p) {
  const int wave = __builtin_amdgcn_readfirstlane(threadIdx.x >> 6), lane = threadIdx.x & 63;
  const int xcd = blockIdx.x & 7, idx = blockIdx.x >> 3;
  STAMP(0); STAMPC(14);
  unsigned pf = 0;
  if (CODEKB > 0) {
    const unsigned ln = PFMAP ? (31 - idx) * 8 + wave : idx * 8 + wave;
    const GAS unsigned* code = (const GAS unsigned*)((__builtin_amdgcn_s_getpc() & ~255ull) + (ln < CODEKB * 8 ? ln : 0) * 128);
    if (ln < CODEKB * 8) pf = __hip_atomic_load(code, __ATOMIC_RELAXED, __HIP_MEMORY_SCOPE_WORKGROUP);
  }
  const bool isx = idx < 8;
  const int xst = 4 * (xcd >> 1) + (idx >> 1), etl = 2 * (xcd & 1) + (idx & 1);
  const int wk = idx - 8;
  const int et = wk & 7, gt = 3 * xcd + (wk >> 3);
  const int rotc = (3 * et >> 1) + ROTC * 4 * (wk >> 3);
  const int dj = xcd * 24 + wk, det = xcd, dkp = wk;
  const int rotx = ROTX * ((idx >> 1) * 2 + (idx & 1) * 5) % 10;
  XLoader xl; VLoader vl;
  unsigned pf2 = 0, pf3 = 0;
  if (!isx && wave < 4) { if (DPRIO) __builtin_amdgcn_s_setprio(3); d_issue(p, wave, lane, det, dkp); }
  if (isx && wave >= 4) { xl.init(p, xst, etl, wave - 4, lane, rotx); xl.issue(0); xl.issue(1); }
  vl.init(p, gt, wave & 3, rotc >= 12 ? rotc - 12 : rotc, lane);
  if (!isx && wave >= 4) {
    const unsigned w96 = wk * 4 + wave - 4;
    if (PFX == 1 || PFX == 3) {
      const unsigned n = w96 * 35 + (lane < 35 ? lane : 0), lu = n - 2560, ur = (lu * 13108) >> 16;
      const char* a = n < 2560 ? p.x + (size_t)xcd * (256 * 1280) + n * 128
                               : p.U + (xcd & 1) * 640 + ((xcd >> 1) * 160 + ur) * 1280 + (lu - ur * 5) * 128;
      if (PFX == 3) { vl.issue(0); vl.issue(1); if (!WMAP) vl.issue(2); }
      FENCE();
      pf2 = __hip_atomic_load((const GAS unsigned*)a, __ATOMIC_RELAXED, __HIP_MEMORY_SCOPE_WORKGROUP);
      FENCE();
    } else if (PFX == 4) {
      const unsigned n = w96 * 28 + (lane < 28 ? lane : 0), lu = n - 2048, ur = (lu * 13108) >> 16;
      const char* a = n < 2048 ? p.x + (size_t)(xcd * 256 + (n >> 3)) * 1280 + (2 + (n & 7)) * 128
                               : p.U + (xcd & 1) * 640 + (128 + ur * 4 + (xcd >> 1)) * 1280 + (lu - ur * 5) * 128;
      vl.issue(0); vl.issue(1);
      FENCE();
      pf2 = __hip_atomic_load((const GAS unsigned*)a, __ATOMIC_RELAXED, __HIP_MEMORY_SCOPE_WORKGROUP);
      FENCE();
    } else if (PFX == 2) {
      const unsigned n = w96 * 54 + (lane < 54 ? lane : 0), lu = w96 * 34 + (lane < 34 ? lane : 0), ur = (lu * 13108) >> 16;
      FENCE();
      pf2 = __hip_atomic_load((const GAS unsigned*)(p.x + (size_t)(xcd >> 1) * (512 * 1280) + (n < 5120 ? n : 0) * 128), __ATOMIC_RELAXED, __HIP_MEMORY_SCOPE_WORKGROUP);
      pf3 = __hip_atomic_load((const GAS unsigned*)(p.U + (xcd & 1) * 640 + (ur < 640 ? ur : 0) * 1280 + (lu - ur * 5) * 128), __ATOMIC_RELAXED, __HIP_MEMORY_SCOPE_WORKGROUP);
      FENCE();
    }
    if (PFX < 3) { vl.issue(0); vl.issue(1); }
  }
  const __amdgpu_buffer_rsrc_t wsr = __builtin_amdgcn_make_buffer_rsrc(p.ws, 0, WS_END, 0x00020000);
  u64 tag = 0;
  const int yst = 4 * (xcd >> 1) + (idx & 3), ygt = 8 * (xcd & 1) + (idx >> 2);
  if (wave < 4) {
    if (isx) {
      x_compute(p, wsr, wave, lane, xst, etl, tag, idx == 0 && wave == 0);
      asm volatile("" :: "v"(pf));
    } else {
      WAIT_VM(0);
      const u64 ev = load_tag_v(p);
      STAMP(1);
      d_compute(p, wsr, wave, lane, det, dkp);
      STAMP(2);
      WAIT_VM(0);
      tag = make_tag_v(ev);
      asm volatile("" :: "v"(pf), "s"(tag));
      BAR();
      if (wave == 0) publish(p.ws, WS_DF + dj * FS, tag, lane);
      STAMP(3);
      if (DPRIO) __builtin_amdgcn_s_setprio(0);
      w_body<2>(p, wsr, vl, wave, lane, et, gt, tag);
    }
    y_compute(p, wsr, wave, lane, yst, ygt, 0, tag, !isx);
  } else {
    if (isx) {
      u64 ts[10];
      xl.run(p, tag, ts);
      asm volatile("" :: "v"(pf));
      __builtin_amdgcn_s_setprio(3);
    } else {
      BAR();
      tag = load_tag(p);
      STAMP(3);
#pragma unroll 1
      for (int i = WMAP ? 2 : 3; i < 10; ++i) vl.issue(i);
      make_tag(tag);
      if (wave == 4) wait_flags(p.ws, WS_DF + (et * 24 + (lane < 24 ? lane : 0)) * FS, tag);
      WAIT_VM(0);
      asm volatile("" :: "v"(pf), "v"(pf2), "v"(pf3));
      STAMP(4);
      __builtin_amdgcn_s_setprio(WPRIO);
      w_body<3>(p, wsr, vl, wave, lane, et, gt, tag);
    }
    y_load(p, wave, lane, yst, ygt, 0, tag, isx);
  }
  STAMP(13); STAMPC(15);
}

extern "C" void kernel_launch(void* const* d_in, const int* in_sizes, int n_in, void* d_out, int out_size, void* d_ws,
                              size_t ws_size, hipStream_t stream) {
  Params p;
  p.x = (const char*)d_in[0]; p.qw = (const char*)d_in[1]; p.sc = (const char*)d_in[2]; p.zr = (const char*)d_in[3];
  p.U = (const char*)d_in[4]; p.V = (const char*)d_in[5]; p.y = (char*)d_out; p.ws = (char*)d_ws;
  (void)hipFuncSetAttribute((const void*)quip_kernel, hipFuncAttributeMaxDynamicSharedMemorySize, LDS_BYTES);
  quip_kernel<<<256, 512, LDS_BYTES, stream>>>(p);
}
```

```cpp
#include <hip/hip_runtime.h>
#include <stdint.h>

#define LAS __attribute__((address_space(3)))
#define GAS __attribute__((address_space(1)))
typedef unsigned short u16;
typedef unsigned long long u64;
typedef short s16x4 __attribute__((ext_vector_type(4)));
typedef short s16x8 __attribute__((ext_vector_type(8)));
typedef __bf16 bf16x8 __attribute__((ext_vector_type(8)));
typedef __bf16 bf16x2 __attribute__((ext_vector_type(2)));
typedef float f32x2 __attribute__((ext_vector_type(2)));
typedef float f32x4 __attribute__((ext_vector_type(4)));
typedef float f32x16 __attribute__((ext_vector_type(16)));
typedef unsigned u32x4 __attribute__((ext_vector_type(4)));
typedef int i32x4 __attribute__((ext_vector_type(4)));
typedef GAS u64 gu64;

constexpr int S_ = 2048, D_ = 640, F_ = 1536;
constexpr unsigned WS_EPOCH = 0;
constexpr unsigned FS = 128;
constexpr unsigned WS_DF = 4096;
constexpr unsigned WS_XF = WS_DF + 192 * FS;
constexpr unsigned WS_WF = WS_XF + 256 * FS;
constexpr unsigned WS_STAMP = 512 * 1024;
constexpr unsigned WS_WQ = 1u << 20;
constexpr unsigned WS_XR = WS_WQ + D_ * F_ * 2;
constexpr unsigned WS_W2 = WS_XR + S_ * D_ * 2;
constexpr unsigned WS_END = WS_W2 + D_ * F_ * 2;
constexpr int LDS_BYTES = 160 * 1024;

#define XSPREAD 1
#define ROTX 0
#define ROTC 0
#define WMAP 1
#define DPOLL 1
#define PFMAP 0
#define VDELAY 0
#define FPACK 1
#define FTOUCH 0
#define ROTY 0
#define YDELAY 12
#define YMAP 1
#define VMID 0
#define VPAUSE 0
#define DDRY 0
#define EPI 2
#define PFX 3
#define VMODE 1
#define PDELAY 0
#define XWAIT 0
#define XDELAY 0
#define FLANES 16
#define EDUMP 1
#define DPRIO 0
#define WPRIO 0
#define KS0 16
#define XSPLIT 0
#define XSLEEP 0
#define XREGS 30
#define XLA 6
#define VEARLY 2
#define SEAM 2
#define POLLFIRST 1
#define PSLEEP 2
#define XPRE 5
#define YAUX 19
#define CODEKB 16

__device__ __forceinline__ unsigned xf_off(int st, int etl) { return WS_XF + (st * 4 + etl) * FS; }
__device__ __forceinline__ unsigned wf_off(int i) { return WS_WF + i * FS; }
constexpr unsigned LW = 79 * 1024;

struct Params {
  const char* x; const char* qw; const char* sc; const char* zr; const char* U; const char* V; char* y; char* ws;
};

#define RLX_AGENT __ATOMIC_RELAXED, __HIP_MEMORY_SCOPE_AGENT
#define WAIT_VM(n) asm volatile("s_waitcnt vmcnt(%0)" :: "n"(n) : "memory")
#define WAIT_LGKM0() asm volatile("s_waitcnt lgkmcnt(0)" ::: "memory")
#define BAR() do { asm volatile("" ::: "memory"); __builtin_amdgcn_s_barrier(); asm volatile("" ::: "memory"); } while (0)
#define FENCE() asm volatile("" ::: "memory")

#define STAMP(i) do {} while (0)
#define STAMPC(i) do {} while (0)

#define TS(v)
#define TS2(v)
template <int I> struct IC { static constexpr int value = I; };
template <int B, int E, class Fn>
__device__ __forceinline__ void static_for(Fn&& fn) {
  if constexpr (B < E) {
    if constexpr (E - B == 1) fn(IC<B>{});
    else { static_for<B, (B + E) / 2>(fn); static_for<(B + E) / 2, E>(fn); }
  }
}

template <int OFF, bool SC1>
__device__ __forceinline__ void glds(const char* sbase, unsigned voff, unsigned lds) {
  if constexpr (SC1)
    asm volatile("s_mov_b32 m0, %2\n\ts_nop 0\n\tglobal_load_lds_dwordx4 %0, %1 offset:%3 sc1" :: "v"(voff), "s"(sbase), "s"(lds), "n"(OFF) : "memory");
  else
    asm volatile("s_mov_b32 m0, %2\n\ts_nop 0\n\tglobal_load_lds_dwordx4 %0, %1 offset:%3" :: "v"(voff), "s"(sbase), "s"(lds), "n"(OFF) : "memory");
}
__device__ __forceinline__ void glds4(const char* sbase, unsigned v0, unsigned v1, unsigned v2, unsigned v3, unsigned lds) {
  asm volatile("s_mov_b32 m0, %5\n\ts_nop 0\n\tglobal_load_lds_dwordx4 %0, %4\n\tglobal_load_lds_dwordx4 %1, %4 offset:1024\n\t"
               "global_load_lds_dwordx4 %2, %4 offset:2048\n\tglobal_load_lds_dwordx4 %3, %4 offset:3072"
               :: "v"(v0), "v"(v1), "v"(v2), "v"(v3), "s"(sbase), "s"(lds) : "memory");
}
__device__ __forceinline__ void glds4_lin_sc1(const char* sbase, unsigned v, unsigned lds) {
  asm volatile("s_mov_b32 m0, %2\n\ts_nop 0\n\tglobal_load_lds_dwordx4 %0, %1 sc1\n\tglobal_load_lds_dwordx4 %0, %1 offset:1024 sc1\n\t"
               "global_load_lds_dwordx4 %0, %1 offset:2048 sc1\n\tglobal_load_lds_dwordx4 %0, %1 offset:3072 sc1"
               :: "v"(v), "s"(sbase), "s"(lds) : "memory");
}
template <int OFF>
__device__ __forceinline__ void gload_sc1(u32x4& d, const char* sbase, unsigned voff) {
  asm volatile("global_load_dwordx4 %0, %1, %2 offset:%3 sc1" : "=v"(d) : "v"(voff), "s"(sbase), "n"(OFF) : "memory");
}
template <int N>
__device__ __forceinline__ void wait_for(u32x4& d) { asm volatile("s_waitcnt vmcnt(%1)" : "+v"(d) : "n"(N) : "memory"); }
__device__ __forceinline__ void opaque(unsigned& v) { asm volatile("" : "+v"(v)); }
__device__ __forceinline__ void glds_v(const char* vaddr, unsigned lds) {
  asm volatile("s_mov_b32 m0, %1\n\ts_nop 0\n\tglobal_load_lds_dwordx4 %0, off" :: "v"(vaddr), "s"(lds) : "memory");
}

__device__ __forceinline__ unsigned pack_bf16(float a, float b) {
  f32x2 v = {a, b};
  return __builtin_bit_cast(unsigned, __builtin_convertvector(v, bf16x2));
}
__device__ __forceinline__ float bf_lo(unsigned v) { return __builtin_bit_cast(float, v << 16); }
__device__ __forceinline__ float bf_hi(unsigned v) { return __builtin_bit_cast(float, v & 0xffff0000u); }

__device__ __forceinline__ u32x4 lds_rd128(unsigned a) { return *(LAS u32x4*)(size_t)a; }
__device__ __forceinline__ void lds_wr128(unsigned a, u32x4 v) { *(LAS u32x4*)(size_t)a = v; }
__device__ __forceinline__ bf16x8 lds_tr2(unsigned a0, unsigned a1) {
  s16x4 lo = __builtin_amdgcn_ds_read_tr16_b64_v4i16((LAS s16x4*)(size_t)a0);
  s16x4 hi = __builtin_amdgcn_ds_read_tr16_b64_v4i16((LAS s16x4*)(size_t)a1);
  return __builtin_bit_cast(bf16x8, __builtin_shufflevector(lo, hi, 0, 1, 2, 3, 4, 5, 6, 7));
}

__device__ __forceinline__ u64 load_tag(const Params& p) {
  const char* e = p.ws + WS_EPOCH;
  asm volatile("" : "+s"(e));
  return *(const __attribute__((address_space(4))) u64*)e;
}
__device__ __forceinline__ u64 load_tag_v(const Params& p) { FENCE(); const u64 e = __hip_atomic_load((gu64*)(p.ws + WS_EPOCH), RLX_AGENT); FENCE(); return e; }
__device__ __forceinline__ u64 make_tag_v(u64 e) {
  asm volatile("" : "+v"(e));
  const u64 u = ((u64)(unsigned)__builtin_amdgcn_readfirstlane((unsigned)(e >> 32)) << 32) | (unsigned)__builtin_amdgcn_readfirstlane((unsigned)e);
  return u + 0x9E3779B97F4A7C15ull;
}
__device__ __forceinline__ void make_tag(u64& e) {
  asm volatile("" : "+s"(e));
  e += 0x9E3779B97F4A7C15ull;
  asm volatile("" : "+s"(e));
}

__device__ __forceinline__ void wait_flags(const char* ws, unsigned off, u64 tag) {
  gu64* f = (gu64*)(ws + off);
#pragma unroll 1
  for (unsigned spins = 0; spins < (1u << 20); ++spins) {
    u64 v = __hip_atomic_load(f, RLX_AGENT);
    if (__all(v == tag)) break;
    __builtin_amdgcn_s_sleep(PSLEEP);
  }
  FENCE();
}
typedef LAS unsigned lu32;
__device__ __forceinline__ void lds_set(unsigned a) { __hip_atomic_store((lu32*)(size_t)a, 1u, __ATOMIC_RELAXED, __HIP_MEMORY_SCOPE_WORKGROUP); FENCE(); }
__device__ __forceinline__ void lds_wait(unsigned a) {
#pragma unroll 1
  for (unsigned spins = 0; spins < (1u << 24); ++spins) {
    if (__builtin_amdgcn_readfirstlane(__hip_atomic_load((lu32*)(size_t)a, __ATOMIC_RELAXED, __HIP_MEMORY_SCOPE_WORKGROUP))) break;
    __builtin_amdgcn_s_sleep(1);
  }
  FENCE();
}
__device__ __forceinline__ void wait_flags2(__amdgpu_buffer_rsrc_t wsr, unsigned off, u64 tag) {
  const unsigned lo = (unsigned)tag, hi = (unsigned)(tag >> 32);
#pragma unroll 1
  for (unsigned spins = 0; spins < (1u << 20); ++spins) {
    FENCE();
    const u32x4 v = __builtin_amdgcn_raw_buffer_load_b128(wsr, off, 0, 16);
    if (__all(v[0] == lo && v[1] == hi && v[2] == lo && v[3] == hi)) break;
    __builtin_amdgcn_s_sleep(PSLEEP);
  }
  FENCE();
}
__device__ __forceinline__ void publish(char* ws, unsigned off, u64 tag, int lane) {
  WAIT_VM(0);
  if (lane < FLANES) __hip_atomic_store((gu64*)(ws + off + lane * 8), tag, RLX_AGENT);
  FENCE();
}

__device__ __forceinline__ unsigned dequant2(int q0, int q1, float z0, float z1, float s0, float s1) {
  unsigned qb = pack_bf16((float)q0, (float)q1);
  unsigned t = pack_bf16(bf_lo(qb) - z0, bf_hi(qb) - z1);
  return pack_bf16(bf_lo(t) * s0, bf_hi(t) * s1);
}

constexpr unsigned XSTG = 36 * 1024, XUOFF = 16 * 1024;

struct XLoader {
  unsigned xo[4], uo[5]; const char* xb; const char* ub; int rot, lw;
  __device__ __forceinline__ void init(const Params& p, int st, int etl, int lw_, int lane, int rot_) {
    lw = lw_;
    rot = rot_;
#pragma unroll
    for (int i = 0; i < 4; ++i) {
      const int row = 8 * i + (lane >> 3);
      xo[i] = row * 1280 + (((lane & 7) ^ ((row >> 1) & 7)) << 4) - i * 1024;
    }
#pragma unroll
    for (int i = 0; i < 5; ++i) {
      const int u = i * 64 + lane, row = (u * 3277) >> 16;
      uo[i] = row * 960 + u * 16 - (i & 3) * 1024;
    }
    xb = p.x + (size_t)(st * 128 + lw * 32) * 1280;
    ub = p.U + etl * 320 + lw * (16 * 1280);
  }
  __device__ __forceinline__ void issue(int i) {
    int kt = i + rot; if (kt >= 10) kt -= 10;
    const unsigned slot = (i & 3) * XSTG;
    const char* us = ub + (size_t)kt * (64 * 1280);
    glds4(us, uo[0], uo[1], uo[2], uo[3], slot + XUOFF + lw * 5120);
    glds<0, false>(us, uo[4], slot + XUOFF + lw * 5120 + 4096);
    glds4(xb + kt * 128, xo[0], xo[1], xo[2], xo[3], slot + lw * 4096);
  }
  __device__ __forceinline__ void run(const Params& p, u64& tag, u64 (&ts)[10]) {
    WAIT_VM(9); TS2(ts[0]); BAR();
    tag = load_tag(p);
#pragma unroll 1
    for (int i = 1; i < 9; ++i) {
      issue(i + 1); WAIT_VM(9);
      if (i == 1) TS2(ts[1]); if (i == 2) TS2(ts[2]); if (i == 4) TS2(ts[4]); if (i == 7) TS2(ts[6]);
      BAR();
      if (i == 2) TS2(ts[3]); if (i == 4) TS2(ts[5]); if (i == 7) TS2(ts[7]);
    }
    make_tag(tag);
    WAIT_VM(0); BAR();
    lds_wr128(LW, (u32x4){0u, 0u, 0u, 0u});
    WAIT_LGKM0();
    BAR();
    BAR();
  }
};

__device__ __forceinline__ void x_compute(const Params& p, __amdgpu_buffer_rsrc_t wsr, int wave, int lane, int st, int etl, u64& tag, bool first) {
  const int r = lane & 31, h = lane >> 5;
  unsigned xa0[4];
#pragma unroll
  for (int kk = 0; kk < 4; ++kk) xa0[kk] = wave * 4096 + r * 128 + (((2 * kk + h) ^ ((r >> 1) & 7)) << 4);
  const unsigned ua0 = XUOFF + (8 * h + ((lane & 15) >> 2)) * 320 + ((lane >> 4) & 1) * 32 + (lane & 3) * 8;
  f32x16 acc[5];
#pragma unroll
  for (int t = 0; t < 5; ++t)
#pragma unroll
    for (int j = 0; j < 16; ++j) acc[t][j] = 0.f;
  constexpr int L = XLA;
  bf16x8 au[10], bx[4];
#pragma unroll
  for (int t = 0; t < 5; ++t) asm volatile("" : "+v"(acc[t]));
  u64 ev = 0;
  if (first) ev = load_tag_v(p);
  BAR();
  if (!first) ev = load_tag_v(p);
  STAMP(1);
#pragma unroll
  for (int n = 0; n < L; ++n) {
    if (n % 5 == 0) bx[n / 5] = __builtin_bit_cast(bf16x8, lds_rd128(xa0[n / 5]));
    __builtin_amdgcn_sched_barrier(0);
    au[n] = lds_tr2(ua0 + (n / 5) * 16 * 320 + (n % 5) * 64, ua0 + (n / 5) * 16 * 320 + (n % 5) * 64 + 4 * 320);
    __builtin_amdgcn_sched_barrier(0);
  }
#pragma unroll 1
  for (int i = 0; i < 10; ++i) {
    const unsigned cur = (i & 3) * XSTG, nxt = ((i + 1) & 3) * XSTG;
    const unsigned ua = ua0 + cur, un = ua0 + nxt;
    static_for<0, 20>([&](auto jc) {
      constexpr int j = decltype(jc)::value, n = j + L;
      if constexpr (n == 20) BAR();
      constexpr int kk = (n % 20) / 5, t = n % 5;
      if constexpr (t == 0) { bx[kk] = __builtin_bit_cast(bf16x8, lds_rd128(xa0[kk] + (n < 20 ? cur : nxt))); __builtin_amdgcn_sched_barrier(0); }
      const unsigned ad = (n < 20 ? ua : un) + kk * 16 * 320 + t * 64;
      au[n % 10] = lds_tr2(ad, ad + 4 * 320);
      acc[j % 5] = __builtin_amdgcn_mfma_f32_32x32x16_bf16(au[j % 10], bx[j / 5], acc[j % 5], 0, 0, 0);
      __builtin_amdgcn_sched_barrier(0);
    });
  }
  STAMP(6);
  const unsigned ob = WS_XR + ((st * 4 + wave) * 40 + etl * 10) * 1024;
#pragma unroll
  for (int t = 0; t < 5; ++t)
#pragma unroll
    for (int s2 = 0; s2 < 2; ++s2) {
      u32x4 v;
#pragma unroll
      for (int j = 0; j < 4; ++j) v[j] = pack_bf16(acc[t][8 * s2 + 2 * j], acc[t][8 * s2 + 2 * j + 1]);
      __builtin_amdgcn_raw_buffer_store_b128(v, wsr, lane * 16, ob + (2 * t + s2) * 1024, 16);
    }
  STAMP(7);
  WAIT_LGKM0();
  WAIT_VM(0);
  tag = make_tag_v(ev);
  asm volatile("" :: "s"(tag));
  BAR();
  if (wave == 0) publish(p.ws, xf_off(st, etl), tag, lane);
}

__device__ __forceinline__ void d_issue(const Params& p, int wave, int lane, int det, int dkp) {
  const unsigned dl = (wave * 10 + 8) * 4096;
  const int k2 = wave & 1, nfr = wave < 2 ? 3 : 2;
  unsigned qo[2];
#pragma unroll
  for (int i = 0; i < 2; ++i) {
    const int row = 8 * i + (lane >> 3);
    qo[i] = row * 6144 + (((lane & 7) ^ ((row >> 1) & 7)) << 4) - i * 1024;
  }
  const char* qb = p.qw + (size_t)(80 * det + 16 * (wave >> 1)) * 6144 + (64 * dkp + 32 * k2) * 4;
  glds4(qb, qo[0], qo[1], qo[0] + (32 * 6144 - 2048), qo[1] + (32 * 6144 - 2048), dl);
  if (wave < 2) {
    glds<0, false>(qb + 64 * 6144, qo[0], dl + 4096);
    glds<1024, false>(qb + 64 * 6144, qo[1], dl + 4096);
  }
  int i = (lane >> 3) & 3; if (i >= nfr) i = 0;
  const int grp = (5 * det + (wave >> 1) + 2 * i) >> 1;
  const char* sa = (((lane >> 2) & 1) ? p.zr : p.sc) + (size_t)grp * 3072 + (64 * dkp + 32 * k2) * 2 + (lane & 3) * 16;
  glds_v(sa, dl + 6144);
}

__device__ __forceinline__ void d_compute(const Params& p, __amdgpu_buffer_rsrc_t wsr, int wave, int lane, int det, int dkp) {
  const unsigned dl = (wave * 10 + 8) * 4096;
  const int r = lane & 15, q = lane >> 4;
  const unsigned a0 = dl + r * 128 + (((2 * q) ^ ((r >> 1) & 7)) << 4), a1 = dl + r * 128 + (((2 * q + 1) ^ ((r >> 1) & 7)) << 4);
  const unsigned as = dl + 6144 + q * 16;
  const unsigned ob = WS_WQ + ((det * 48 + 2 * dkp + (wave & 1)) * 5 + (wave >> 1)) * 1024;
  const int nfr = wave < 2 ? 3 : 2;
  i32x4 q0 = __builtin_bit_cast(i32x4, lds_rd128(a0)), q1 = __builtin_bit_cast(i32x4, lds_rd128(a1));
  u32x4 s = lds_rd128(as), z = lds_rd128(as + 64);
#pragma unroll 1
  for (int i = 0; i < nfr; ++i) {
    const i32x4 nq0 = __builtin_bit_cast(i32x4, lds_rd128(a0 + i * 2048 + 2048)), nq1 = __builtin_bit_cast(i32x4, lds_rd128(a1 + i * 2048 + 2048));
    const u32x4 ns = lds_rd128(as + i * 128 + 128), nz = lds_rd128(as + i * 128 + 192);
    u32x4 o;
    o[0] = dequant2(q0[0], q0[1], bf_lo(z[0]), bf_hi(z[0]), bf_lo(s[0]), bf_hi(s[0]));
    o[1] = dequant2(q0[2], q0[3], bf_lo(z[1]), bf_hi(z[1]), bf_lo(s[1]), bf_hi(s[1]));
    o[2] = dequant2(q1[0], q1[1], bf_lo(z[2]), bf_hi(z[2]), bf_lo(s[2]), bf_hi(s[2]));
    o[3] = dequant2(q1[2], q1[3], bf_lo(z[3]), bf_hi(z[3]), bf_lo(s[3]), bf_hi(s[3]));
    __builtin_amdgcn_raw_buffer_store_b128(o, wsr, lane * 16, ob + i * 2048, 16);
    q0 = nq0; q1 = nq1; s = ns; z = nz;
  }
}

struct VLoader {
  unsigned vo[4]; const char* vb; int rot, kq;
  __device__ __forceinline__ void init(const Params& p, int gt, int kq_, int rot_, int lane) {
    kq = kq_; rot = rot_;
#pragma unroll
    for (int pc = 0; pc < 4; ++pc) {
      const int row = 8 * pc + (lane >> 3), swz = ((row >> 1) & 1) | (((row >> 3) & 1) << 1);
      vo[pc] = row * 3072 + ((((lane & 7) >> 1) ^ swz) << 5) + ((lane & 1) << 4) - pc * 1024;
    }
    vb = p.V + gt * 128 + (size_t)kq * (12 * 32 * 3072);
  }
  __device__ __forceinline__ int kstep(int i) const { const int t = i + rot; return t >= 12 ? t - 12 : t; }
  __device__ __forceinline__ void issue_at(int i, unsigned lds) const {
    glds4(vb + (size_t)kstep(i) * (32 * 3072), vo[0], vo[1], vo[2], vo[3], lds);
  }
  __device__ __forceinline__ void issue(int i) const { issue_at(i, (kq * 10 + i) * 4096); }
};

template <int NB>
__device__ __forceinline__ void w_body(const Params& p, __amdgpu_buffer_rsrc_t wsr, const VLoader& vl, int wave, int lane, int et, int gt, u64 tag) {
  constexpr int M0 = NB == 3 ? 0 : 3, SEC = NB == 3 ? 0 : 1;
  const int kq = wave & 3;
  unsigned va0[4];
  {
    const int g = lane >> 4, qq = (lane & 15) >> 2, sw = ((qq >> 1) & 1) | ((g & 1) << 1);
#pragma unroll
    for (int c = 0; c < 4; ++c) va0[c] = kq * 40960 + 128 * (8 * g + qq) + 32 * (c ^ sw) + 8 * (lane & 3);
  }
  const unsigned ab = WS_WQ + ((et * 48 + kq * 12) * 5 + M0) * 1024;
  u32x4 af[2][NB];
  f32x4 acc[NB][4];
#pragma unroll
  for (int m = 0; m < NB; ++m)
#pragma unroll
    for (int c = 0; c < 4; ++c) acc[m][c] = (f32x4){0.f, 0.f, 0.f, 0.f};
  s16x4 bh[2][8];
  const char* wq = p.ws;
  const unsigned vl16 = lane * 16;
#pragma unroll
  for (int m = 0; m < NB; ++m)
#pragma unroll
    for (int c = 0; c < 4; ++c) asm volatile("" : "+v"(acc[m][c]));
  BAR();
  STAMP(5);
  static_for<0, 2 * NB>([&](auto ic) {
    constexpr int n = decltype(ic)::value / NB, m = decltype(ic)::value % NB;
    gload_sc1<m * 1024>(af[n][m], wq + (ab + vl.kstep(n) * 5120), vl16);
  });
#pragma unroll
  for (int j = 0; j < 8; ++j) bh[0][j] = __builtin_amdgcn_ds_read_tr16_b64_v4i16((LAS s16x4*)(size_t)(va0[j >> 1] + (j & 1) * 512));
  __builtin_amdgcn_sched_barrier(0);
  unsigned vc[4] = {va0[0], va0[1], va0[2], va0[3]};
  const unsigned red = 81920 + kq * 20480 + lane * 16;
  const unsigned vlast = (kq & 1) * 40960 + (kq >> 1) * 8192;
  auto iter = [&](int it, auto modec) {
    constexpr int MODE = decltype(modec)::value;
    const unsigned step = it == 4 ? vlast - kq * 40960 - 32768 : 8192;
    unsigned vn[4];
#pragma unroll
    for (int c = 0; c < 4; ++c) { vn[c] = vc[c] + step; opaque(vn[c]); }
    if constexpr (MODE == 1) { BAR(); vl.issue_at(10 + SEC, vlast + SEC * 4096); }
    static_for<0, 2>([&](auto hc) {
      constexpr int hf = decltype(hc)::value;
      if constexpr (hf == 1 && MODE == 0) { if (it == 4) BAR(); }
      const unsigned so = ab + vl.kstep(MODE == 2 ? 0 : 2 * it + 2 + hf) * 5120;
      static_for<0, NB * 4>([&](auto jc) {
        constexpr int j = decltype(jc)::value, m = j / 4, c = j % 4;
        if constexpr (c == 0) {
          constexpr int N = MODE == 1 ? 2 * NB + 3 : MODE == 0 ? 2 * NB - 1 : 2 * NB - 1 - (hf * NB + m);
          wait_for<N>(af[hf][m]);
        }
        const bf16x8 bv = __builtin_bit_cast(bf16x8, __builtin_shufflevector(bh[hf][2 * c], bh[hf][2 * c + 1], 0, 1, 2, 3, 4, 5, 6, 7));
        acc[m][c] = __builtin_amdgcn_mfma_f32_16x16x32_bf16(__builtin_bit_cast(bf16x8, af[hf][m]), bv, acc[m][c], 0, 0, 0);
        __builtin_amdgcn_sched_barrier(0);
        if constexpr (j < 8 && !(MODE == 2 && hf == 1))
          bh[hf ^ 1][j] = __builtin_amdgcn_ds_read_tr16_b64_v4i16((LAS s16x4*)(size_t)(hf == 0 ? vc[j >> 1] + 4096 + (j & 1) * 512 : vn[j >> 1] + (j & 1) * 512));
        if constexpr (EDUMP && MODE == 2 && hf == 1 && m > 0) lds_wr128(red + ((M0 + m - 1) * 4 + c) * 1024, __builtin_bit_cast(u32x4, acc[m - 1][c]));
        if constexpr (MODE != 2 && c == 3) gload_sc1<m * 1024>(af[hf][m], wq + so, vl16);
        __builtin_amdgcn_sched_barrier(0);
      });
    });
#pragma unroll
    for (int c = 0; c < 4; ++c) vc[c] = vn[c];
  };
#pragma unroll 1
  for (int it = 0; it < 2; ++it) iter(it, IC<0>{});
  iter(2, IC<1>{});
#pragma unroll 1
  for (int it = 3; it < 5; ++it) iter(it, IC<0>{});
  iter(5, IC<2>{});
  STAMP(6);
#pragma unroll
  for (int m = EDUMP ? NB - 1 : 0; m < NB; ++m)
#pragma unroll
    for (int c = 0; c < 4; ++c) lds_wr128(red + ((M0 + m) * 4 + c) * 1024, __builtin_bit_cast(u32x4, acc[m][c]));
  if constexpr (NB == 2) lds_wr128(LW, (u32x4){0u, 0u, 0u, 0u});
  WAIT_LGKM0();
  BAR();
  if constexpr (NB == 3) {
    __builtin_amdgcn_s_setprio(3);
    STAMP(10);
    const unsigned ol = ((lane & 15) + 16 * (lane >> 5) + 32 * ((lane >> 4) & 1)) * 16;
    f32x4 t[3][2][4];
#pragma unroll
    for (int j = 0; j < 3; ++j) {
      const int fi = kq + (j == 2 && kq >= 2 ? 4 : 4 * j);
      const unsigned rb = 81920 + lane * 16 + fi * 2048;
#pragma unroll
      for (int hf = 0; hf < 2; ++hf)
#pragma unroll
        for (int q = 0; q < 4; ++q) t[j][hf][q] = __builtin_bit_cast(f32x4, lds_rd128(rb + q * 20480 + hf * 1024));
    }
#pragma unroll
    for (int j = 0; j < 3; ++j) {
      const int fi = kq + (j == 2 && kq >= 2 ? 4 : 4 * j);
      const f32x4 a = (t[j][0][0] + t[j][0][1]) + (t[j][0][2] + t[j][0][3]), b = (t[j][1][0] + t[j][1][1]) + (t[j][1][2] + t[j][1][3]);
      auto r0 = __builtin_amdgcn_permlane32_swap(pack_bf16(a[0], a[1]), pack_bf16(b[0], b[1]), false, false);
      auto r1 = __builtin_amdgcn_permlane32_swap(pack_bf16(a[2], a[3]), pack_bf16(b[2], b[3]), false, false);
      const u32x4 v = {r0[0], r1[0], r0[1], r1[1]};
      __builtin_amdgcn_raw_buffer_store_b128(v, wsr, ol, WS_W2 + ((2 * gt + (fi & 1)) * 40 + 5 * et + (fi >> 1)) * 1024, 16);
    }
    if (SEAM == 1) BAR();
    STAMP(7);
    WAIT_VM(0);
    unsigned old = 0;
    if (lane == 0) old = __hip_atomic_fetch_add((lu32*)(size_t)LW, 1u, __ATOMIC_RELAXED, __HIP_MEMORY_SCOPE_WORKGROUP);
    if (__builtin_amdgcn_readfirstlane(old) == 3) publish(p.ws, wf_off(gt * 8 + et), tag, lane);
    FENCE();
    if (SEAM == 2) BAR();
  }
}

constexpr int XREG = XREGS;
constexpr int YT = 40 - KS0;
constexpr int y_ks(int n) { return n < 3 * KS0 ? n / 3 : KS0 + (n - 3 * KS0) % YT; }
constexpr int y_tt(int n) { return n < 3 * KS0 ? n % 3 : (n - 3 * KS0) / YT; }
constexpr int y_lds(int n) { return n < 3 * KS0 ? ((n / 12) * 3 + n % 3) * 4096 + ((n % 12) / 3) * 1024 : n * 1024; }
constexpr int y_t(int n, int g) { return n < 3 * KS0 ? g % 3 : y_tt(n); }
constexpr int y_ks0(int g) { return 4 * g < 3 * KS0 ? (g / 3) * 4 : y_ks(4 * g); }
__device__ __forceinline__ void y_load(const Params& p, int wave, int lane, int st, int gt, int rot, u64 tag, bool isx) {
  const int lw = wave - 4;
  STAMP(8);
  if (lw == 0) {
    if (XWAIT && isx) lds_wait(LW + 12);
    else if (YDELAY > 0) __builtin_amdgcn_s_sleep(YDELAY);
    wait_flags(p.ws, wf_off((3 * gt / 2) * 8 + (lane & 15)), tag);
    lds_set(LW + 4);
  } else lds_wait(LW + 4);
  if (blockIdx.x == 0) {
    const int w = lw * 64 + lane;
    wait_flags(p.ws, w < 64 ? xf_off(w >> 2, w & 3) : wf_off(w - 64), tag);
  }
  STAMP(11);
  const char* wb = p.ws + WS_W2 + (size_t)(3 * gt * 40 + lw) * 1024;
  const char* xb = p.ws + WS_XR + (size_t)(st * 4 + lw) * 40960;
  const unsigned vo = lane * 16;
  auto issue = [&](auto cc) {
    static_for<3 * decltype(cc)::value, 3 * decltype(cc)::value + 3>([&](auto gc) {
      constexpr int g = decltype(gc)::value;
      glds<0, true>(wb + (y_t(4 * g, g) * 40 + y_ks0(g)) * 1024, vo, g * 4096 + lw * 1024);
    });
  };
  issue(IC<0>{}); issue(IC<1>{}); issue(IC<2>{});
  WAIT_VM(6); BAR();
  if (blockIdx.x == 0 && threadIdx.x == 256) __hip_atomic_store((gu64*)(p.ws + WS_EPOCH), tag, RLX_AGENT);
  issue(IC<3>{}); issue(IC<4>{});
  WAIT_VM(9); BAR();
  const unsigned xl = 122880 + lw * 10240;
  if constexpr (XREG == 30) {
    glds<0, true>(xb + 30 * 1024, vo, xl); glds<1024, true>(xb + 30 * 1024, vo, xl);
    glds4_lin_sc1(xb + 32 * 1024, vo, xl + 2048);
    glds4_lin_sc1(xb + 36 * 1024, vo, xl + 6144);
  }
  issue(IC<5>{});
  WAIT_VM(6 + 40 - XREG); BAR();
  issue(IC<6>{}); issue(IC<7>{});
  WAIT_VM(6); BAR();
  issue(IC<8>{}); issue(IC<9>{});
  WAIT_VM(6); BAR();
  WAIT_VM(0); BAR();
}

__device__ __forceinline__ void y_compute(const Params& p, __amdgpu_buffer_rsrc_t wsr, int wave, int lane, int st, int gt, int rot, u64 tag, bool b4) {
  STAMP(8);
  if (wave == 0) { if (XDELAY > 0 && !b4) __builtin_amdgcn_s_sleep(XDELAY); wait_flags(p.ws, xf_off(st, lane & 3), tag); lds_set(LW + 8); } else lds_wait(LW + 8);
  if (SEAM > 0 && b4) BAR();
  STAMP(9);
  u32x4 xf[XREG];
  const unsigned xbase = WS_XR + (st * 4 + wave) * 40 * 1024;
#pragma unroll
  for (int ks = 0; ks < XREG; ++ks) {
    if (XSPLIT > 0 && ks == XSPLIT) { if (b4) { lds_wait(LW + 4); if (XSLEEP > 0) __builtin_amdgcn_s_sleep(XSLEEP); } }
    xf[ks] = __builtin_amdgcn_raw_buffer_load_b128(wsr, lane * 16 + (ks & 3) * 1024, xbase + (ks >> 2) * 4096, 16);
  }
  FENCE();
  if (XWAIT && wave == 3) lds_set(LW + 12);
  STAMP(10);
  f32x16 acc[3];
#pragma unroll
  for (int t = 0; t < 3; ++t)
#pragma unroll
    for (int j = 0; j < 16; ++j) acc[t][j] = 0.f;
  {
    constexpr int L = 6;
    bf16x8 aw[L + 1], bx[10];
    const unsigned a0 = lane * 16, x0 = 122880 + wave * 10240 + lane * 16;
    unsigned a1 = lane * 16 + 61440;
    opaque(a1);
    typedef unsigned u32x2 __attribute__((ext_vector_type(2)));
    const __amdgpu_buffer_rsrc_t yr = __builtin_amdgcn_make_buffer_rsrc(p.y, 0, S_ * F_ * 2, 0x00020000);
    constexpr unsigned EB = XREG == 40 ? 122880 : 0;
    const unsigned ew = EB + wave * 8192 + (lane & 31) * 80 + (lane >> 5) * 8, er = EB + wave * 8192 + (lane >> 2) * 80 + (lane & 3) * 16;
    const unsigned yo = (lane >> 2) * 3072 + (lane & 3) * 16, ys = (st * 128 + wave * 32) * 3072 + gt * 192;
    u32x4 eo[2];
    auto epi = [&](auto tc, auto sc) {
      constexpr int t = decltype(tc)::value, e = decltype(sc)::value;
      if constexpr (e < 2) {
#pragma unroll
        for (int i = 2 * e; i < 2 * e + 2; ++i) {
          const u32x2 v = {pack_bf16(acc[t][4 * i], acc[t][4 * i + 1]), pack_bf16(acc[t][4 * i + 2], acc[t][4 * i + 3])};
          *(LAS u32x2*)(size_t)(ew + t * 2560 + i * 16) = v;
        }
        if constexpr (e == 1) { eo[0] = lds_rd128(er + t * 2560); eo[1] = lds_rd128(er + t * 2560 + 1280); }
      } else if constexpr (e >= 5) {
        __builtin_amdgcn_raw_buffer_store_b128(eo[e - 5], yr, yo + (e - 5) * (16 * 3072) + t * 64, ys, YAUX);
      }
    };
    static_for<0, 120 + L>([&](auto ic) {
      constexpr int n = decltype(ic)::value, j = n - L;
      if constexpr (n < 120) {
        if constexpr (n == 0 || n == 12 || n == 24 || n == 48 || n == 72 || n == 96) {
          BAR();
          if constexpr (n == 0) STAMP(11);
        }
        if constexpr (y_tt(n) == 0 && y_ks(n) >= XREG) bx[y_ks(n) - XREG] = __builtin_bit_cast(bf16x8, lds_rd128(x0 + (y_ks(n) - XREG) * 1024));
        aw[n % (L + 1)] = __builtin_bit_cast(bf16x8, lds_rd128(y_lds(n) < 61440 ? a0 + y_lds(n) : a1 + (y_lds(n) - 61440)));
      }
      if constexpr (j >= 0) {
        constexpr int ks = y_ks(j), t = y_tt(j);
        if constexpr (ks < XREG) acc[t] = __builtin_amdgcn_mfma_f32_32x32x16_bf16(aw[j % (L + 1)], __builtin_bit_cast(bf16x8, xf[ks]), acc[t], 0, 0, 0);
        else acc[t] = __builtin_amdgcn_mfma_f32_32x32x16_bf16(aw[j % (L + 1)], bx[ks - XREG], acc[t], 0, 0, 0);
        __builtin_amdgcn_sched_barrier(0);
        if constexpr (j >= 3 * KS0 && t >= 1 && ks - KS0 >= 1 && ks - KS0 <= 7) epi(IC<t - 1>{}, IC<ks - KS0 - 1>{});
      }
      __builtin_amdgcn_sched_barrier(0);
    });
    STAMP(12);
    static_for<0, 7>([&](auto sc) { epi(IC<2>{}, sc); });
  }
}

__global__ void __launch_bounds__(512, 2) quip_kernel(Params p) {
  const int wave = __builtin_amdgcn_readfirstlane(threadIdx.x >> 6), lane = threadIdx.x & 63;
  const int xcd = blockIdx.x & 7, idx = blockIdx.x >> 3;
  STAMP(0); STAMPC(14);
  unsigned pf = 0;
  if (CODEKB > 0) {
    const unsigned ln = PFMAP ? (31 - idx) * 8 + wave : idx * 8 + wave;
    const GAS unsigned* code = (const GAS unsigned*)((__builtin_amdgcn_s_getpc() & ~255ull) + (ln < CODEKB * 8 ? ln : 0) * 128);
    if (ln < CODEKB * 8) pf = __hip_atomic_load(code, __ATOMIC_RELAXED, __HIP_MEMORY_SCOPE_WORKGROUP);
  }
  const bool isx = idx < 8;
  const int xst = 4 * (xcd >> 1) + (idx >> 1), etl = 2 * (xcd & 1) + (idx & 1);
  const int wk = idx - 8;
  const int et = wk & 7, gt = 3 * xcd + (wk >> 3);
  const int rotc = (3 * et >> 1) + ROTC * 4 * (wk >> 3);
  const int dj = xcd * 24 + wk, det = xcd, dkp = wk;
  const int rotx = ROTX * ((idx >> 1) * 2 + (idx & 1) * 5) % 10;
  XLoader xl; VLoader vl;
  unsigned pf2 = 0, pf3 = 0;
  if (!isx && wave < 4) { if (DPRIO) __builtin_amdgcn_s_setprio(3); d_issue(p, wave, lane, det, dkp); }
  if (isx && wave >= 4) { xl.init(p, xst, etl, wave - 4, lane, rotx); xl.issue(0); xl.issue(1); }
  vl.init(p, gt, wave & 3, rotc >= 12 ? rotc - 12 : rotc, lane);
  if (!isx && wave >= 4) {
    const unsigned w96 = wk * 4 + wave - 4;
    if (PFX == 1 || PFX == 3) {
      const unsigned n = w96 * 35 + (lane < 35 ? lane : 0), lu = n - 2560, ur = (lu * 13108) >> 16;
      const char* a = n < 2560 ? p.x + (size_t)xcd * (256 * 1280) + n * 128
                               : p.U + (xcd & 1) * 640 + ((xcd >> 1) * 160 + ur) * 1280 + (lu - ur * 5) * 128;
      if (PFX == 3) { vl.issue(0); vl.issue(1); if (!WMAP) vl.issue(2); }
      FENCE();
      pf2 = __hip_atomic_load((const GAS unsigned*)a, __ATOMIC_RELAXED, __HIP_MEMORY_SCOPE_WORKGROUP);
      FENCE();
    } else if (PFX == 4) {
      const unsigned n = w96 * 28 + (lane < 28 ? lane : 0), lu = n - 2048, ur = (lu * 13108) >> 16;
      const char* a = n < 2048 ? p.x + (size_t)(xcd * 256 + (n >> 3)) * 1280 + (2 + (n & 7)) * 128
                               : p.U + (xcd & 1) * 640 + (128 + ur * 4 + (xcd >> 1)) * 1280 + (lu - ur * 5) * 128;
      vl.issue(0); vl.issue(1);
      FENCE();
      pf2 = __hip_atomic_load((const GAS unsigned*)a, __ATOMIC_RELAXED, __HIP_MEMORY_SCOPE_WORKGROUP);
      FENCE();
    } else if (PFX == 2) {
      const unsigned n = w96 * 54 + (lane < 54 ? lane : 0), lu = w96 * 34 + (lane < 34 ? lane : 0), ur = (lu * 13108) >> 16;
      FENCE();
      pf2 = __hip_atomic_load((const GAS unsigned*)(p.x + (size_t)(xcd >> 1) * (512 * 1280) + (n < 5120 ? n : 0) * 128), __ATOMIC_RELAXED, __HIP_MEMORY_SCOPE_WORKGROUP);
      pf3 = __hip_atomic_load((const GAS unsigned*)(p.U + (xcd & 1) * 640 + (ur < 640 ? ur : 0) * 1280 + (lu - ur * 5) * 128), __ATOMIC_RELAXED, __HIP_MEMORY_SCOPE_WORKGROUP);
      FENCE();
    }
    if (PFX < 3) { vl.issue(0); vl.issue(1); }
  }
  const __amdgpu_buffer_rsrc_t wsr = __builtin_amdgcn_make_buffer_rsrc(p.ws, 0, WS_END, 0x00020000);
  u64 tag = 0;
  const int yst = 4 * (xcd >> 1) + (idx & 3), ygt = 8 * (xcd & 1) + (idx >> 2);
  if (wave < 4) {
    if (isx) {
      x_compute(p, wsr, wave, lane, xst, etl, tag, idx == 0 && wave == 0);
      asm volatile("" :: "v"(pf));
    } else {
      WAIT_VM(0);
      const u64 ev = load_tag_v(p);
      STAMP(1);
      d_compute(p, wsr, wave, lane, det, dkp);
      STAMP(2);
      WAIT_VM(0);
      tag = make_tag_v(ev);
      asm volatile("" :: "v"(pf), "s"(tag));
      BAR();
      if (wave == 0) publish(p.ws, WS_DF + dj * FS, tag, lane);
      STAMP(3);
      if (DPRIO) __builtin_amdgcn_s_setprio(0);
      w_body<2>(p, wsr, vl, wave, lane, et, gt, tag);
    }
    y_compute(p, wsr, wave, lane, yst, ygt, 0, tag, !isx);
  } else {
    if (isx) {
      u64 ts[10];
      xl.run(p, tag, ts);
      asm volatile("" :: "v"(pf));
      __builtin_amdgcn_s_setprio(3);
    } else {
      BAR();
      tag = load_tag(p);
      STAMP(3);
#pragma unroll 1
      for (int i = WMAP ? 2 : 3; i < 10; ++i) vl.issue(i);
      make_tag(tag);
      if (wave == 4) wait_flags(p.ws, WS_DF + (et * 24 + (lane < 24 ? lane : 0)) * FS, tag);
      WAIT_VM(0);
      asm volatile("" :: "v"(pf), "v"(pf2), "v"(pf3));
      STAMP(4);
      __builtin_amdgcn_s_setprio(WPRIO);
      w_body<3>(p, wsr, vl, wave, lane, et, gt, tag);
    }
    y_load(p, wave, lane, yst, ygt, 0, tag, isx);
  }
  STAMP(13); STAMPC(15);
}

extern "C" void kernel_launch(void* const* d_in, const int* in_sizes, int n_in, void* d_out, int out_size, void* d_ws,
                              size_t ws_size, hipStream_t stream) {
  Params p;
  p.x = (const char*)d_in[0]; p.qw = (const char*)d_in[1]; p.sc = (const char*)d_in[2]; p.zr = (const char*)d_in[3];
  p.U = (const char*)d_in[4]; p.V = (const char*)d_in[5]; p.y = (char*)d_out; p.ws = (char*)d_ws;
  (void)hipFuncSetAttribute((const void*)quip_kernel, hipFuncAttributeMaxDynamicSharedMemorySize, LDS_BYTES);
  quip_kernel<<<256, 512, LDS_BYTES, stream>>>(p);
}
```

```cpp
#include <hip/hip_runtime.h>
#include <stdint.h>

#define LAS __attribute__((address_space(3)))
#define GAS __attribute__((address_space(1)))
typedef unsigned short u16;
typedef unsigned long long u64;
typedef short s16x4 __attribute__((ext_vector_type(4)));
typedef short s16x8 __attribute__((ext_vector_type(8)));
typedef __bf16 bf16x8 __attribute__((ext_vector_type(8)));
typedef __bf16 bf16x2 __attribute__((ext_vector_type(2)));
typedef float f32x2 __attribute__((ext_vector_type(2)));
typedef float f32x4 __attribute__((ext_vector_type(4)));
typedef float f32x16 __attribute__((ext_vector_type(16)));
typedef unsigned u32x4 __attribute__((ext_vector_type(4)));
typedef int i32x4 __attribute__((ext_vector_type(4)));
typedef GAS u64 gu64;

constexpr int S_ = 2048, D_ = 640, F_ = 1536;
constexpr unsigned WS_EPOCH = 0;
constexpr unsigned FS = 128;
constexpr unsigned WS_DF = 4096;
constexpr unsigned WS_XF = WS_DF + 192 * FS;
constexpr unsigned WS_WF = WS_XF + 256 * FS;
constexpr unsigned WS_STAMP = 512 * 1024;
constexpr unsigned WS_WQ = 1u << 20;
constexpr unsigned WS_XR = WS_WQ + D_ * F_ * 2;
constexpr unsigned WS_W2 = WS_XR + S_ * D_ * 2;
constexpr unsigned WS_END = WS_W2 + D_ * F_ * 2;
constexpr int LDS_BYTES = 160 * 1024;

#define XSPREAD 1
#define ROTX 0
#define ROTC 0
#define WMAP 1
#define DPOLL 1
#define PFMAP 0
#define VDELAY 0
#define FPACK 1
#define FTOUCH 0
#define ROTY 0
#define YDELAY 12
#define YMAP 1
#define VMID 0
#define VPAUSE 0
#define DDRY 0
#define EPI 2
#define PFX 3
#define VMODE 1
#define PDELAY 0
#define XWAIT 0
#define XDELAY 0
#define FLANES 16
#define EDUMP 1
#define DPRIO 0
#define WPRIO 0
#define KS0 16
#define XSPLIT 0
#define XSLEEP 0
#define XREGS 30
#define QNT 0
#define DAUX 16
#define XAUX 16
#define WAUX 16
#define WSPOS 4
#define XLA 6
#define VEARLY 2
#define SEAM 2
#define POLLFIRST 1
#define PSLEEP 2
#define XPRE 5
#define YAUX 18
#define CODEKB 16

__device__ __forceinline__ unsigned xf_off(int st, int etl) { return WS_XF + (st * 4 + etl) * FS; }
__device__ __forceinline__ unsigned wf_off(int i) { return WS_WF + i * FS; }
constexpr unsigned LW = 79 * 1024;

struct Params {
  const char* x; const char* qw; const char* sc; const char* zr; const char* U; const char* V; char* y; char* ws;
};

#define RLX_AGENT __ATOMIC_RELAXED, __HIP_MEMORY_SCOPE_AGENT
#define WAIT_VM(n) asm volatile("s_waitcnt vmcnt(%0)" :: "n"(n) : "memory")
#define WAIT_LGKM0() asm volatile("s_waitcnt lgkmcnt(0)" ::: "memory")
#define BAR() do { asm volatile("" ::: "memory"); __builtin_amdgcn_s_barrier(); asm volatile("" ::: "memory"); } while (0)
#define FENCE() asm volatile("" ::: "memory")

#define STAMP(i) do {} while (0)
#define STAMPC(i) do {} while (0)

#define TS(v)
#define TS2(v)
template <int I> struct IC { static constexpr int value = I; };
template <int B, int E, class Fn>
__device__ __forceinline__ void static_for(Fn&& fn) {
  if constexpr (B < E) {
    if constexpr (E - B == 1) fn(IC<B>{});
    else { static_for<B, (B + E) / 2>(fn); static_for<(B + E) / 2, E>(fn); }
  }
}

template <int OFF, bool SC1>
__device__ __forceinline__ void glds(const char* sbase, unsigned voff, unsigned lds) {
  if constexpr (SC1)
    asm volatile("s_mov_b32 m0, %2\n\ts_nop 0\n\tglobal_load_lds_dwordx4 %0, %1 offset:%3 sc1" :: "v"(voff), "s"(sbase), "s"(lds), "n"(OFF) : "memory");
  else
    asm volatile("s_mov_b32 m0, %2\n\ts_nop 0\n\tglobal_load_lds_dwordx4 %0, %1 offset:%3" :: "v"(voff), "s"(sbase), "s"(lds), "n"(OFF) : "memory");
}
__device__ __forceinline__ void glds4(const char* sbase, unsigned v0, unsigned v1, unsigned v2, unsigned v3, unsigned lds) {
  asm volatile("s_mov_b32 m0, %5\n\ts_nop 0\n\tglobal_load_lds_dwordx4 %0, %4\n\tglobal_load_lds_dwordx4 %1, %4 offset:1024\n\t"
               "global_load_lds_dwordx4 %2, %4 offset:2048\n\tglobal_load_lds_dwordx4 %3, %4 offset:3072"
               :: "v"(v0), "v"(v1), "v"(v2), "v"(v3), "s"(sbase), "s"(lds) : "memory");
}
__device__ __forceinline__ void glds4_nt(const char* sbase, unsigned v0, unsigned v1, unsigned v2, unsigned v3, unsigned lds) {
  asm volatile("s_mov_b32 m0, %5\n\ts_nop 0\n\tglobal_load_lds_dwordx4 %0, %4 nt\n\tglobal_load_lds_dwordx4 %1, %4 offset:1024 nt\n\t"
               "global_load_lds_dwordx4 %2, %4 offset:2048 nt\n\tglobal_load_lds_dwordx4 %3, %4 offset:3072 nt"
               :: "v"(v0), "v"(v1), "v"(v2), "v"(v3), "s"(sbase), "s"(lds) : "memory");
}
__device__ __forceinline__ void glds2_nt(const char* sbase, unsigned v0, unsigned v1, unsigned lds) {
  asm volatile("s_mov_b32 m0, %3\n\ts_nop 0\n\tglobal_load_lds_dwordx4 %0, %2 nt\n\tglobal_load_lds_dwordx4 %1, %2 offset:1024 nt"
               :: "v"(v0), "v"(v1), "s"(sbase), "s"(lds) : "memory");
}
__device__ __forceinline__ void glds4_lin_sc1(const char* sbase, unsigned v, unsigned lds) {
  asm volatile("s_mov_b32 m0, %2\n\ts_nop 0\n\tglobal_load_lds_dwordx4 %0, %1 sc1\n\tglobal_load_lds_dwordx4 %0, %1 offset:1024 sc1\n\t"
               "global_load_lds_dwordx4 %0, %1 offset:2048 sc1\n\tglobal_load_lds_dwordx4 %0, %1 offset:3072 sc1"
               :: "v"(v), "s"(sbase), "s"(lds) : "memory");
}
template <int OFF>
__device__ __forceinline__ void gload_sc1(u32x4& d, const char* sbase, unsigned voff) {
  asm volatile("global_load_dwordx4 %0, %1, %2 offset:%3 sc1" : "=v"(d) : "v"(voff), "s"(sbase), "n"(OFF) : "memory");
}
template <int N>
__device__ __forceinline__ void wait_for(u32x4& d) { asm volatile("s_waitcnt vmcnt(%1)" : "+v"(d) : "n"(N) : "memory"); }
__device__ __forceinline__ void opaque(unsigned& v) { asm volatile("" : "+v"(v)); }
__device__ __forceinline__ void glds_v(const char* vaddr, unsigned lds) {
  asm volatile("s_mov_b32 m0, %1\n\ts_nop 0\n\tglobal_load_lds_dwordx4 %0, off" :: "v"(vaddr), "s"(lds) : "memory");
}

__device__ __forceinline__ unsigned pack_bf16(float a, float b) {
  f32x2 v = {a, b};
  return __builtin_bit_cast(unsigned, __builtin_convertvector(v, bf16x2));
}
__device__ __forceinline__ float bf_lo(unsigned v) { return __builtin_bit_cast(float, v << 16); }
__device__ __forceinline__ float bf_hi(unsigned v) { return __builtin_bit_cast(float, v & 0xffff0000u); }

__device__ __forceinline__ u32x4 lds_rd128(unsigned a) { return *(LAS u32x4*)(size_t)a; }
__device__ __forceinline__ void lds_wr128(unsigned a, u32x4 v) { *(LAS u32x4*)(size_t)a = v; }
__device__ __forceinline__ bf16x8 lds_tr2(unsigned a0, unsigned a1) {
  s16x4 lo = __builtin_amdgcn_ds_read_tr16_b64_v4i16((LAS s16x4*)(size_t)a0);
  s16x4 hi = __builtin_amdgcn_ds_read_tr16_b64_v4i16((LAS s16x4*)(size_t)a1);
  return __builtin_bit_cast(bf16x8, __builtin_shufflevector(lo, hi, 0, 1, 2, 3, 4, 5, 6, 7));
}

__device__ __forceinline__ u64 load_tag(const Params& p) {
  const char* e = p.ws + WS_EPOCH;
  asm volatile("" : "+s"(e));
  return *(const __attribute__((address_space(4))) u64*)e;
}
__device__ __forceinline__ u64 load_tag_v(const Params& p) { FENCE(); const u64 e = __hip_atomic_load((gu64*)(p.ws + WS_EPOCH), RLX_AGENT); FENCE(); return e; }
__device__ __forceinline__ u64 make_tag_v(u64 e) {
  asm volatile("" : "+v"(e));
  const u64 u = ((u64)(unsigned)__builtin_amdgcn_readfirstlane((unsigned)(e >> 32)) << 32) | (unsigned)__builtin_amdgcn_readfirstlane((unsigned)e);
  return u + 0x9E3779B97F4A7C15ull;
}
__device__ __forceinline__ void make_tag(u64& e) {
  asm volatile("" : "+s"(e));
  e += 0x9E3779B97F4A7C15ull;
  asm volatile("" : "+s"(e));
}

__device__ __forceinline__ void wait_flags(const char* ws, unsigned off, u64 tag) {
  gu64* f = (gu64*)(ws + off);
#pragma unroll 1
  for (unsigned spins = 0; spins < (1u << 20); ++spins) {
    u64 v = __hip_atomic_load(f, RLX_AGENT);
    if (__all(v == tag)) break;
    __builtin_amdgcn_s_sleep(PSLEEP);
  }
  FENCE();
}
typedef LAS unsigned lu32;
__device__ __forceinline__ void lds_set(unsigned a) { __hip_atomic_store((lu32*)(size_t)a, 1u, __ATOMIC_RELAXED, __HIP_MEMORY_SCOPE_WORKGROUP); FENCE(); }
__device__ __forceinline__ void lds_wait(unsigned a) {
#pragma unroll 1
  for (unsigned spins = 0; spins < (1u << 24); ++spins) {
    if (__builtin_amdgcn_readfirstlane(__hip_atomic_load((lu32*)(size_t)a, __ATOMIC_RELAXED, __HIP_MEMORY_SCOPE_WORKGROUP))) break;
    __builtin_amdgcn_s_sleep(1);
  }
  FENCE();
}
__device__ __forceinline__ void wait_flags2(__amdgpu_buffer_rsrc_t wsr, unsigned off, u64 tag) {
  const unsigned lo = (unsigned)tag, hi = (unsigned)(tag >> 32);
#pragma unroll 1
  for (unsigned spins = 0; spins < (1u << 20); ++spins) {
    FENCE();
    const u32x4 v = __builtin_amdgcn_raw_buffer_load_b128(wsr, off, 0, 16);
    if (__all(v[0] == lo && v[1] == hi && v[2] == lo && v[3] == hi)) break;
    __builtin_amdgcn_s_sleep(PSLEEP);
  }
  FENCE();
}
__device__ __forceinline__ void publish(char* ws, unsigned off, u64 tag, int lane) {
  WAIT_VM(0);
  if (lane < FLANES) __hip_atomic_store((gu64*)(ws + off + lane * 8), tag, RLX_AGENT);
  FENCE();
}

__device__ __forceinline__ unsigned dequant2(int q0, int q1, float z0, float z1, float s0, float s1) {
  unsigned qb = pack_bf16((float)q0, (float)q1);
  unsigned t = pack_bf16(bf_lo(qb) - z0, bf_hi(qb) - z1);
  return pack_bf16(bf_lo(t) * s0, bf_hi(t) * s1);
}

constexpr unsigned XSTG = 36 * 1024, XUOFF = 16 * 1024;

struct XLoader {
  unsigned xo[4], uo[5]; const char* xb; const char* ub; int rot, lw;
  __device__ __forceinline__ void init(const Params& p, int st, int etl, int lw_, int lane, int rot_) {
    lw = lw_;
    rot = rot_;
#pragma unroll
    for (int i = 0; i < 4; ++i) {
      const int row = 8 * i + (lane >> 3);
      xo[i] = row * 1280 + (((lane & 7) ^ ((row >> 1) & 7)) << 4) - i * 1024;
    }
#pragma unroll
    for (int i = 0; i < 5; ++i) {
      const int u = i * 64 + lane, row = (u * 3277) >> 16;
      uo[i] = row * 960 + u * 16 - (i & 3) * 1024;
    }
    xb = p.x + (size_t)(st * 128 + lw * 32) * 1280;
    ub = p.U + etl * 320 + lw * (16 * 1280);
  }
  __device__ __forceinline__ void issue(int i) {
    int kt = i + rot; if (kt >= 10) kt -= 10;
    const unsigned slot = (i & 3) * XSTG;
    const char* us = ub + (size_t)kt * (64 * 1280);
    glds4(us, uo[0], uo[1], uo[2], uo[3], slot + XUOFF + lw * 5120);
    glds<0, false>(us, uo[4], slot + XUOFF + lw * 5120 + 4096);
    glds4(xb + kt * 128, xo[0], xo[1], xo[2], xo[3], slot + lw * 4096);
  }
  __device__ __forceinline__ void run(const Params& p, u64& tag, u64 (&ts)[10]) {
    WAIT_VM(9); TS2(ts[0]); BAR();
    tag = load_tag(p);
#pragma unroll 1
    for (int i = 1; i < 9; ++i) {
      issue(i + 1); WAIT_VM(9);
      if (i == 1) TS2(ts[1]); if (i == 2) TS2(ts[2]); if (i == 4) TS2(ts[4]); if (i == 7) TS2(ts[6]);
      BAR();
      if (i == 2) TS2(ts[3]); if (i == 4) TS2(ts[5]); if (i == 7) TS2(ts[7]);
    }
    make_tag(tag);
    WAIT_VM(0); BAR();
    lds_wr128(LW, (u32x4){0u, 0u, 0u, 0u});
    WAIT_LGKM0();
    BAR();
    BAR();
  }
};

__device__ __forceinline__ void x_compute(const Params& p, __amdgpu_buffer_rsrc_t wsr, int wave, int lane, int st, int etl, u64& tag, bool first) {
  const int r = lane & 31, h = lane >> 5;
  unsigned xa0[4];
#pragma unroll
  for (int kk = 0; kk < 4; ++kk) xa0[kk] = wave * 4096 + r * 128 + (((2 * kk + h) ^ ((r >> 1) & 7)) << 4);
  const unsigned ua0 = XUOFF + (8 * h + ((lane & 15) >> 2)) * 320 + ((lane >> 4) & 1) * 32 + (lane & 3) * 8;
  f32x16 acc[5];
#pragma unroll
  for (int t = 0; t < 5; ++t)
#pragma unroll
    for (int j = 0; j < 16; ++j) acc[t][j] = 0.f;
  constexpr int L = XLA;
  bf16x8 au[10], bx[4];
#pragma unroll
  for (int t = 0; t < 5; ++t) asm volatile("" : "+v"(acc[t]));
  u64 ev = 0;
  if (first) ev = load_tag_v(p);
  BAR();
  if (!first) ev = load_tag_v(p);
  STAMP(1);
#pragma unroll
  for (int n = 0; n < L; ++n) {
    if (n % 5 == 0) bx[n / 5] = __builtin_bit_cast(bf16x8, lds_rd128(xa0[n / 5]));
    __builtin_amdgcn_sched_barrier(0);
    au[n] = lds_tr2(ua0 + (n / 5) * 16 * 320 + (n % 5) * 64, ua0 + (n / 5) * 16 * 320 + (n % 5) * 64 + 4 * 320);
    __builtin_amdgcn_sched_barrier(0);
  }
#pragma unroll 1
  for (int i = 0; i < 10; ++i) {
    const unsigned cur = (i & 3) * XSTG, nxt = ((i + 1) & 3) * XSTG;
    const unsigned ua = ua0 + cur, un = ua0 + nxt;
    static_for<0, 20>([&](auto jc) {
      constexpr int j = decltype(jc)::value, n = j + L;
      if constexpr (n == 20) BAR();
      constexpr int kk = (n % 20) / 5, t = n % 5;
      if constexpr (t == 0) { bx[kk] = __builtin_bit_cast(bf16x8, lds_rd128(xa0[kk] + (n < 20 ? cur : nxt))); __builtin_amdgcn_sched_barrier(0); }
      const unsigned ad = (n < 20 ? ua : un) + kk * 16 * 320 + t * 64;
      au[n % 10] = lds_tr2(ad, ad + 4 * 320);
      acc[j % 5] = __builtin_amdgcn_mfma_f32_32x32x16_bf16(au[j % 10], bx[j / 5], acc[j % 5], 0, 0, 0);
      __builtin_amdgcn_sched_barrier(0);
    });
  }
  STAMP(6);
  const unsigned ob = WS_XR + ((st * 4 + wave) * 40 + etl * 10) * 1024;
#pragma unroll
  for (int t = 0; t < 5; ++t)
#pragma unroll
    for (int s2 = 0; s2 < 2; ++s2) {
      u32x4 v;
#pragma unroll
      for (int j = 0; j < 4; ++j) v[j] = pack_bf16(acc[t][8 * s2 + 2 * j], acc[t][8 * s2 + 2 * j + 1]);
      __builtin_amdgcn_raw_buffer_store_b128(v, wsr, lane * 16, ob + (2 * t + s2) * 1024, XAUX);
    }
  STAMP(7);
  WAIT_LGKM0();
  WAIT_VM(0);
  tag = make_tag_v(ev);
  asm volatile("" :: "s"(tag));
  BAR();
  if (wave == 0) publish(p.ws, xf_off(st, etl), tag, lane);
}

__device__ __forceinline__ void d_issue(const Params& p, int wave, int lane, int det, int dkp) {
  const unsigned dl = (wave * 10 + 8) * 4096;
  const int k2 = wave & 1, nfr = wave < 2 ? 3 : 2;
  unsigned qo[2];
#pragma unroll
  for (int i = 0; i < 2; ++i) {
    const int row = 8 * i + (lane >> 3);
    qo[i] = row * 6144 + (((lane & 7) ^ ((row >> 1) & 7)) << 4) - i * 1024;
  }
  const char* qb = p.qw + (size_t)(80 * det + 16 * (wave >> 1)) * 6144 + (64 * dkp + 32 * k2) * 4;
  if (QNT) {
    glds4_nt(qb, qo[0], qo[1], qo[0] + (32 * 6144 - 2048), qo[1] + (32 * 6144 - 2048), dl);
    if (wave < 2) glds2_nt(qb + 64 * 6144, qo[0], qo[1], dl + 4096);
  } else {
    glds4(qb, qo[0], qo[1], qo[0] + (32 * 6144 - 2048), qo[1] + (32 * 6144 - 2048), dl);
    if (wave < 2) {
      glds<0, false>(qb + 64 * 6144, qo[0], dl + 4096);
      glds<1024, false>(qb + 64 * 6144, qo[1], dl + 4096);
    }
  }
  int i = (lane >> 3) & 3; if (i >= nfr) i = 0;
  const int grp = (5 * det + (wave >> 1) + 2 * i) >> 1;
  const char* sa = (((lane >> 2) & 1) ? p.zr : p.sc) + (size_t)grp * 3072 + (64 * dkp + 32 * k2) * 2 + (lane & 3) * 16;
  glds_v(sa, dl + 6144);
}

__device__ __forceinline__ void d_compute(const Params& p, __amdgpu_buffer_rsrc_t wsr, int wave, int lane, int det, int dkp) {
  const unsigned dl = (wave * 10 + 8) * 4096;
  const int r = lane & 15, q = lane >> 4;
  const unsigned a0 = dl + r * 128 + (((2 * q) ^ ((r >> 1) & 7)) << 4), a1 = dl + r * 128 + (((2 * q + 1) ^ ((r >> 1) & 7)) << 4);
  const unsigned as = dl + 6144 + q * 16;
  const unsigned ob = WS_WQ + ((det * 48 + 2 * dkp + (wave & 1)) * 5 + (wave >> 1)) * 1024;
  const int nfr = wave < 2 ? 3 : 2;
  i32x4 q0 = __builtin_bit_cast(i32x4, lds_rd128(a0)), q1 = __builtin_bit_cast(i32x4, lds_rd128(a1));
  u32x4 s = lds_rd128(as), z = lds_rd128(as + 64);
#pragma unroll 1
  for (int i = 0; i < nfr; ++i) {
    const i32x4 nq0 = __builtin_bit_cast(i32x4, lds_rd128(a0 + i * 2048 + 2048)), nq1 = __builtin_bit_cast(i32x4, lds_rd128(a1 + i * 2048 + 2048));
    const u32x4 ns = lds_rd128(as + i * 128 + 128), nz = lds_rd128(as + i * 128 + 192);
    u32x4 o;
    o[0] = dequant2(q0[0], q0[1], bf_lo(z[0]), bf_hi(z[0]), bf_lo(s[0]), bf_hi(s[0]));
    o[1] = dequant2(q0[2], q0[3], bf_lo(z[1]), bf_hi(z[1]), bf_lo(s[1]), bf_hi(s[1]));
    o[2] = dequant2(q1[0], q1[1], bf_lo(z[2]), bf_hi(z[2]), bf_lo(s[2]), bf_hi(s[2]));
    o[3] = dequant2(q1[2], q1[3], bf_lo(z[3]), bf_hi(z[3]), bf_lo(s[3]), bf_hi(s[3]));
    __builtin_amdgcn_raw_buffer_store_b128(o, wsr, lane * 16, ob + i * 2048, DAUX);
    q0 = nq0; q1 = nq1; s = ns; z = nz;
  }
}

struct VLoader {
  unsigned vo[4]; const char* vb; int rot, kq;
  __device__ __forceinline__ void init(const Params& p, int gt, int kq_, int rot_, int lane) {
    kq = kq_; rot = rot_;
#pragma unroll
    for (int pc = 0; pc < 4; ++pc) {
      const int row = 8 * pc + (lane >> 3), swz = ((row >> 1) & 1) | (((row >> 3) & 1) << 1);
      vo[pc] = row * 3072 + ((((lane & 7) >> 1) ^ swz) << 5) + ((lane & 1) << 4) - pc * 1024;
    }
    vb = p.V + gt * 128 + (size_t)kq * (12 * 32 * 3072);
  }
  __device__ __forceinline__ int kstep(int i) const { const int t = i + rot; return t >= 12 ? t - 12 : t; }
  __device__ __forceinline__ void issue_at(int i, unsigned lds) const {
    glds4(vb + (size_t)kstep(i) * (32 * 3072), vo[0], vo[1], vo[2], vo[3], lds);
  }
  __device__ __forceinline__ void issue(int i) const { issue_at(i, (kq * 10 + i) * 4096); }
};

template <int NB>
__device__ __forceinline__ void w_body(const Params& p, __amdgpu_buffer_rsrc_t wsr, const VLoader& vl, int wave, int lane, int et, int gt, u64 tag) {
  constexpr int M0 = NB == 3 ? 0 : 3, SEC = NB == 3 ? 0 : 1;
  const int kq = wave & 3;
  unsigned va0[4];
  {
    const int g = lane >> 4, qq = (lane & 15) >> 2, sw = ((qq >> 1) & 1) | ((g & 1) << 1);
#pragma unroll
    for (int c = 0; c < 4; ++c) va0[c] = kq * 40960 + 128 * (8 * g + qq) + 32 * (c ^ sw) + 8 * (lane & 3);
  }
  const unsigned ab = WS_WQ + ((et * 48 + kq * 12) * 5 + M0) * 1024;
  u32x4 af[2][NB];
  f32x4 acc[NB][4];
#pragma unroll
  for (int m = 0; m < NB; ++m)
#pragma unroll
    for (int c = 0; c < 4; ++c) acc[m][c] = (f32x4){0.f, 0.f, 0.f, 0.f};
  s16x4 bh[2][8];
  const char* wq = p.ws;
  const unsigned vl16 = lane * 16;
#pragma unroll
  for (int m = 0; m < NB; ++m)
#pragma unroll
    for (int c = 0; c < 4; ++c) asm volatile("" : "+v"(acc[m][c]));
  BAR();
  STAMP(5);
  static_for<0, 2 * NB>([&](auto ic) {
    constexpr int n = decltype(ic)::value / NB, m = decltype(ic)::value % NB;
    gload_sc1<m * 1024>(af[n][m], wq + (ab + vl.kstep(n) * 5120), vl16);
  });
#pragma unroll
  for (int j = 0; j < 8; ++j) bh[0][j] = __builtin_amdgcn_ds_read_tr16_b64_v4i16((LAS s16x4*)(size_t)(va0[j >> 1] + (j & 1) * 512));
  __builtin_amdgcn_sched_barrier(0);
  unsigned vc[4] = {va0[0], va0[1], va0[2], va0[3]};
  const unsigned red = 81920 + kq * 20480 + lane * 16;
  const unsigned vlast = (kq & 1) * 40960 + (kq >> 1) * 8192;
  auto iter = [&](int it, auto modec) {
    constexpr int MODE = decltype(modec)::value;
    const unsigned step = it == 4 ? vlast - kq * 40960 - 32768 : 8192;
    unsigned vn[4];
#pragma unroll
    for (int c = 0; c < 4; ++c) { vn[c] = vc[c] + step; opaque(vn[c]); }
    if constexpr (MODE == 1) { BAR(); vl.issue_at(10 + SEC, vlast + SEC * 4096); }
    static_for<0, 2>([&](auto hc) {
      constexpr int hf = decltype(hc)::value;
      if constexpr (hf == 1 && MODE == 0) { if (it == 4) BAR(); }
      const unsigned so = ab + vl.kstep(MODE == 2 ? 0 : 2 * it + 2 + hf) * 5120;
      static_for<0, NB * 4>([&](auto jc) {
        constexpr int j = decltype(jc)::value, m = j / 4, c = j % 4;
        if constexpr (c == 0) {
          constexpr int N = MODE == 1 ? 2 * NB + 3 : MODE == 0 ? 2 * NB - 1 : 2 * NB - 1 - (hf * NB + m);
          wait_for<N>(af[hf][m]);
        }
        const bf16x8 bv = __builtin_bit_cast(bf16x8, __builtin_shufflevector(bh[hf][2 * c], bh[hf][2 * c + 1], 0, 1, 2, 3, 4, 5, 6, 7));
        acc[m][c] = __builtin_amdgcn_mfma_f32_16x16x32_bf16(__builtin_bit_cast(bf16x8, af[hf][m]), bv, acc[m][c], 0, 0, 0);
        __builtin_amdgcn_sched_barrier(0);
        if constexpr (j < 8 && !(MODE == 2 && hf == 1))
          bh[hf ^ 1][j] = __builtin_amdgcn_ds_read_tr16_b64_v4i16((LAS s16x4*)(size_t)(hf == 0 ? vc[j >> 1] + 4096 + (j & 1) * 512 : vn[j >> 1] + (j & 1) * 512));
        if constexpr (EDUMP && MODE == 2 && hf == 1 && m > 0) lds_wr128(red + ((M0 + m - 1) * 4 + c) * 1024, __builtin_bit_cast(u32x4, acc[m - 1][c]));
        if constexpr (MODE != 2 && c == 3) gload_sc1<m * 1024>(af[hf][m], wq + so, vl16);
        __builtin_amdgcn_sched_barrier(0);
      });
    });
#pragma unroll
    for (int c = 0; c < 4; ++c) vc[c] = vn[c];
  };
#pragma unroll 1
  for (int it = 0; it < 2; ++it) iter(it, IC<0>{});
  iter(2, IC<1>{});
#pragma unroll 1
  for (int it = 3; it < 5; ++it) iter(it, IC<0>{});
  iter(5, IC<2>{});
  STAMP(6);
#pragma unroll
  for (int m = EDUMP ? NB - 1 : 0; m < NB; ++m)
#pragma unroll
    for (int c = 0; c < 4; ++c) lds_wr128(red + ((M0 + m) * 4 + c) * 1024, __builtin_bit_cast(u32x4, acc[m][c]));
  if constexpr (NB == 2) lds_wr128(LW, (u32x4){0u, 0u, 0u, 0u});
  WAIT_LGKM0();
  BAR();
  if constexpr (NB == 3) {
    __builtin_amdgcn_s_setprio(3);
    STAMP(10);
    const unsigned ol = ((lane & 15) + 16 * (lane >> 5) + 32 * ((lane >> 4) & 1)) * 16;
    f32x4 t[3][2][4];
#pragma unroll
    for (int j = 0; j < 3; ++j) {
      const int fi = kq + (j == 2 && kq >= 2 ? 4 : 4 * j);
      const unsigned rb = 81920 + lane * 16 + fi * 2048;
#pragma unroll
      for (int hf = 0; hf < 2; ++hf)
#pragma unroll
        for (int q = 0; q < 4; ++q) t[j][hf][q] = __builtin_bit_cast(f32x4, lds_rd128(rb + q * 20480 + hf * 1024));
    }
#pragma unroll
    for (int j = 0; j < 3; ++j) {
      const int fi = kq + (j == 2 && kq >= 2 ? 4 : 4 * j);
      const f32x4 a = (t[j][0][0] + t[j][0][1]) + (t[j][0][2] + t[j][0][3]), b = (t[j][1][0] + t[j][1][1]) + (t[j][1][2] + t[j][1][3]);
      auto r0 = __builtin_amdgcn_permlane32_swap(pack_bf16(a[0], a[1]), pack_bf16(b[0], b[1]), false, false);
      auto r1 = __builtin_amdgcn_permlane32_swap(pack_bf16(a[2], a[3]), pack_bf16(b[2], b[3]), false, false);
      const u32x4 v = {r0[0], r1[0], r0[1], r1[1]};
      __builtin_amdgcn_raw_buffer_store_b128(v, wsr, ol, WS_W2 + ((2 * gt + (fi & 1)) * 40 + 5 * et + (fi >> 1)) * 1024, WAUX);
    }
    if (SEAM == 1) BAR();
    STAMP(7);
    WAIT_VM(0);
    unsigned old = 0;
    if (lane == 0) old = __hip_atomic_fetch_add((lu32*)(size_t)LW, 1u, __ATOMIC_RELAXED, __HIP_MEMORY_SCOPE_WORKGROUP);
    if (__builtin_amdgcn_readfirstlane(old) == 3) publish(p.ws, wf_off(gt * 8 + et), tag, lane);
    FENCE();
    if (SEAM == 2) BAR();
  }
}

constexpr int XREG = XREGS;
constexpr int YT = 40 - KS0;
constexpr int y_ks(int n) { return n < 3 * KS0 ? n / 3 : KS0 + (n - 3 * KS0) % YT; }
constexpr int y_tt(int n) { return n < 3 * KS0 ? n % 3 : (n - 3 * KS0) / YT; }
constexpr int y_lds(int n) { return n < 3 * KS0 ? ((n / 12) * 3 + n % 3) * 4096 + ((n % 12) / 3) * 1024 : n * 1024; }
constexpr int y_t(int n, int g) { return n < 3 * KS0 ? g % 3 : y_tt(n); }
constexpr int y_ks0(int g) { return 4 * g < 3 * KS0 ? (g / 3) * 4 : y_ks(4 * g); }
__device__ __forceinline__ void y_load(const Params& p, int wave, int lane, int st, int gt, int rot, u64 tag, bool isx) {
  const int lw = wave - 4;
  STAMP(8);
  if (lw == 0) {
    if (XWAIT && isx) lds_wait(LW + 12);
    else if (YDELAY > 0) __builtin_amdgcn_s_sleep(YDELAY);
    wait_flags(p.ws, wf_off((3 * gt / 2) * 8 + (lane & 15)), tag);
    lds_set(LW + 4);
  } else lds_wait(LW + 4);
  if (blockIdx.x == 0) {
    const int w = lw * 64 + lane;
    wait_flags(p.ws, w < 64 ? xf_off(w >> 2, w & 3) : wf_off(w - 64), tag);
  }
  STAMP(11);
  const char* wb = p.ws + WS_W2 + (size_t)(3 * gt * 40 + lw) * 1024;
  const char* xb = p.ws + WS_XR + (size_t)(st * 4 + lw) * 40960;
  const unsigned vo = lane * 16;
  auto issue = [&](auto cc) {
    static_for<3 * decltype(cc)::value, 3 * decltype(cc)::value + 3>([&](auto gc) {
      constexpr int g = decltype(gc)::value;
      glds<0, true>(wb + (y_t(4 * g, g) * 40 + y_ks0(g)) * 1024, vo, g * 4096 + lw * 1024);
    });
  };
  issue(IC<0>{}); issue(IC<1>{}); issue(IC<2>{});
  WAIT_VM(6); BAR();
  if (blockIdx.x == 0 && threadIdx.x == 256) __hip_atomic_store((gu64*)(p.ws + WS_EPOCH), tag, RLX_AGENT);
  issue(IC<3>{}); issue(IC<4>{});
  WAIT_VM(9); BAR();
  const unsigned xl = 122880 + lw * 10240;
  if constexpr (XREG == 30) {
    glds<0, true>(xb + 30 * 1024, vo, xl); glds<1024, true>(xb + 30 * 1024, vo, xl);
    glds4_lin_sc1(xb + 32 * 1024, vo, xl + 2048);
    glds4_lin_sc1(xb + 36 * 1024, vo, xl + 6144);
  }
  issue(IC<5>{});
  WAIT_VM(6 + 40 - XREG); BAR();
  issue(IC<6>{}); issue(IC<7>{});
  WAIT_VM(6); BAR();
  issue(IC<8>{}); issue(IC<9>{});
  WAIT_VM(6); BAR();
  WAIT_VM(0); BAR();
}

__device__ __forceinline__ void y_compute(const Params& p, __amdgpu_buffer_rsrc_t wsr, int wave, int lane, int st, int gt, int rot, u64 tag, bool b4) {
  STAMP(8);
  if (wave == 0) { if (XDELAY > 0 && !b4) __builtin_amdgcn_s_sleep(XDELAY); wait_flags(p.ws, xf_off(st, lane & 3), tag); lds_set(LW + 8); } else lds_wait(LW + 8);
  if (SEAM > 0 && b4) BAR();
  STAMP(9);
  u32x4 xf[XREG];
  const unsigned xbase = WS_XR + (st * 4 + wave) * 40 * 1024;
#pragma unroll
  for (int ks = 0; ks < XREG; ++ks) {
    if (XSPLIT > 0 && ks == XSPLIT) { if (b4) { lds_wait(LW + 4); if (XSLEEP > 0) __builtin_amdgcn_s_sleep(XSLEEP); } }
    xf[ks] = __builtin_amdgcn_raw_buffer_load_b128(wsr, lane * 16 + (ks & 3) * 1024, xbase + (ks >> 2) * 4096, 16);
  }
  FENCE();
  if (XWAIT && wave == 3) lds_set(LW + 12);
  STAMP(10);
  f32x16 acc[3];
#pragma unroll
  for (int t = 0; t < 3; ++t)
#pragma unroll
    for (int j = 0; j < 16; ++j) acc[t][j] = 0.f;
  {
    constexpr int L = 6;
    bf16x8 aw[L + 1], bx[10];
    const unsigned a0 = lane * 16, x0 = 122880 + wave * 10240 + lane * 16;
    unsigned a1 = lane * 16 + 61440;
    opaque(a1);
    typedef unsigned u32x2 __attribute__((ext_vector_type(2)));
    const __amdgpu_buffer_rsrc_t yr = __builtin_amdgcn_make_buffer_rsrc(p.y, 0, S_ * F_ * 2, 0x00020000);
    constexpr unsigned EB = XREG == 40 ? 122880 : 0;
    const unsigned ew = EB + wave * 8192 + (lane & 31) * 80 + (lane >> 5) * 8, er = EB + wave * 8192 + (lane >> 2) * 80 + (lane & 3) * 16;
    const unsigned yo = (lane >> 2) * 3072 + (lane & 3) * 16, ys = (st * 128 + wave * 32) * 3072 + gt * 192;
    u32x4 eo[2];
    auto epi = [&](auto tc, auto sc) {
      constexpr int t = decltype(tc)::value, e = decltype(sc)::value;
      if constexpr (e < 2) {
#pragma unroll
        for (int i = 2 * e; i < 2 * e + 2; ++i) {
          const u32x2 v = {pack_bf16(acc[t][4 * i], acc[t][4 * i + 1]), pack_bf16(acc[t][4 * i + 2], acc[t][4 * i + 3])};
          *(LAS u32x2*)(size_t)(ew + t * 2560 + i * 16) = v;
        }
        if constexpr (e == 1) { eo[0] = lds_rd128(er + t * 2560); eo[1] = lds_rd128(er + t * 2560 + 1280); }
      } else if constexpr (e >= 5) {
        __builtin_amdgcn_raw_buffer_store_b128(eo[e - 5], yr, yo + (e - 5) * (16 * 3072) + t * 64, ys, YAUX);
      }
    };
    static_for<0, 120 + L>([&](auto ic) {
      constexpr int n = decltype(ic)::value, j = n - L;
      if constexpr (n < 120) {
        if constexpr (n == 0 || n == 12 || n == 24 || n == 48 || n == 72 || n == 96) {
          BAR();
          if constexpr (n == 0) STAMP(11);
        }
        if constexpr (y_tt(n) == 0 && y_ks(n) >= XREG) bx[y_ks(n) - XREG] = __builtin_bit_cast(bf16x8, lds_rd128(x0 + (y_ks(n) - XREG) * 1024));
        aw[n % (L + 1)] = __builtin_bit_cast(bf16x8, lds_rd128(y_lds(n) < 61440 ? a0 + y_lds(n) : a1 + (y_lds(n) - 61440)));
      }
      if constexpr (j >= 0) {
        constexpr int ks = y_ks(j), t = y_tt(j);
        if constexpr (ks < XREG) acc[t] = __builtin_amdgcn_mfma_f32_32x32x16_bf16(aw[j % (L + 1)], __builtin_bit_cast(bf16x8, xf[ks]), acc[t], 0, 0, 0);
        else acc[t] = __builtin_amdgcn_mfma_f32_32x32x16_bf16(aw[j % (L + 1)], bx[ks - XREG], acc[t], 0, 0, 0);
        __builtin_amdgcn_sched_barrier(0);
        if constexpr (j >= 3 * KS0 && t >= 1 && ks - KS0 >= 1 && ks - KS0 <= 7) epi(IC<t - 1>{}, IC<ks - KS0 - 1>{});
      }
      __builtin_amdgcn_sched_barrier(0);
    });
    STAMP(12);
    static_for<0, 7>([&](auto sc) { epi(IC<2>{}, sc); });
  }
}

__global__ void __launch_bounds__(512, 2) quip_kernel(Params p) {
  const int wave = __builtin_amdgcn_readfirstlane(threadIdx.x >> 6), lane = threadIdx.x & 63;
  const int xcd = blockIdx.x & 7, idx = blockIdx.x >> 3;
  STAMP(0); STAMPC(14);
  unsigned pf = 0;
  if (CODEKB > 0) {
    const unsigned ln = PFMAP ? (31 - idx) * 8 + wave : idx * 8 + wave;
    const GAS unsigned* code = (const GAS unsigned*)((__builtin_amdgcn_s_getpc() & ~255ull) + (ln < CODEKB * 8 ? ln : 0) * 128);
    if (ln < CODEKB * 8) pf = __hip_atomic_load(code, __ATOMIC_RELAXED, __HIP_MEMORY_SCOPE_WORKGROUP);
  }
  const bool isx = idx < 8;
  const int xst = 4 * (xcd >> 1) + (idx >> 1), etl = 2 * (xcd & 1) + (idx & 1);
  const int wk = idx - 8;
  const int et = wk & 7, gt = 3 * xcd + (wk >> 3);
  const int rotc = (3 * et >> 1) + ROTC * 4 * (wk >> 3);
  const int dj = xcd * 24 + wk, det = xcd, dkp = wk;
  const int rotx = ROTX * ((idx >> 1) * 2 + (idx & 1) * 5) % 10;
  XLoader xl; VLoader vl;
  unsigned pf2 = 0, pf3 = 0;
  if (!isx && wave < 4) { if (DPRIO) __builtin_amdgcn_s_setprio(3); d_issue(p, wave, lane, det, dkp); }
  if (isx && wave >= 4) { xl.init(p, xst, etl, wave - 4, lane, rotx); xl.issue(0); xl.issue(1); }
  vl.init(p, gt, wave & 3, rotc >= 12 ? rotc - 12 : rotc, lane);
  if (!isx && wave >= 4) {
    const unsigned w96 = wk * 4 + wave - 4;
    if (PFX == 1 || PFX == 3) {
      const unsigned n = w96 * 35 + (lane < 35 ? lane : 0), lu = n - 2560, ur = (lu * 13108) >> 16;
      const char* a = n < 2560 ? p.x + (size_t)xcd * (256 * 1280) + n * 128
                               : p.U + (xcd & 1) * 640 + ((xcd >> 1) * 160 + ur) * 1280 + (lu - ur * 5) * 128;
      if (PFX == 3) { vl.issue(0); vl.issue(1); if (!WMAP) vl.issue(2); }
      FENCE();
      pf2 = __hip_atomic_load((const GAS unsigned*)a, __ATOMIC_RELAXED, __HIP_MEMORY_SCOPE_WORKGROUP);
      FENCE();
    } else if (PFX == 4) {
      const unsigned n = w96 * 28 + (lane < 28 ? lane : 0), lu = n - 2048, ur = (lu * 13108) >> 16;
      const char* a = n < 2048 ? p.x + (size_t)(xcd * 256 + (n >> 3)) * 1280 + (2 + (n & 7)) * 128
                               : p.U + (xcd & 1) * 640 + (128 + ur * 4 + (xcd >> 1)) * 1280 + (lu - ur * 5) * 128;
      vl.issue(0); vl.issue(1);
      FENCE();
      pf2 = __hip_atomic_load((const GAS unsigned*)a, __ATOMIC_RELAXED, __HIP_MEMORY_SCOPE_WORKGROUP);
      FENCE();
    } else if (PFX == 2) {
      const unsigned n = w96 * 54 + (lane < 54 ? lane : 0), lu = w96 * 34 + (lane < 34 ? lane : 0), ur = (lu * 13108) >> 16;
      FENCE();
      pf2 = __hip_atomic_load((const GAS unsigned*)(p.x + (size_t)(xcd >> 1) * (512 * 1280) + (n < 5120 ? n : 0) * 128), __ATOMIC_RELAXED, __HIP_MEMORY_SCOPE_WORKGROUP);
      pf3 = __hip_atomic_load((const GAS unsigned*)(p.U + (xcd & 1) * 640 + (ur < 640 ? ur : 0) * 1280 + (lu - ur * 5) * 128), __ATOMIC_RELAXED, __HIP_MEMORY_SCOPE_WORKGROUP);
      FENCE();
    }
    if (PFX < 3) { vl.issue(0); vl.issue(1); }
  }
  const __amdgpu_buffer_rsrc_t wsr = __builtin_amdgcn_make_buffer_rsrc(p.ws, 0, WS_END, 0x00020000);
  u64 tag = 0;
  const int yst = 4 * (xcd >> 1) + (idx & 3), ygt = 8 * (xcd & 1) + (idx >> 2);
  if (wave < 4) {
    if (isx) {
      x_compute(p, wsr, wave, lane, xst, etl, tag, idx == 0 && wave == 0);
      asm volatile("" :: "v"(pf));
    } else {
      WAIT_VM(0);
      const u64 ev = load_tag_v(p);
      STAMP(1);
      d_compute(p, wsr, wave, lane, det, dkp);
      STAMP(2);
      WAIT_VM(0);
      tag = make_tag_v(ev);
      asm volatile("" :: "v"(pf), "s"(tag));
      BAR();
      if (wave == 0) publish(p.ws, WS_DF + dj * FS, tag, lane);
      STAMP(3);
      if (DPRIO) __builtin_amdgcn_s_setprio(0);
      w_body<2>(p, wsr, vl, wave, lane, et, gt, tag);
    }
    y_compute(p, wsr, wave, lane, yst, ygt, 0, tag, !isx);
  } else {
    if (isx) {
      u64 ts[10];
      xl.run(p, tag, ts);
      asm volatile("" :: "v"(pf));
      __builtin_amdgcn_s_setprio(3);
    } else {
      BAR();
      tag = load_tag(p);
      STAMP(3);
#pragma unroll 1
      for (int i = WMAP ? 2 : 3; i < 10; ++i) vl.issue(i);
      make_tag(tag);
      if (wave == 4) wait_flags(p.ws, WS_DF + (et * 24 + (lane < 24 ? lane : 0)) * FS, tag);
      WAIT_VM(0);
      asm volatile("" :: "v"(pf), "v"(pf2), "v"(pf3));
      STAMP(4);
      __builtin_amdgcn_s_setprio(WPRIO);
      w_body<3>(p, wsr, vl, wave, lane, et, gt, tag);
    }
    y_load(p, wave, lane, yst, ygt, 0, tag, isx);
  }
  STAMP(13); STAMPC(15);
}

extern "C" void kernel_launch(void* const* d_in, const int* in_sizes, int n_in, void* d_out, int out_size, void* d_ws,
                              size_t ws_size, hipStream_t stream) {
  Params p;
  p.x = (const char*)d_in[0]; p.qw = (const char*)d_in[1]; p.sc = (const char*)d_in[2]; p.zr = (const char*)d_in[3];
  p.U = (const char*)d_in[4]; p.V = (const char*)d_in[5]; p.y = (char*)d_out; p.ws = (char*)d_ws + (WSPOS == 0 ? 0 : ((ws_size - WS_END) / 4 * WSPOS) & ~(size_t)0x1fffff);
  (void)hipFuncSetAttribute((const void*)quip_kernel, hipFuncAttributeMaxDynamicSharedMemorySize, LDS_BYTES);
  quip_kernel<<<256, 512, LDS_BYTES, stream>>>(p);
}
```

```cpp
#include <hip/hip_runtime.h>
#include <stdint.h>

#define LAS __attribute__((address_space(3)))
#define GAS __attribute__((address_space(1)))
typedef unsigned short u16;
typedef unsigned long long u64;
typedef short s16x4 __attribute__((ext_vector_type(4)));
typedef __bf16 bf16x8 __attribute__((ext_vector_type(8)));
typedef __bf16 bf16x2 __attribute__((ext_vector_type(2)));
typedef float f32x2 __attribute__((ext_vector_type(2)));
typedef float f32x4 __attribute__((ext_vector_type(4)));
typedef float f32x16 __attribute__((ext_vector_type(16)));
typedef unsigned u32x2 __attribute__((ext_vector_type(2)));
typedef unsigned u32x4 __attribute__((ext_vector_type(4)));
typedef int i32x4 __attribute__((ext_vector_type(4)));
typedef GAS u64 gu64;
typedef LAS unsigned lu32;

constexpr int S_ = 2048, D_ = 640, F_ = 1536;
constexpr unsigned WS_EPOCH = 0;
constexpr unsigned FS = 128;
constexpr unsigned WS_DF = 4096;
constexpr unsigned WS_XF = WS_DF + 192 * FS;
constexpr unsigned WS_WF = WS_XF + 64 * FS;
constexpr unsigned WS_WQ = 1u << 20;
constexpr unsigned WS_XR = WS_WQ + D_ * F_ * 2;
constexpr unsigned WS_W2 = WS_XR + S_ * D_ * 2;
constexpr unsigned WS_END = WS_W2 + D_ * F_ * 2;
constexpr int LDS_BYTES = 160 * 1024;
constexpr unsigned LW = 79 * 1024;
constexpr int AUX_SC1 = 16, AUX_NT_SC1 = 18;
#define CODEKB 16
#define LASTAUX 18
#define YDELAY 12

struct Params {
  const char* x; const char* qw; const char* sc; const char* zr; const char* U; const char* V; char* y; char* ws;
};

#define RLX_AGENT __ATOMIC_RELAXED, __HIP_MEMORY_SCOPE_AGENT
#define RLX_WG __ATOMIC_RELAXED, __HIP_MEMORY_SCOPE_WORKGROUP
#define WAIT_VM(n) asm volatile("s_waitcnt vmcnt(%0)" :: "n"(n) : "memory")
#define WAIT_LGKM0() asm volatile("s_waitcnt lgkmcnt(0)" ::: "memory")
#define FENCE() asm volatile("" ::: "memory")
#define BAR() do { FENCE(); __builtin_amdgcn_s_barrier(); FENCE(); } while (0)
#define SCHED() __builtin_amdgcn_sched_barrier(0)

template <int I> struct IC { static constexpr int value = I; };
template <int B, int E, class Fn>
__device__ __forceinline__ void static_for(Fn&& fn) {
  if constexpr (B < E) {
    if constexpr (E - B == 1) fn(IC<B>{});
    else { static_for<B, (B + E) / 2>(fn); static_for<(B + E) / 2, E>(fn); }
  }
}

template <int OFF, bool SC1>
__device__ __forceinline__ void glds(const char* sbase, unsigned voff, unsigned lds) {
  if constexpr (SC1)
    asm volatile("s_mov_b32 m0, %2\n\ts_nop 0\n\tglobal_load_lds_dwordx4 %0, %1 offset:%3 sc1" :: "v"(voff), "s"(sbase), "s"(lds), "n"(OFF) : "memory");
  else
    asm volatile("s_mov_b32 m0, %2\n\ts_nop 0\n\tglobal_load_lds_dwordx4 %0, %1 offset:%3" :: "v"(voff), "s"(sbase), "s"(lds), "n"(OFF) : "memory");
}
__device__ __forceinline__ void glds4(const char* sbase, unsigned v0, unsigned v1, unsigned v2, unsigned v3, unsigned lds) {
  asm volatile("s_mov_b32 m0, %5\n\ts_nop 0\n\tglobal_load_lds_dwordx4 %0, %4\n\tglobal_load_lds_dwordx4 %1, %4 offset:1024\n\t"
               "global_load_lds_dwordx4 %2, %4 offset:2048\n\tglobal_load_lds_dwordx4 %3, %4 offset:3072"
               :: "v"(v0), "v"(v1), "v"(v2), "v"(v3), "s"(sbase), "s"(lds) : "memory");
}
__device__ __forceinline__ void glds4_lin_sc1(const char* sbase, unsigned v, unsigned lds) {
  asm volatile("s_mov_b32 m0, %2\n\ts_nop 0\n\tglobal_load_lds_dwordx4 %0, %1 sc1\n\tglobal_load_lds_dwordx4 %0, %1 offset:1024 sc1\n\t"
               "global_load_lds_dwordx4 %0, %1 offset:2048 sc1\n\tglobal_load_lds_dwordx4 %0, %1 offset:3072 sc1"
               :: "v"(v), "s"(sbase), "s"(lds) : "memory");
}
__device__ __forceinline__ void glds_v(const char* vaddr, unsigned lds) {
  asm volatile("s_mov_b32 m0, %1\n\ts_nop 0\n\tglobal_load_lds_dwordx4 %0, off" :: "v"(vaddr), "s"(lds) : "memory");
}
template <int OFF>
__device__ __forceinline__ void gload_sc1(u32x4& d, const char* sbase, unsigned voff) {
  asm volatile("global_load_dwordx4 %0, %1, %2 offset:%3 sc1" : "=v"(d) : "v"(voff), "s"(sbase), "n"(OFF) : "memory");
}
template <int N>
__device__ __forceinline__ void wait_for(u32x4& d) { asm volatile("s_waitcnt vmcnt(%1)" : "+v"(d) : "n"(N) : "memory"); }
__device__ __forceinline__ void opaque(unsigned& v) { asm volatile("" : "+v"(v)); }

__device__ __forceinline__ unsigned pack_bf16(float a, float b) {
  f32x2 v = {a, b};
  return __builtin_bit_cast(unsigned, __builtin_convertvector(v, bf16x2));
}
__device__ __forceinline__ float bf_lo(unsigned v) { return __builtin_bit_cast(float, v << 16); }
__device__ __forceinline__ float bf_hi(unsigned v) { return __builtin_bit_cast(float, v & 0xffff0000u); }

__device__ __forceinline__ u32x4 lds_rd128(unsigned a) { return *(LAS u32x4*)(size_t)a; }
__device__ __forceinline__ void lds_wr128(unsigned a, u32x4 v) { *(LAS u32x4*)(size_t)a = v; }
__device__ __forceinline__ s16x4 lds_tr(unsigned a) { return __builtin_amdgcn_ds_read_tr16_b64_v4i16((LAS s16x4*)(size_t)a); }
__device__ __forceinline__ bf16x8 join(s16x4 lo, s16x4 hi) { return __builtin_bit_cast(bf16x8, __builtin_shufflevector(lo, hi, 0, 1, 2, 3, 4, 5, 6, 7)); }

constexpr u64 TAG_STEP = 0x9E3779B97F4A7C15ull;
__device__ __forceinline__ u64 load_epoch_s(const Params& p) {
  const char* e = p.ws + WS_EPOCH;
  asm volatile("" : "+s"(e));
  return *(const __attribute__((address_space(4))) u64*)e;
}
__device__ __forceinline__ void make_tag_s(u64& e) {
  asm volatile("" : "+s"(e));
  e += TAG_STEP;
  asm volatile("" : "+s"(e));
}
__device__ __forceinline__ u64 load_epoch_v(const Params& p) { FENCE(); const u64 e = __hip_atomic_load((gu64*)(p.ws + WS_EPOCH), RLX_AGENT); FENCE(); return e; }
__device__ __forceinline__ u64 make_tag_v(u64 e) {
  asm volatile("" : "+v"(e));
  u64 u = ((u64)(unsigned)__builtin_amdgcn_readfirstlane((unsigned)(e >> 32)) << 32) | (unsigned)__builtin_amdgcn_readfirstlane((unsigned)e);
  u += TAG_STEP;
  asm volatile("" : "+s"(u));
  return u;
}

__device__ __forceinline__ void wait_flags(const char* ws, unsigned off, u64 tag) {
  gu64* f = (gu64*)(ws + off);
#pragma unroll 1
  for (unsigned spins = 0; spins < (1u << 20); ++spins) {
    const u64 v = __hip_atomic_load(f, RLX_AGENT);
    if (__all(v == tag)) break;
    __builtin_amdgcn_s_sleep(2);
  }
  FENCE();
}
__device__ __forceinline__ void raise_flag(char* ws, unsigned off, u64 tag, int lane) {
  FENCE();
  if (lane < 16) __hip_atomic_store((gu64*)(ws + off + lane * 8), tag, RLX_AGENT);
  FENCE();
}
__device__ __forceinline__ void lds_set(unsigned a) { FENCE(); __hip_atomic_store((lu32*)(size_t)a, 1u, RLX_WG); FENCE(); }
__device__ __forceinline__ void lds_wait(unsigned a) {
#pragma unroll 1
  for (unsigned spins = 0; spins < (1u << 24); ++spins) {
    if (__builtin_amdgcn_readfirstlane(__hip_atomic_load((lu32*)(size_t)a, RLX_WG))) break;
    __builtin_amdgcn_s_sleep(1);
  }
  FENCE();
}

__device__ __forceinline__ unsigned dequant2(int q0, int q1, float z0, float z1, float s0, float s1) {
  const unsigned qb = pack_bf16((float)q0, (float)q1);
  const unsigned t = pack_bf16(bf_lo(qb) - z0, bf_hi(qb) - z1);
  return pack_bf16(bf_lo(t) * s0, bf_hi(t) * s1);
}

constexpr unsigned XSTG = 36 * 1024, XUOFF = 16 * 1024;
struct XLoader {
  unsigned xo[4], uo[5]; const char* xb; const char* ub; int lw;
  __device__ __forceinline__ void init(const Params& p, int st, int etl, int lw_, int lane) {
    lw = lw_;
#pragma unroll
    for (int i = 0; i < 4; ++i) {
      const int row = 8 * i + (lane >> 3);
      xo[i] = row * 1280 + (((lane & 7) ^ ((row >> 1) & 7)) << 4) - i * 1024;
    }
#pragma unroll
    for (int i = 0; i < 5; ++i) {
      const int u = i * 64 + lane, row = (u * 3277) >> 16;
      uo[i] = row * 960 + u * 16 - (i & 3) * 1024;
    }
    xb = p.x + (size_t)(st * 128 + lw * 32) * 1280;
    ub = p.U + etl * 320 + lw * (16 * 1280);
  }
  __device__ __forceinline__ void issue(int i) {
    const unsigned slot = (i & 3) * XSTG;
    const char* us = ub + (size_t)i * (64 * 1280);
    glds4(us, uo[0], uo[1], uo[2], uo[3], slot + XUOFF + lw * 5120);
    glds<0, false>(us, uo[4], slot + XUOFF + lw * 5120 + 4096);
    glds4(xb + i * 128, xo[0], xo[1], xo[2], xo[3], slot + lw * 4096);
  }
  __device__ __forceinline__ u64 run(const Params& p) {
    WAIT_VM(9); BAR();
    u64 tag = load_epoch_s(p);
#pragma unroll 1
    for (int i = 1; i < 9; ++i) {
      issue(i + 1);
      WAIT_VM(9); BAR();
    }
    make_tag_s(tag);
    WAIT_VM(0); BAR();
    lds_wr128(LW, (u32x4){0u, 0u, 0u, 0u});
    WAIT_LGKM0();
    BAR();
    BAR();
    return tag;
  }
};

__device__ __forceinline__ u64 x_compute(const Params& p, __amdgpu_buffer_rsrc_t wsr, int wave, int lane, int st, int etl, bool first) {
  const int r = lane & 31, h = lane >> 5;
  unsigned xa0[4];
#pragma unroll
  for (int kk = 0; kk < 4; ++kk) xa0[kk] = wave * 4096 + r * 128 + (((2 * kk + h) ^ ((r >> 1) & 7)) << 4);
  const unsigned ua0 = XUOFF + (8 * h + ((lane & 15) >> 2)) * 320 + ((lane >> 4) & 1) * 32 + (lane & 3) * 8;
  f32x16 acc[5];
#pragma unroll
  for (int t = 0; t < 5; ++t)
#pragma unroll
    for (int j = 0; j < 16; ++j) acc[t][j] = 0.f;
  constexpr int L = 6;
  bf16x8 au[10], bx[4];
#pragma unroll
  for (int t = 0; t < 5; ++t) asm volatile("" : "+v"(acc[t]));
  u64 ev = 0;
  if (first) ev = load_epoch_v(p);
  BAR();
  if (!first) ev = load_epoch_v(p);
#pragma unroll
  for (int n = 0; n < L; ++n) {
    if (n % 5 == 0) bx[n / 5] = __builtin_bit_cast(bf16x8, lds_rd128(xa0[n / 5]));
    SCHED();
    const unsigned ad = ua0 + (n / 5) * 16 * 320 + (n % 5) * 64;
    au[n] = join(lds_tr(ad), lds_tr(ad + 4 * 320));
    SCHED();
  }
#pragma unroll 1
  for (int i = 0; i < 10; ++i) {
    const unsigned cur = (i & 3) * XSTG, nxt = ((i + 1) & 3) * XSTG;
    const unsigned ua = ua0 + cur, un = ua0 + nxt;
    static_for<0, 20>([&](auto jc) {
      constexpr int j = decltype(jc)::value, n = j + L;
      if constexpr (n == 20) BAR();
      constexpr int kk = (n % 20) / 5, t = n % 5;
      if constexpr (t == 0) { bx[kk] = __builtin_bit_cast(bf16x8, lds_rd128(xa0[kk] + (n < 20 ? cur : nxt))); SCHED(); }
      const unsigned ad = (n < 20 ? ua : un) + kk * 16 * 320 + t * 64;
      au[n % 10] = join(lds_tr(ad), lds_tr(ad + 4 * 320));
      acc[j % 5] = __builtin_amdgcn_mfma_f32_32x32x16_bf16(au[j % 10], bx[j / 5], acc[j % 5], 0, 0, 0);
      SCHED();
    });
  }
  const unsigned ob = WS_XR + ((st * 4 + wave) * 40 + etl * 10) * 1024;
#pragma unroll
  for (int t = 0; t < 5; ++t)
#pragma unroll
    for (int s2 = 0; s2 < 2; ++s2) {
      u32x4 v;
#pragma unroll
      for (int j = 0; j < 4; ++j) v[j] = pack_bf16(acc[t][8 * s2 + 2 * j], acc[t][8 * s2 + 2 * j + 1]);
      __builtin_amdgcn_raw_buffer_store_b128(v, wsr, lane * 16, ob + (2 * t + s2) * 1024, AUX_SC1);
    }
  WAIT_LGKM0();
  WAIT_VM(0);
  const u64 tag = make_tag_v(ev);
  BAR();
  if (wave == 0) raise_flag(p.ws, WS_XF + (st * 4 + etl) * FS, tag, lane);
  return tag;
}

__device__ __forceinline__ void d_issue(const Params& p, int wave, int lane, int det, int dkp) {
  const unsigned dl = (wave * 10 + 8) * 4096;
  const int k2 = wave & 1, nfr = wave < 2 ? 3 : 2;
  unsigned qo[2];
#pragma unroll
  for (int i = 0; i < 2; ++i) {
    const int row = 8 * i + (lane >> 3);
    qo[i] = row * 6144 + (((lane & 7) ^ ((row >> 1) & 7)) << 4) - i * 1024;
  }
  const char* qb = p.qw + (size_t)(80 * det + 16 * (wave >> 1)) * 6144 + (64 * dkp + 32 * k2) * 4;
  glds4(qb, qo[0], qo[1], qo[0] + (32 * 6144 - 2048), qo[1] + (32 * 6144 - 2048), dl);
  if (wave < 2) {
    glds<0, false>(qb + 64 * 6144, qo[0], dl + 4096);
    glds<1024, false>(qb + 64 * 6144, qo[1], dl + 4096);
  }
  int i = (lane >> 3) & 3; if (i >= nfr) i = 0;
  const int grp = (5 * det + (wave >> 1) + 2 * i) >> 1;
  const char* sa = (((lane >> 2) & 1) ? p.zr : p.sc) + (size_t)grp * 3072 + (64 * dkp + 32 * k2) * 2 + (lane & 3) * 16;
  glds_v(sa, dl + 6144);
}

__device__ __forceinline__ void d_compute(__amdgpu_buffer_rsrc_t wsr, int wave, int lane, int det, int dkp) {
  const unsigned dl = (wave * 10 + 8) * 4096;
  const int r = lane & 15, q = lane >> 4;
  const unsigned a0 = dl + r * 128 + (((2 * q) ^ ((r >> 1) & 7)) << 4), a1 = dl + r * 128 + (((2 * q + 1) ^ ((r >> 1) & 7)) << 4);
  const unsigned as = dl + 6144 + q * 16;
  const unsigned ob = WS_WQ + ((det * 48 + 2 * dkp + (wave & 1)) * 5 + (wave >> 1)) * 1024;
  const int nfr = wave < 2 ? 3 : 2;
  i32x4 q0 = __builtin_bit_cast(i32x4, lds_rd128(a0)), q1 = __builtin_bit_cast(i32x4, lds_rd128(a1));
  u32x4 s = lds_rd128(as), z = lds_rd128(as + 64);
#pragma unroll 1
  for (int i = 0; i < nfr; ++i) {
    const i32x4 nq0 = __builtin_bit_cast(i32x4, lds_rd128(a0 + i * 2048 + 2048)), nq1 = __builtin_bit_cast(i32x4, lds_rd128(a1 + i * 2048 + 2048));
    const u32x4 ns = lds_rd128(as + i * 128 + 128), nz = lds_rd128(as + i * 128 + 192);
    u32x4 o;
    o[0] = dequant2(q0[0], q0[1], bf_lo(z[0]), bf_hi(z[0]), bf_lo(s[0]), bf_hi(s[0]));
    o[1] = dequant2(q0[2], q0[3], bf_lo(z[1]), bf_hi(z[1]), bf_lo(s[1]), bf_hi(s[1]));
    o[2] = dequant2(q1[0], q1[1], bf_lo(z[2]), bf_hi(z[2]), bf_lo(s[2]), bf_hi(s[2]));
    o[3] = dequant2(q1[2], q1[3], bf_lo(z[3]), bf_hi(z[3]), bf_lo(s[3]), bf_hi(s[3]));
    __builtin_amdgcn_raw_buffer_store_b128(o, wsr, lane * 16, ob + i * 2048, AUX_SC1);
    q0 = nq0; q1 = nq1; s = ns; z = nz;
  }
}

struct VLoader {
  unsigned vo[4]; const char* vb; int rot, kq;
  __device__ __forceinline__ void init(const Params& p, int gt, int kq_, int rot_, int lane) {
    kq = kq_; rot = rot_;
#pragma unroll
    for (int pc = 0; pc < 4; ++pc) {
      const int row = 8 * pc + (lane >> 3), swz = ((row >> 1) & 1) | (((row >> 3) & 1) << 1);
      vo[pc] = row * 3072 + ((((lane & 7) >> 1) ^ swz) << 5) + ((lane & 1) << 4) - pc * 1024;
    }
    vb = p.V + gt * 128 + (size_t)kq * (12 * 32 * 3072);
  }
  __device__ __forceinline__ int kstep(int i) const { const int t = i + rot; return t >= 12 ? t - 12 : t; }
  __device__ __forceinline__ void issue_at(int i, unsigned lds) const {
    glds4(vb + (size_t)kstep(i) * (32 * 3072), vo[0], vo[1], vo[2], vo[3], lds);
  }
  __device__ __forceinline__ void issue(int i) const { issue_at(i, (kq * 10 + i) * 4096); }
};

template <int NB>
__device__ __forceinline__ void w_body(const Params& p, __amdgpu_buffer_rsrc_t wsr, const VLoader& vl, int wave, int lane, int et, int gt) {
  constexpr int M0 = NB == 3 ? 0 : 3, SEC = NB == 3 ? 0 : 1;
  const int kq = wave & 3;
  unsigned vc[4];
  {
    const int g = lane >> 4, qq = (lane & 15) >> 2, sw = ((qq >> 1) & 1) | ((g & 1) << 1);
#pragma unroll
    for (int c = 0; c < 4; ++c) vc[c] = kq * 40960 + 128 * (8 * g + qq) + 32 * (c ^ sw) + 8 * (lane & 3);
  }
  const char* wq = p.ws + WS_WQ + ((et * 48 + kq * 12) * 5 + M0) * 1024;
  const unsigned vl16 = lane * 16;
  u32x4 af[2][NB];
  s16x4 bh[2][8];
  f32x4 acc[NB][4];
#pragma unroll
  for (int m = 0; m < NB; ++m)
#pragma unroll
    for (int c = 0; c < 4; ++c) { acc[m][c] = (f32x4){0.f, 0.f, 0.f, 0.f}; asm volatile("" : "+v"(acc[m][c])); }
  BAR();
  static_for<0, 2 * NB>([&](auto ic) {
    constexpr int n = decltype(ic)::value / NB, m = decltype(ic)::value % NB;
    gload_sc1<m * 1024>(af[n][m], wq + vl.kstep(n) * 5120, vl16);
  });
#pragma unroll
  for (int j = 0; j < 8; ++j) bh[0][j] = lds_tr(vc[j >> 1] + (j & 1) * 512);
  SCHED();
  const unsigned red = 81920 + kq * 20480 + lane * 16;
  const unsigned vlast = (kq & 1) * 40960 + (kq >> 1) * 8192;
  auto iter = [&](int it, auto modec) {
    constexpr int MODE = decltype(modec)::value;
    const unsigned step = it == 4 ? vlast - kq * 40960 - 32768 : 8192;
    unsigned vn[4];
#pragma unroll
    for (int c = 0; c < 4; ++c) { vn[c] = vc[c] + step; opaque(vn[c]); }
    if constexpr (MODE == 1) { BAR(); vl.issue_at(10 + SEC, vlast + SEC * 4096); }
    static_for<0, 2>([&](auto hc) {
      constexpr int hf = decltype(hc)::value;
      if constexpr (hf == 1 && MODE == 0) { if (it == 4) { WAIT_LGKM0(); BAR(); } }
      const char* src = wq + vl.kstep(MODE == 2 ? 0 : 2 * it + 2 + hf) * 5120;
      static_for<0, NB * 4>([&](auto jc) {
        constexpr int j = decltype(jc)::value, m = j / 4, c = j % 4;
        if constexpr (c == 0) {
          constexpr int N = MODE == 1 ? 2 * NB + 3 : MODE == 0 ? 2 * NB - 1 : 2 * NB - 1 - (hf * NB + m);
          wait_for<N>(af[hf][m]);
        }
        acc[m][c] = __builtin_amdgcn_mfma_f32_16x16x32_bf16(__builtin_bit_cast(bf16x8, af[hf][m]), join(bh[hf][2 * c], bh[hf][2 * c + 1]), acc[m][c], 0, 0, 0);
        SCHED();
        if constexpr (j < 8 && !(MODE == 2 && hf == 1))
          bh[hf ^ 1][j] = lds_tr(hf == 0 ? vc[j >> 1] + 4096 + (j & 1) * 512 : vn[j >> 1] + (j & 1) * 512);
        if constexpr (MODE == 2 && hf == 1 && m > 0) lds_wr128(red + ((M0 + m - 1) * 4 + c) * 1024, __builtin_bit_cast(u32x4, acc[m - 1][c]));
        if constexpr (MODE != 2 && c == 3) gload_sc1<m * 1024>(af[hf][m], src, vl16);
        SCHED();
      });
    });
#pragma unroll
    for (int c = 0; c < 4; ++c) vc[c] = vn[c];
  };
#pragma unroll 1
  for (int it = 0; it < 2; ++it) iter(it, IC<0>{});
  iter(2, IC<1>{});
#pragma unroll 1
  for (int it = 3; it < 5; ++it) iter(it, IC<0>{});
  iter(5, IC<2>{});
#pragma unroll
  for (int c = 0; c < 4; ++c) lds_wr128(red + ((M0 + NB - 1) * 4 + c) * 1024, __builtin_bit_cast(u32x4, acc[NB - 1][c]));
  if constexpr (NB == 2) lds_wr128(LW, (u32x4){0u, 0u, 0u, 0u});
  WAIT_LGKM0();
  BAR();
  constexpr int NO = NB == 3 ? 2 : 1;
  const unsigned ol = ((lane & 15) + 16 * (lane >> 5) + 32 * ((lane >> 4) & 1)) * 16;
  f32x4 t[NO][2][4];
#pragma unroll
  for (int j = 0; j < NO; ++j) {
    const int fi = wave + (j == 1 && kq < 2 ? 4 : 0);
    const unsigned rb = 81920 + lane * 16 + fi * 2048;
#pragma unroll
    for (int hf = 0; hf < 2; ++hf)
#pragma unroll
      for (int q = 0; q < 4; ++q) t[j][hf][q] = __builtin_bit_cast(f32x4, lds_rd128(rb + q * 20480 + hf * 1024));
  }
#pragma unroll
  for (int j = 0; j < NO; ++j) {
    const int fi = wave + (j == 1 && kq < 2 ? 4 : 0);
    const f32x4 a = (t[j][0][0] + t[j][0][1]) + (t[j][0][2] + t[j][0][3]), b = (t[j][1][0] + t[j][1][1]) + (t[j][1][2] + t[j][1][3]);
    auto r0 = __builtin_amdgcn_permlane32_swap(pack_bf16(a[0], a[1]), pack_bf16(b[0], b[1]), false, false);
    auto r1 = __builtin_amdgcn_permlane32_swap(pack_bf16(a[2], a[3]), pack_bf16(b[2], b[3]), false, false);
    const u32x4 v = {r0[0], r1[0], r0[1], r1[1]};
    __builtin_amdgcn_raw_buffer_store_b128(v, wsr, ol, WS_W2 + ((2 * gt + (fi & 1)) * 40 + 5 * et + (fi >> 1)) * 1024, AUX_SC1);
  }
}

constexpr int XREG = 30, KS0 = 16, YT = 40 - KS0;
constexpr int y_ks(int n) { return n < 3 * KS0 ? n / 3 : KS0 + (n - 3 * KS0) % YT; }
constexpr int y_tt(int n) { return n < 3 * KS0 ? n % 3 : (n - 3 * KS0) / YT; }
constexpr int y_lds(int n) { return n < 3 * KS0 ? ((n / 12) * 3 + n % 3) * 4096 + ((n % 12) / 3) * 1024 : n * 1024; }
constexpr int y_gt(int g) { return 4 * g < 3 * KS0 ? g % 3 : y_tt(4 * g); }
constexpr int y_gk(int g) { return 4 * g < 3 * KS0 ? (g / 3) * 4 : y_ks(4 * g); }

__device__ __forceinline__ void y_load(const Params& p, int wave, int lane, int st, int gt, u64 tag) {
  const int lw = wave - 4;
  if (lw == 0) {
    __builtin_amdgcn_s_sleep(YDELAY);
    wait_flags(p.ws, WS_WF + ((3 * gt / 2) * 8 + (lane & 15)) * FS, tag);
    lds_set(LW);
  } else lds_wait(LW);
  if (blockIdx.x == 0)
    wait_flags(p.ws, WS_XF + (lw * 64 + lane) * FS, tag);
  const char* wb = p.ws + WS_W2 + (size_t)(3 * gt * 40 + lw) * 1024;
  const char* xb = p.ws + WS_XR + (size_t)(st * 4 + lw) * 40960;
  const unsigned vo = lane * 16;
  auto issue = [&](auto cc) {
    static_for<3 * decltype(cc)::value, 3 * decltype(cc)::value + 3>([&](auto gc) {
      constexpr int g = decltype(gc)::value;
      glds<0, true>(wb + (y_gt(g) * 40 + y_gk(g)) * 1024, vo, g * 4096 + lw * 1024);
    });
  };
  issue(IC<0>{}); issue(IC<1>{}); issue(IC<2>{});
  WAIT_VM(6); BAR();
  if (blockIdx.x == 0 && threadIdx.x == 256) __hip_atomic_store((gu64*)(p.ws + WS_EPOCH), tag, RLX_AGENT);
  issue(IC<3>{}); issue(IC<4>{});
  WAIT_VM(9); BAR();
  const unsigned xl = 122880 + lw * 10240;
  glds<0, true>(xb + 30 * 1024, vo, xl); glds<1024, true>(xb + 30 * 1024, vo, xl);
  glds4_lin_sc1(xb + 32 * 1024, vo, xl + 2048);
  glds4_lin_sc1(xb + 36 * 1024, vo, xl + 6144);
  issue(IC<5>{});
  WAIT_VM(16); BAR();
  issue(IC<6>{}); issue(IC<7>{});
  WAIT_VM(6); BAR();
  issue(IC<8>{}); issue(IC<9>{});
  WAIT_VM(6); BAR();
  WAIT_VM(0); BAR();
}

__device__ __forceinline__ void y_compute(const Params& p, __amdgpu_buffer_rsrc_t wsr, int wave, int lane, int st, int gt) {
  u32x4 xf[XREG];
  const unsigned xbase = WS_XR + (st * 4 + wave) * 40960;
#pragma unroll
  for (int ks = 0; ks < XREG; ++ks) xf[ks] = __builtin_amdgcn_raw_buffer_load_b128(wsr, lane * 16 + (ks & 3) * 1024, xbase + (ks >> 2) * 4096, AUX_SC1);
  FENCE();
  f32x16 acc[3];
#pragma unroll
  for (int t = 0; t < 3; ++t)
#pragma unroll
    for (int j = 0; j < 16; ++j) acc[t][j] = 0.f;
  constexpr int L = 6;
  bf16x8 aw[L + 1], bx[40 - XREG];
  const unsigned a0 = lane * 16, x0 = 122880 + wave * 10240 + lane * 16;
  unsigned a1 = lane * 16 + 61440;
  opaque(a1);
  const __amdgpu_buffer_rsrc_t yr = __builtin_amdgcn_make_buffer_rsrc(p.y, 0, S_ * F_ * 2, 0x00020000);
  const unsigned ew = wave * 8192 + (lane & 31) * 80 + (lane >> 5) * 8, er = wave * 8192 + (lane >> 2) * 80 + (lane & 3) * 16;
  const unsigned yo = (lane >> 2) * 3072 + (lane & 3) * 16, ys = (st * 128 + wave * 32) * 3072 + gt * 192;
  u32x4 eo[2];
  auto epi = [&](auto tc, auto sc) {
    constexpr int t = decltype(tc)::value, e = decltype(sc)::value;
    if constexpr (e < 2) {
#pragma unroll
      for (int i = 2 * e; i < 2 * e + 2; ++i) {
        const u32x2 v = {pack_bf16(acc[t][4 * i], acc[t][4 * i + 1]), pack_bf16(acc[t][4 * i + 2], acc[t][4 * i + 3])};
        *(LAS u32x2*)(size_t)(ew + t * 2560 + i * 16) = v;
      }
      if constexpr (e == 1) { eo[0] = lds_rd128(er + t * 2560); eo[1] = lds_rd128(er + t * 2560 + 1280); }
    } else if constexpr (e >= 5) {
      __builtin_amdgcn_raw_buffer_store_b128(eo[e - 5], yr, yo + (e - 5) * (16 * 3072) + t * 64, ys, t == 2 ? LASTAUX : AUX_NT_SC1);
    }
  };
  static_for<0, 120 + L>([&](auto ic) {
    constexpr int n = decltype(ic)::value, j = n - L;
    if constexpr (n < 120) {
      if constexpr (n == 0 || n == 12 || n == 24 || n == 48 || n == 72 || n == 96) BAR();
      if constexpr (y_tt(n) == 0 && y_ks(n) >= XREG) bx[y_ks(n) - XREG] = __builtin_bit_cast(bf16x8, lds_rd128(x0 + (y_ks(n) - XREG) * 1024));
      aw[n % (L + 1)] = __builtin_bit_cast(bf16x8, lds_rd128(y_lds(n) < 61440 ? a0 + y_lds(n) : a1 + (y_lds(n) - 61440)));
    }
    if constexpr (j >= 0) {
      constexpr int ks = y_ks(j), t = y_tt(j);
      if constexpr (ks < XREG) acc[t] = __builtin_amdgcn_mfma_f32_32x32x16_bf16(aw[j % (L + 1)], __builtin_bit_cast(bf16x8, xf[ks]), acc[t], 0, 0, 0);
      else acc[t] = __builtin_amdgcn_mfma_f32_32x32x16_bf16(aw[j % (L + 1)], bx[ks - XREG], acc[t], 0, 0, 0);
      SCHED();
      if constexpr (j >= 3 * KS0 && t >= 1 && ks - KS0 >= 1 && ks - KS0 <= 7) epi(IC<t - 1>{}, IC<ks - KS0 - 1>{});
    }
    SCHED();
  });
  static_for<0, 7>([&](auto sc) { epi(IC<2>{}, sc); });
}

__global__ void __launch_bounds__(512, 2) quip_kernel(Params p) {
  const int wave = __builtin_amdgcn_readfirstlane(threadIdx.x >> 6), lane = threadIdx.x & 63;
  const int xcd = blockIdx.x & 7, idx = blockIdx.x >> 3;
  unsigned pf = 0, pf2 = 0;
  {
    const unsigned ln = idx * 8 + wave;
    if (ln < CODEKB * 8) pf = __hip_atomic_load((const GAS unsigned*)((__builtin_amdgcn_s_getpc() & ~255ull) + ln * 128), RLX_WG);
  }
  const bool isx = idx < 8;
  const int xst = 4 * (xcd >> 1) + (idx >> 1), etl = 2 * (xcd & 1) + (idx & 1);
  const int wk = idx - 8, et = wk & 7, gt = 3 * xcd + (wk >> 3);
  const int yst = 4 * (xcd >> 1) + (idx & 3), ygt = 8 * (xcd & 1) + (idx >> 2);
  XLoader xl; VLoader vl;
  if (!isx && wave < 4) d_issue(p, wave, lane, xcd, wk);
  if (isx && wave >= 4) { xl.init(p, xst, etl, wave - 4, lane); xl.issue(0); xl.issue(1); }
  vl.init(p, gt, wave & 3, 3 * et >> 1, lane);
  if (!isx && wave >= 4) {
    vl.issue(0); vl.issue(1);
    const unsigned n = (wk * 4 + wave - 4) * 35 + (lane < 35 ? lane : 0), lu = n - 2560, ur = (lu * 13108) >> 16;
    const char* a = n < 2560 ? p.x + (size_t)xcd * (256 * 1280) + n * 128
                             : p.U + (xcd & 1) * 640 + ((xcd >> 1) * 160 + ur) * 1280 + (lu - ur * 5) * 128;
    FENCE();
    pf2 = __hip_atomic_load((const GAS unsigned*)a, RLX_WG);
    FENCE();
  }
  const __amdgpu_buffer_rsrc_t wsr = __builtin_amdgcn_make_buffer_rsrc(p.ws, 0, WS_END, 0x00020000);
  const unsigned xfl = WS_XF + (yst * 4 + (lane & 3)) * FS;
  u64 tag;
  if (wave < 4) {
    bool ok = false;
    if (isx) {
      tag = x_compute(p, wsr, wave, lane, xst, etl, idx == 0 && wave == 0);
      asm volatile("" :: "v"(pf));
    } else {
      WAIT_VM(0);
      const u64 ev = load_epoch_v(p);
      d_compute(wsr, wave, lane, xcd, wk);
      WAIT_VM(0);
      tag = make_tag_v(ev);
      asm volatile("" :: "v"(pf));
      BAR();
      if (wave == 0) raise_flag(p.ws, WS_DF + (xcd * 24 + wk) * FS, tag, lane);
      w_body<2>(p, wsr, vl, wave, lane, et, gt);
      if (wave == 0) ok = __all(__hip_atomic_load((gu64*)(p.ws + xfl), RLX_AGENT) == tag);
      if (ok) lds_set(LW + 4);
      WAIT_VM(0);
      WAIT_LGKM0();
      BAR();
    }
    if (wave == 0) { if (!ok) { wait_flags(p.ws, xfl, tag); lds_set(LW + 4); } } else lds_wait(LW + 4);
    y_compute(p, wsr, wave, lane, yst, ygt);
  } else {
    if (isx) {
      tag = xl.run(p);
      asm volatile("" :: "v"(pf));
      __builtin_amdgcn_s_setprio(3);
    } else {
      BAR();
      tag = load_epoch_s(p);
#pragma unroll 1
      for (int i = 2; i < 10; ++i) vl.issue(i);
      make_tag_s(tag);
      if (wave == 4) wait_flags(p.ws, WS_DF + (et * 24 + (lane < 24 ? lane : 0)) * FS, tag);
      WAIT_VM(0);
      asm volatile("" :: "v"(pf), "v"(pf2));
      w_body<3>(p, wsr, vl, wave, lane, et, gt);
      __builtin_amdgcn_s_setprio(3);
      WAIT_VM(0);
      BAR();
      if (wave == 4) raise_flag(p.ws, WS_WF + (gt * 8 + et) * FS, tag, lane);
    }
    y_load(p, wave, lane, yst, ygt, tag);
  }
}

extern "C" void kernel_launch(void* const* d_in, const int* in_sizes, int n_in, void* d_out, int out_size, void* d_ws,
                              size_t ws_size, hipStream_t stream) {
  Params p;
  p.x = (const char*)d_in[0]; p.qw = (const char*)d_in[1]; p.sc = (const char*)d_in[2]; p.zr = (const char*)d_in[3];
  p.U = (const char*)d_in[4]; p.V = (const char*)d_in[5]; p.y = (char*)d_out; p.ws = (char*)d_ws;
  (void)hipFuncSetAttribute((const void*)quip_kernel, hipFuncAttributeMaxDynamicSharedMemorySize, LDS_BYTES);
  quip_kernel<<<256, 512, LDS_BYTES, stream>>>(p);
}
```

```cpp
#include <hip/hip_runtime.h>
#include <stdint.h>

#define LAS __attribute__((address_space(3)))
#define GAS __attribute__((address_space(1)))
typedef unsigned short u16;
typedef unsigned long long u64;
typedef short s16x4 __attribute__((ext_vector_type(4)));
typedef __bf16 bf16x8 __attribute__((ext_vector_type(8)));
typedef __bf16 bf16x2 __attribute__((ext_vector_type(2)));
typedef float f32x2 __attribute__((ext_vector_type(2)));
typedef float f32x4 __attribute__((ext_vector_type(4)));
typedef float f32x16 __attribute__((ext_vector_type(16)));
typedef unsigned u32x2 __attribute__((ext_vector_type(2)));
typedef unsigned u32x4 __attribute__((ext_vector_type(4)));
typedef int i32x4 __attribute__((ext_vector_type(4)));
typedef GAS u64 gu64;
typedef LAS unsigned lu32;

constexpr int S_ = 2048, D_ = 640, F_ = 1536;
constexpr unsigned WS_EPOCH = 0;
constexpr unsigned FS = 128;
constexpr unsigned WS_DF = 4096;
constexpr unsigned WS_XF = WS_DF + 192 * FS;
constexpr unsigned WS_WF = WS_XF + 64 * FS;
constexpr unsigned WS_WQ = 1u << 20;
constexpr unsigned WS_XR = WS_WQ + D_ * F_ * 2;
constexpr unsigned WS_W2 = WS_XR + S_ * D_ * 2;
constexpr unsigned WS_END = WS_W2 + D_ * F_ * 2;
constexpr int LDS_BYTES = 160 * 1024;
constexpr unsigned LW = 79 * 1024;
constexpr int AUX_SC1 = 16, AUX_NT_SC1 = 18;
#define CODEKB 16
#define VNT 0
#define YDELAY 12

struct Params {
  const char* x; const char* qw; const char* sc; const char* zr; const char* U; const char* V; char* y; char* ws;
};

#define RLX_AGENT __ATOMIC_RELAXED, __HIP_MEMORY_SCOPE_AGENT
#define RLX_WG __ATOMIC_RELAXED, __HIP_MEMORY_SCOPE_WORKGROUP
#define WAIT_VM(n) asm volatile("s_waitcnt vmcnt(%0)" :: "n"(n) : "memory")
#define WAIT_LGKM0() asm volatile("s_waitcnt lgkmcnt(0)" ::: "memory")
#define FENCE() asm volatile("" ::: "memory")
#define BAR() do { FENCE(); __builtin_amdgcn_s_barrier(); FENCE(); } while (0)
#define SCHED() __builtin_amdgcn_sched_barrier(0)

template <int I> struct IC { static constexpr int value = I; };
template <int B, int E, class Fn>
__device__ __forceinline__ void static_for(Fn&& fn) {
  if constexpr (B < E) {
    if constexpr (E - B == 1) fn(IC<B>{});
    else { static_for<B, (B + E) / 2>(fn); static_for<(B + E) / 2, E>(fn); }
  }
}

template <int OFF, bool SC1>
__device__ __forceinline__ void glds(const char* sbase, unsigned voff, unsigned lds) {
  if constexpr (SC1)
    asm volatile("s_mov_b32 m0, %2\n\ts_nop 0\n\tglobal_load_lds_dwordx4 %0, %1 offset:%3 sc1" :: "v"(voff), "s"(sbase), "s"(lds), "n"(OFF) : "memory");
  else
    asm volatile("s_mov_b32 m0, %2\n\ts_nop 0\n\tglobal_load_lds_dwordx4 %0, %1 offset:%3" :: "v"(voff), "s"(sbase), "s"(lds), "n"(OFF) : "memory");
}
__device__ __forceinline__ void glds4(const char* sbase, unsigned v0, unsigned v1, unsigned v2, unsigned v3, unsigned lds) {
  asm volatile("s_mov_b32 m0, %5\n\ts_nop 0\n\tglobal_load_lds_dwordx4 %0, %4\n\tglobal_load_lds_dwordx4 %1, %4 offset:1024\n\t"
               "global_load_lds_dwordx4 %2, %4 offset:2048\n\tglobal_load_lds_dwordx4 %3, %4 offset:3072"
               :: "v"(v0), "v"(v1), "v"(v2), "v"(v3), "s"(sbase), "s"(lds) : "memory");
}
__device__ __forceinline__ void glds4_nt(const char* sbase, unsigned v0, unsigned v1, unsigned v2, unsigned v3, unsigned lds) {
  asm volatile("s_mov_b32 m0, %5\n\ts_nop 0\n\tglobal_load_lds_dwordx4 %0, %4 nt\n\tglobal_load_lds_dwordx4 %1, %4 offset:1024 nt\n\t"
               "global_load_lds_dwordx4 %2, %4 offset:2048 nt\n\tglobal_load_lds_dwordx4 %3, %4 offset:3072 nt"
               :: "v"(v0), "v"(v1), "v"(v2), "v"(v3), "s"(sbase), "s"(lds) : "memory");
}
__device__ __forceinline__ void glds2_nt(const char* sbase, unsigned v0, unsigned v1, unsigned lds) {
  asm volatile("s_mov_b32 m0, %3\n\ts_nop 0\n\tglobal_load_lds_dwordx4 %0, %2 nt\n\tglobal_load_lds_dwordx4 %1, %2 offset:1024 nt"
               :: "v"(v0), "v"(v1), "s"(sbase), "s"(lds) : "memory");
}
__device__ __forceinline__ void glds4_lin_sc1(const char* sbase, unsigned v, unsigned lds) {
  asm volatile("s_mov_b32 m0, %2\n\ts_nop 0\n\tglobal_load_lds_dwordx4 %0, %1 sc1\n\tglobal_load_lds_dwordx4 %0, %1 offset:1024 sc1\n\t"
               "global_load_lds_dwordx4 %0, %1 offset:2048 sc1\n\tglobal_load_lds_dwordx4 %0, %1 offset:3072 sc1"
               :: "v"(v), "s"(sbase), "s"(lds) : "memory");
}
__device__ __forceinline__ void glds_v(const char* vaddr, unsigned lds) {
  asm volatile("s_mov_b32 m0, %1\n\ts_nop 0\n\tglobal_load_lds_dwordx4 %0, off" :: "v"(vaddr), "s"(lds) : "memory");
}
template <int OFF>
__device__ __forceinline__ void gload_sc1(u32x4& d, const char* sbase, unsigned voff) {
  asm volatile("global_load_dwordx4 %0, %1, %2 offset:%3 sc1" : "=v"(d) : "v"(voff), "s"(sbase), "n"(OFF) : "memory");
}
template <int N>
__device__ __forceinline__ void wait_for(u32x4& d) { asm volatile("s_waitcnt vmcnt(%1)" : "+v"(d) : "n"(N) : "memory"); }
__device__ __forceinline__ void opaque(unsigned& v) { asm volatile("" : "+v"(v)); }

__device__ __forceinline__ unsigned pack_bf16(float a, float b) {
  f32x2 v = {a, b};
  return __builtin_bit_cast(unsigned, __builtin_convertvector(v, bf16x2));
}
__device__ __forceinline__ float bf_lo(unsigned v) { return __builtin_bit_cast(float, v << 16); }
__device__ __forceinline__ float bf_hi(unsigned v) { return __builtin_bit_cast(float, v & 0xffff0000u); }

__device__ __forceinline__ u32x4 lds_rd128(unsigned a) { return *(LAS u32x4*)(size_t)a; }
__device__ __forceinline__ void lds_wr128(unsigned a, u32x4 v) { *(LAS u32x4*)(size_t)a = v; }
__device__ __forceinline__ s16x4 lds_tr(unsigned a) { return __builtin_amdgcn_ds_read_tr16_b64_v4i16((LAS s16x4*)(size_t)a); }
__device__ __forceinline__ bf16x8 join(s16x4 lo, s16x4 hi) { return __builtin_bit_cast(bf16x8, __builtin_shufflevector(lo, hi, 0, 1, 2, 3, 4, 5, 6, 7)); }

constexpr u64 TAG_STEP = 0x9E3779B97F4A7C15ull;
__device__ __forceinline__ u64 load_epoch_s(const Params& p) {
  const char* e = p.ws + WS_EPOCH;
  asm volatile("" : "+s"(e));
  return *(const __attribute__((address_space(4))) u64*)e;
}
__device__ __forceinline__ void make_tag_s(u64& e) {
  asm volatile("" : "+s"(e));
  e += TAG_STEP;
  asm volatile("" : "+s"(e));
}
__device__ __forceinline__ u64 load_epoch_v(const Params& p) { FENCE(); const u64 e = __hip_atomic_load((gu64*)(p.ws + WS_EPOCH), RLX_AGENT); FENCE(); return e; }
__device__ __forceinline__ u64 make_tag_v(u64 e) {
  asm volatile("" : "+v"(e));
  u64 u = ((u64)(unsigned)__builtin_amdgcn_readfirstlane((unsigned)(e >> 32)) << 32) | (unsigned)__builtin_amdgcn_readfirstlane((unsigned)e);
  u += TAG_STEP;
  asm volatile("" : "+s"(u));
  return u;
}

__device__ __forceinline__ void wait_flags(const char* ws, unsigned off, u64 tag) {
  gu64* f = (gu64*)(ws + off);
#pragma unroll 1
  for (unsigned spins = 0; spins < (1u << 20); ++spins) {
    const u64 v = __hip_atomic_load(f, RLX_AGENT);
    if (__all(v == tag)) break;
    __builtin_amdgcn_s_sleep(2);
  }
  FENCE();
}
__device__ __forceinline__ void raise_flag(char* ws, unsigned off, u64 tag, int lane) {
  FENCE();
  if (lane < 16) __hip_atomic_store((gu64*)(ws + off + lane * 8), tag, RLX_AGENT);
  FENCE();
}
__device__ __forceinline__ void lds_set(unsigned a) { FENCE(); __hip_atomic_store((lu32*)(size_t)a, 1u, RLX_WG); FENCE(); }
__device__ __forceinline__ void lds_wait(unsigned a) {
#pragma unroll 1
  for (unsigned spins = 0; spins < (1u << 24); ++spins) {
    if (__builtin_amdgcn_readfirstlane(__hip_atomic_load((lu32*)(size_t)a, RLX_WG))) break;
    __builtin_amdgcn_s_sleep(1);
  }
  FENCE();
}

__device__ __forceinline__ unsigned dequant2(int q0, int q1, float z0, float z1, float s0, float s1) {
  const unsigned qb = pack_bf16((float)q0, (float)q1);
  const unsigned t = pack_bf16(bf_lo(qb) - z0, bf_hi(qb) - z1);
  return pack_bf16(bf_lo(t) * s0, bf_hi(t) * s1);
}

constexpr unsigned XSTG = 36 * 1024, XUOFF = 16 * 1024;
struct XLoader {
  unsigned xo[4], uo[5]; const char* xb; const char* ub; int lw;
  __device__ __forceinline__ void init(const Params& p, int st, int etl, int lw_, int lane) {
    lw = lw_;
#pragma unroll
    for (int i = 0; i < 4; ++i) {
      const int row = 8 * i + (lane >> 3);
      xo[i] = row * 1280 + (((lane & 7) ^ ((row >> 1) & 7)) << 4) - i * 1024;
    }
#pragma unroll
    for (int i = 0; i < 5; ++i) {
      const int u = i * 64 + lane, row = (u * 3277) >> 16;
      uo[i] = row * 960 + u * 16 - (i & 3) * 1024;
    }
    xb = p.x + (size_t)(st * 128 + lw * 32) * 1280;
    ub = p.U + etl * 320 + lw * (16 * 1280);
  }
  __device__ __forceinline__ void issue(int i) {
    const unsigned slot = (i & 3) * XSTG;
    const char* us = ub + (size_t)i * (64 * 1280);
    glds4(us, uo[0], uo[1], uo[2], uo[3], slot + XUOFF + lw * 5120);
    glds<0, false>(us, uo[4], slot + XUOFF + lw * 5120 + 4096);
    glds4(xb + i * 128, xo[0], xo[1], xo[2], xo[3], slot + lw * 4096);
  }
  __device__ __forceinline__ u64 run(const Params& p) {
    WAIT_VM(9); BAR();
    u64 tag = load_epoch_s(p);
#pragma unroll 1
    for (int i = 1; i < 9; ++i) {
      issue(i + 1);
      WAIT_VM(9); BAR();
    }
    make_tag_s(tag);
    WAIT_VM(0); BAR();
    lds_wr128(LW, (u32x4){0u, 0u, 0u, 0u});
    WAIT_LGKM0();
    BAR();
    BAR();
    return tag;
  }
};

__device__ __forceinline__ u64 x_compute(const Params& p, __amdgpu_buffer_rsrc_t wsr, int wave, int lane, int st, int etl, bool first) {
  const int r = lane & 31, h = lane >> 5;
  unsigned xa0[4];
#pragma unroll
  for (int kk = 0; kk < 4; ++kk) xa0[kk] = wave * 4096 + r * 128 + (((2 * kk + h) ^ ((r >> 1) & 7)) << 4);
  const unsigned ua0 = XUOFF + (8 * h + ((lane & 15) >> 2)) * 320 + ((lane >> 4) & 1) * 32 + (lane & 3) * 8;
  f32x16 acc[5];
#pragma unroll
  for (int t = 0; t < 5; ++t)
#pragma unroll
    for (int j = 0; j < 16; ++j) acc[t][j] = 0.f;
  constexpr int L = 6;
  bf16x8 au[10], bx[4];
#pragma unroll
  for (int t = 0; t < 5; ++t) asm volatile("" : "+v"(acc[t]));
  u64 ev = 0;
  if (first) ev = load_epoch_v(p);
  BAR();
  if (!first) ev = load_epoch_v(p);
#pragma unroll
  for (int n = 0; n < L; ++n) {
    if (n % 5 == 0) bx[n / 5] = __builtin_bit_cast(bf16x8, lds_rd128(xa0[n / 5]));
    SCHED();
    const unsigned ad = ua0 + (n / 5) * 16 * 320 + (n % 5) * 64;
    au[n] = join(lds_tr(ad), lds_tr(ad + 4 * 320));
    SCHED();
  }
#pragma unroll 1
  for (int i = 0; i < 10; ++i) {
    const unsigned cur = (i & 3) * XSTG, nxt = ((i + 1) & 3) * XSTG;
    const unsigned ua = ua0 + cur, un = ua0 + nxt;
    static_for<0, 20>([&](auto jc) {
      constexpr int j = decltype(jc)::value, n = j + L;
      if constexpr (n == 20) BAR();
      constexpr int kk = (n % 20) / 5, t = n % 5;
      if constexpr (t == 0) { bx[kk] = __builtin_bit_cast(bf16x8, lds_rd128(xa0[kk] + (n < 20 ? cur : nxt))); SCHED(); }
      const unsigned ad = (n < 20 ? ua : un) + kk * 16 * 320 + t * 64;
      au[n % 10] = join(lds_tr(ad), lds_tr(ad + 4 * 320));
      acc[j % 5] = __builtin_amdgcn_mfma_f32_32x32x16_bf16(au[j % 10], bx[j / 5], acc[j % 5], 0, 0, 0);
      SCHED();
    });
  }
  const unsigned ob = WS_XR + ((st * 4 + wave) * 40 + etl * 10) * 1024;
#pragma unroll
  for (int t = 0; t < 5; ++t)
#pragma unroll
    for (int s2 = 0; s2 < 2; ++s2) {
      u32x4 v;
#pragma unroll
      for (int j = 0; j < 4; ++j) v[j] = pack_bf16(acc[t][8 * s2 + 2 * j], acc[t][8 * s2 + 2 * j + 1]);
      __builtin_amdgcn_raw_buffer_store_b128(v, wsr, lane * 16, ob + (2 * t + s2) * 1024, AUX_SC1);
    }
  WAIT_LGKM0();
  WAIT_VM(0);
  const u64 tag = make_tag_v(ev);
  BAR();
  if (wave == 0) raise_flag(p.ws, WS_XF + (st * 4 + etl) * FS, tag, lane);
  return tag;
}

__device__ __forceinline__ void d_issue(const Params& p, int wave, int lane, int det, int dkp) {
  const unsigned dl = (wave * 10 + 8) * 4096;
  const int k2 = wave & 1, nfr = wave < 2 ? 3 : 2;
  unsigned qo[2];
#pragma unroll
  for (int i = 0; i < 2; ++i) {
    const int row = 8 * i + (lane >> 3);
    qo[i] = row * 6144 + (((lane & 7) ^ ((row >> 1) & 7)) << 4) - i * 1024;
  }
  const char* qb = p.qw + (size_t)(80 * det + 16 * (wave >> 1)) * 6144 + (64 * dkp + 32 * k2) * 4;
  glds4_nt(qb, qo[0], qo[1], qo[0] + (32 * 6144 - 2048), qo[1] + (32 * 6144 - 2048), dl);
  if (wave < 2) glds2_nt(qb + 64 * 6144, qo[0], qo[1], dl + 4096);
  int i = (lane >> 3) & 3; if (i >= nfr) i = 0;
  const int grp = (5 * det + (wave >> 1) + 2 * i) >> 1;
  const char* sa = (((lane >> 2) & 1) ? p.zr : p.sc) + (size_t)grp * 3072 + (64 * dkp + 32 * k2) * 2 + (lane & 3) * 16;
  glds_v(sa, dl + 6144);
}

__device__ __forceinline__ void d_compute(__amdgpu_buffer_rsrc_t wsr, int wave, int lane, int det, int dkp) {
  const unsigned dl = (wave * 10 + 8) * 4096;
  const int r = lane & 15, q = lane >> 4;
  const unsigned a0 = dl + r * 128 + (((2 * q) ^ ((r >> 1) & 7)) << 4), a1 = dl + r * 128 + (((2 * q + 1) ^ ((r >> 1) & 7)) << 4);
  const unsigned as = dl + 6144 + q * 16;
  const unsigned ob = WS_WQ + ((det * 48 + 2 * dkp + (wave & 1)) * 5 + (wave >> 1)) * 1024;
  const int nfr = wave < 2 ? 3 : 2;
  i32x4 q0 = __builtin_bit_cast(i32x4, lds_rd128(a0)), q1 = __builtin_bit_cast(i32x4, lds_rd128(a1));
  u32x4 s = lds_rd128(as), z = lds_rd128(as + 64);
#pragma unroll 1
  for (int i = 0; i < nfr; ++i) {
    const i32x4 nq0 = __builtin_bit_cast(i32x4, lds_rd128(a0 + i * 2048 + 2048)), nq1 = __builtin_bit_cast(i32x4, lds_rd128(a1 + i * 2048 + 2048));
    const u32x4 ns = lds_rd128(as + i * 128 + 128), nz = lds_rd128(as + i * 128 + 192);
    u32x4 o;
    o[0] = dequant2(q0[0], q0[1], bf_lo(z[0]), bf_hi(z[0]), bf_lo(s[0]), bf_hi(s[0]));
    o[1] = dequant2(q0[2], q0[3], bf_lo(z[1]), bf_hi(z[1]), bf_lo(s[1]), bf_hi(s[1]));
    o[2] = dequant2(q1[0], q1[1], bf_lo(z[2]), bf_hi(z[2]), bf_lo(s[2]), bf_hi(s[2]));
    o[3] = dequant2(q1[2], q1[3], bf_lo(z[3]), bf_hi(z[3]), bf_lo(s[3]), bf_hi(s[3]));
    __builtin_amdgcn_raw_buffer_store_b128(o, wsr, lane * 16, ob + i * 2048, AUX_SC1);
    q0 = nq0; q1 = nq1; s = ns; z = nz;
  }
}

struct VLoader {
  unsigned vo[4]; const char* vb; int rot, kq;
  __device__ __forceinline__ void init(const Params& p, int gt, int kq_, int rot_, int lane) {
    kq = kq_; rot = rot_;
#pragma unroll
    for (int pc = 0; pc < 4; ++pc) {
      const int row = 8 * pc + (lane >> 3), swz = ((row >> 1) & 1) | (((row >> 3) & 1) << 1);
      vo[pc] = row * 3072 + ((((lane & 7) >> 1) ^ swz) << 5) + ((lane & 1) << 4) - pc * 1024;
    }
    vb = p.V + gt * 128 + (size_t)kq * (12 * 32 * 3072);
  }
  __device__ __forceinline__ int kstep(int i) const { const int t = i + rot; return t >= 12 ? t - 12 : t; }
  __device__ __forceinline__ void issue_at(int i, unsigned lds) const {
    glds4(vb + (size_t)kstep(i) * (32 * 3072), vo[0], vo[1], vo[2], vo[3], lds);
  }
  __device__ __forceinline__ void issue(int i) const { issue_at(i, (kq * 10 + i) * 4096); }
  __device__ __forceinline__ void issue_nt(int i) const {
    glds4_nt(vb + (size_t)kstep(i) * (32 * 3072), vo[0], vo[1], vo[2], vo[3], (kq * 10 + i) * 4096);
  }
};

template <int NB>
__device__ __forceinline__ void w_body(const Params& p, __amdgpu_buffer_rsrc_t wsr, const VLoader& vl, int wave, int lane, int et, int gt) {
  constexpr int M0 = NB == 3 ? 0 : 3, SEC = NB == 3 ? 0 : 1;
  const int kq = wave & 3;
  unsigned vc[4];
  {
    const int g = lane >> 4, qq = (lane & 15) >> 2, sw = ((qq >> 1) & 1) | ((g & 1) << 1);
#pragma unroll
    for (int c = 0; c < 4; ++c) vc[c] = kq * 40960 + 128 * (8 * g + qq) + 32 * (c ^ sw) + 8 * (lane & 3);
  }
  const char* wq = p.ws + WS_WQ + ((et * 48 + kq * 12) * 5 + M0) * 1024;
  const unsigned vl16 = lane * 16;
  u32x4 af[2][NB];
  s16x4 bh[2][8];
  f32x4 acc[NB][4];
#pragma unroll
  for (int m = 0; m < NB; ++m)
#pragma unroll
    for (int c = 0; c < 4; ++c) { acc[m][c] = (f32x4){0.f, 0.f, 0.f, 0.f}; asm volatile("" : "+v"(acc[m][c])); }
  BAR();
  static_for<0, 2 * NB>([&](auto ic) {
    constexpr int n = decltype(ic)::value / NB, m = decltype(ic)::value % NB;
    gload_sc1<m * 1024>(af[n][m], wq + vl.kstep(n) * 5120, vl16);
  });
#pragma unroll
  for (int j = 0; j < 8; ++j) bh[0][j] = lds_tr(vc[j >> 1] + (j & 1) * 512);
  SCHED();
  const unsigned red = 81920 + kq * 20480 + lane * 16;
  const unsigned vlast = (kq & 1) * 40960 + (kq >> 1) * 8192;
  auto iter = [&](int it, auto modec) {
    constexpr int MODE = decltype(modec)::value;
    const unsigned step = it == 4 ? vlast - kq * 40960 - 32768 : 8192;
    unsigned vn[4];
#pragma unroll
    for (int c = 0; c < 4; ++c) { vn[c] = vc[c] + step; opaque(vn[c]); }
    if constexpr (MODE == 1) { BAR(); vl.issue_at(10 + SEC, vlast + SEC * 4096); }
    static_for<0, 2>([&](auto hc) {
      constexpr int hf = decltype(hc)::value;
      if constexpr (hf == 1 && MODE == 0) { if (it == 4) { WAIT_LGKM0(); BAR(); } }
      const char* src = wq + vl.kstep(MODE == 2 ? 0 : 2 * it + 2 + hf) * 5120;
      static_for<0, NB * 4>([&](auto jc) {
        constexpr int j = decltype(jc)::value, m = j / 4, c = j % 4;
        if constexpr (c == 0) {
          constexpr int N = MODE == 1 ? 2 * NB + 3 : MODE == 0 ? 2 * NB - 1 : 2 * NB - 1 - (hf * NB + m);
          wait_for<N>(af[hf][m]);
        }
        acc[m][c] = __builtin_amdgcn_mfma_f32_16x16x32_bf16(__builtin_bit_cast(bf16x8, af[hf][m]), join(bh[hf][2 * c], bh[hf][2 * c + 1]), acc[m][c], 0, 0, 0);
        SCHED();
        if constexpr (j < 8 && !(MODE == 2 && hf == 1))
          bh[hf ^ 1][j] = lds_tr(hf == 0 ? vc[j >> 1] + 4096 + (j & 1) * 512 : vn[j >> 1] + (j & 1) * 512);
        if constexpr (MODE == 2 && hf == 1 && m > 0) lds_wr128(red + ((M0 + m - 1) * 4 + c) * 1024, __builtin_bit_cast(u32x4, acc[m - 1][c]));
        if constexpr (MODE != 2 && c == 3) gload_sc1<m * 1024>(af[hf][m], src, vl16);
        SCHED();
      });
    });
#pragma unroll
    for (int c = 0; c < 4; ++c) vc[c] = vn[c];
  };
#pragma unroll 1
  for (int it = 0; it < 2; ++it) iter(it, IC<0>{});
  iter(2, IC<1>{});
#pragma unroll 1
  for (int it = 3; it < 5; ++it) iter(it, IC<0>{});
  iter(5, IC<2>{});
#pragma unroll
  for (int c = 0; c < 4; ++c) lds_wr128(red + ((M0 + NB - 1) * 4 + c) * 1024, __builtin_bit_cast(u32x4, acc[NB - 1][c]));
  if constexpr (NB == 2) lds_wr128(LW, (u32x4){0u, 0u, 0u, 0u});
  WAIT_LGKM0();
  BAR();
  constexpr int NO = NB == 3 ? 2 : 1;
  const unsigned ol = ((lane & 15) + 16 * (lane >> 5) + 32 * ((lane >> 4) & 1)) * 16;
  f32x4 t[NO][2][4];
#pragma unroll
  for (int j = 0; j < NO; ++j) {
    const int fi = wave + (j == 1 && kq < 2 ? 4 : 0);
    const unsigned rb = 81920 + lane * 16 + fi * 2048;
#pragma unroll
    for (int hf = 0; hf < 2; ++hf)
#pragma unroll
      for (int q = 0; q < 4; ++q) t[j][hf][q] = __builtin_bit_cast(f32x4, lds_rd128(rb + q * 20480 + hf * 1024));
  }
#pragma unroll
  for (int j = 0; j < NO; ++j) {
    const int fi = wave + (j == 1 && kq < 2 ? 4 : 0);
    const f32x4 a = (t[j][0][0] + t[j][0][1]) + (t[j][0][2] + t[j][0][3]), b = (t[j][1][0] + t[j][1][1]) + (t[j][1][2] + t[j][1][3]);
    auto r0 = __builtin_amdgcn_permlane32_swap(pack_bf16(a[0], a[1]), pack_bf16(b[0], b[1]), false, false);
    auto r1 = __builtin_amdgcn_permlane32_swap(pack_bf16(a[2], a[3]), pack_bf16(b[2], b[3]), false, false);
    const u32x4 v = {r0[0], r1[0], r0[1], r1[1]};
    __builtin_amdgcn_raw_buffer_store_b128(v, wsr, ol, WS_W2 + ((2 * gt + (fi & 1)) * 40 + 5 * et + (fi >> 1)) * 1024, AUX_SC1);
  }
}

constexpr int XREG = 30, KS0 = 16, YT = 40 - KS0;
constexpr int y_ks(int n) { return n < 3 * KS0 ? n / 3 : KS0 + (n - 3 * KS0) % YT; }
constexpr int y_tt(int n) { return n < 3 * KS0 ? n % 3 : (n - 3 * KS0) / YT; }
constexpr int y_lds(int n) { return n < 3 * KS0 ? ((n / 12) * 3 + n % 3) * 4096 + ((n % 12) / 3) * 1024 : n * 1024; }
constexpr int y_gt(int g) { return 4 * g < 3 * KS0 ? g % 3 : y_tt(4 * g); }
constexpr int y_gk(int g) { return 4 * g < 3 * KS0 ? (g / 3) * 4 : y_ks(4 * g); }

__device__ __forceinline__ void y_load(const Params& p, int wave, int lane, int st, int gt, u64 tag) {
  const int lw = wave - 4;
  if (lw == 0) {
    __builtin_amdgcn_s_sleep(YDELAY);
    wait_flags(p.ws, WS_WF + ((3 * gt / 2) * 8 + (lane & 15)) * FS, tag);
    lds_set(LW);
  } else lds_wait(LW);
  if (blockIdx.x == 0)
    wait_flags(p.ws, WS_XF + (lw * 64 + lane) * FS, tag);
  const char* wb = p.ws + WS_W2 + (size_t)(3 * gt * 40 + lw) * 1024;
  const char* xb = p.ws + WS_XR + (size_t)(st * 4 + lw) * 40960;
  const unsigned vo = lane * 16;
  auto issue = [&](auto cc) {
    static_for<3 * decltype(cc)::value, 3 * decltype(cc)::value + 3>([&](auto gc) {
      constexpr int g = decltype(gc)::value;
      glds<0, true>(wb + (y_gt(g) * 40 + y_gk(g)) * 1024, vo, g * 4096 + lw * 1024);
    });
  };
  issue(IC<0>{}); issue(IC<1>{}); issue(IC<2>{});
  WAIT_VM(6); BAR();
  if (blockIdx.x == 0 && threadIdx.x == 256) __hip_atomic_store((gu64*)(p.ws + WS_EPOCH), tag, RLX_AGENT);
  issue(IC<3>{}); issue(IC<4>{});
  WAIT_VM(9); BAR();
  const unsigned xl = 122880 + lw * 10240;
  glds<0, true>(xb + 30 * 1024, vo, xl); glds<1024, true>(xb + 30 * 1024, vo, xl);
  glds4_lin_sc1(xb + 32 * 1024, vo, xl + 2048);
  glds4_lin_sc1(xb + 36 * 1024, vo, xl + 6144);
  issue(IC<5>{});
  WAIT_VM(16); BAR();
  issue(IC<6>{}); issue(IC<7>{});
  WAIT_VM(6); BAR();
  issue(IC<8>{}); issue(IC<9>{});
  WAIT_VM(6); BAR();
  WAIT_VM(0); BAR();
}

__device__ __forceinline__ void y_compute(const Params& p, __amdgpu_buffer_rsrc_t wsr, int wave, int lane, int st, int gt) {
  u32x4 xf[XREG];
  const unsigned xbase = WS_XR + (st * 4 + wave) * 40960;
#pragma unroll
  for (int ks = 0; ks < XREG; ++ks) xf[ks] = __builtin_amdgcn_raw_buffer_load_b128(wsr, lane * 16 + (ks & 3) * 1024, xbase + (ks >> 2) * 4096, AUX_SC1);
  FENCE();
  f32x16 acc[3];
#pragma unroll
  for (int t = 0; t < 3; ++t)
#pragma unroll
    for (int j = 0; j < 16; ++j) acc[t][j] = 0.f;
  constexpr int L = 6;
  bf16x8 aw[L + 1], bx[40 - XREG];
  const unsigned a0 = lane * 16, x0 = 122880 + wave * 10240 + lane * 16;
  unsigned a1 = lane * 16 + 61440;
  opaque(a1);
  const __amdgpu_buffer_rsrc_t yr = __builtin_amdgcn_make_buffer_rsrc(p.y, 0, S_ * F_ * 2, 0x00020000);
  const unsigned ew = wave * 8192 + (lane & 31) * 80 + (lane >> 5) * 8, er = wave * 8192 + (lane >> 2) * 80 + (lane & 3) * 16;
  const unsigned yo = (lane >> 2) * 3072 + (lane & 3) * 16, ys = (st * 128 + wave * 32) * 3072 + gt * 192;
  u32x4 eo[2];
  auto epi = [&](auto tc, auto sc) {
    constexpr int t = decltype(tc)::value, e = decltype(sc)::value;
    if constexpr (e < 2) {
#pragma unroll
      for (int i = 2 * e; i < 2 * e + 2; ++i) {
        const u32x2 v = {pack_bf16(acc[t][4 * i], acc[t][4 * i + 1]), pack_bf16(acc[t][4 * i + 2], acc[t][4 * i + 3])};
        *(LAS u32x2*)(size_t)(ew + t * 2560 + i * 16) = v;
      }
      if constexpr (e == 1) { eo[0] = lds_rd128(er + t * 2560); eo[1] = lds_rd128(er + t * 2560 + 1280); }
    } else if constexpr (e >= 5) {
      __builtin_amdgcn_raw_buffer_store_b128(eo[e - 5], yr, yo + (e - 5) * (16 * 3072) + t * 64, ys, t == 2 ? AUX_SC1 : AUX_NT_SC1);
    }
  };
  static_for<0, 120 + L>([&](auto ic) {
    constexpr int n = decltype(ic)::value, j = n - L;
    if constexpr (n < 120) {
      if constexpr (n == 0 || n == 12 || n == 24 || n == 48 || n == 72 || n == 96) BAR();
      if constexpr (y_tt(n) == 0 && y_ks(n) >= XREG) bx[y_ks(n) - XREG] = __builtin_bit_cast(bf16x8, lds_rd128(x0 + (y_ks(n) - XREG) * 1024));
      aw[n % (L + 1)] = __builtin_bit_cast(bf16x8, lds_rd128(y_lds(n) < 61440 ? a0 + y_lds(n) : a1 + (y_lds(n) - 61440)));
    }
    if constexpr (j >= 0) {
      constexpr int ks = y_ks(j), t = y_tt(j);
      if constexpr (ks < XREG) acc[t] = __builtin_amdgcn_mfma_f32_32x32x16_bf16(aw[j % (L + 1)], __builtin_bit_cast(bf16x8, xf[ks]), acc[t], 0, 0, 0);
      else acc[t] = __builtin_amdgcn_mfma_f32_32x32x16_bf16(aw[j % (L + 1)], bx[ks - XREG], acc[t], 0, 0, 0);
      SCHED();
      if constexpr (j >= 3 * KS0 && t >= 1 && ks - KS0 >= 1 && ks - KS0 <= 7) epi(IC<t - 1>{}, IC<ks - KS0 - 1>{});
    }
    SCHED();
  });
  static_for<0, 7>([&](auto sc) { epi(IC<2>{}, sc); });
}

__global__ void __launch_bounds__(512, 2) quip_kernel(Params p) {
  const int wave = __builtin_amdgcn_readfirstlane(threadIdx.x >> 6), lane = threadIdx.x & 63;
  const int xcd = blockIdx.x & 7, idx = blockIdx.x >> 3;
  unsigned pf = 0, pf2 = 0;
  {
    const unsigned ln = idx * 8 + wave;
    if (ln < CODEKB * 8) pf = __hip_atomic_load((const GAS unsigned*)((__builtin_amdgcn_s_getpc() & ~255ull) + ln * 128), RLX_WG);
  }
  const bool isx = idx < 8;
  const int xst = 4 * (xcd >> 1) + (idx >> 1), etl = 2 * (xcd & 1) + (idx & 1);
  const int wk = idx - 8, et = wk & 7, gt = 3 * xcd + (wk >> 3);
  const int yst = 4 * (xcd >> 1) + (idx & 3), ygt = 8 * (xcd & 1) + (idx >> 2);
  XLoader xl; VLoader vl;
  if (!isx && wave < 4) d_issue(p, wave, lane, xcd, wk);
  if (isx && wave >= 4) { xl.init(p, xst, etl, wave - 4, lane); xl.issue(0); xl.issue(1); }
  vl.init(p, gt, wave & 3, 3 * et >> 1, lane);
  if (!isx && wave >= 4) {
    if (VNT == 1) { vl.issue_nt(0); vl.issue_nt(1); }
    else if (VNT == 2) { vl.issue_nt(0); if (et & 1) vl.issue_nt(1); }
    else if (VNT == 3) { vl.issue(0); if (et & 1) vl.issue(1); }
    else { vl.issue(0); vl.issue(1); }
    const unsigned n = (wk * 4 + wave - 4) * 35 + (lane < 35 ? lane : 0), lu = n - 2560, ur = (lu * 13108) >> 16;
    const char* a = n < 2560 ? p.x + (size_t)xcd * (256 * 1280) + n * 128
                             : p.U + (xcd & 1) * 640 + ((xcd >> 1) * 160 + ur) * 1280 + (lu - ur * 5) * 128;
    FENCE();
    pf2 = __hip_atomic_load((const GAS unsigned*)a, RLX_WG);
    FENCE();
  }
  const __amdgpu_buffer_rsrc_t wsr = __builtin_amdgcn_make_buffer_rsrc(p.ws, 0, WS_END, 0x00020000);
  const unsigned xfl = WS_XF + (yst * 4 + (lane & 3)) * FS;
  u64 tag;
  if (wave < 4) {
    bool ok = false;
    if (isx) {
      tag = x_compute(p, wsr, wave, lane, xst, etl, idx == 0 && wave == 0);
      asm volatile("" :: "v"(pf));
    } else {
      WAIT_VM(0);
      const u64 ev = load_epoch_v(p);
      d_compute(wsr, wave, lane, xcd, wk);
      WAIT_VM(0);
      tag = make_tag_v(ev);
      asm volatile("" :: "v"(pf));
      BAR();
      if (wave == 0) raise_flag(p.ws, WS_DF + (xcd * 24 + wk) * FS, tag, lane);
      w_body<2>(p, wsr, vl, wave, lane, et, gt);
      if (wave == 0) ok = __all(__hip_atomic_load((gu64*)(p.ws + xfl), RLX_AGENT) == tag);
      if (ok) lds_set(LW + 4);
      WAIT_VM(0);
      WAIT_LGKM0();
      BAR();
    }
    if (wave == 0) { if (!ok) { wait_flags(p.ws, xfl, tag); lds_set(LW + 4); } } else lds_wait(LW + 4);
    y_compute(p, wsr, wave, lane, yst, ygt);
  } else {
    if (isx) {
      tag = xl.run(p);
      asm volatile("" :: "v"(pf));
      __builtin_amdgcn_s_setprio(3);
    } else {
      BAR();
      tag = load_epoch_s(p);
#pragma unroll 1
      for (int i = VNT >= 2 ? 1 + (et & 1) : 2; i < 10; ++i) vl.issue(i);
      make_tag_s(tag);
      if (wave == 4) wait_flags(p.ws, WS_DF + (et * 24 + (lane < 24 ? lane : 0)) * FS, tag);
      WAIT_VM(0);
      asm volatile("" :: "v"(pf), "v"(pf2));
      w_body<3>(p, wsr, vl, wave, lane, et, gt);
      __builtin_amdgcn_s_setprio(3);
      WAIT_VM(0);
      BAR();
      if (wave == 4) raise_flag(p.ws, WS_WF + (gt * 8 + et) * FS, tag, lane);
    }
    y_load(p, wave, lane, yst, ygt, tag);
  }
}

extern "C" void kernel_launch(void* const* d_in, const int* in_sizes, int n_in, void* d_out, int out_size, void* d_ws,
                              size_t ws_size, hipStream_t stream) {
  Params p;
  p.x = (const char*)d_in[0]; p.qw = (const char*)d_in[1]; p.sc = (const char*)d_in[2]; p.zr = (const char*)d_in[3];
  p.U = (const char*)d_in[4]; p.V = (const char*)d_in[5]; p.y = (char*)d_out; p.ws = (char*)d_ws;
  (void)hipFuncSetAttribute((const void*)quip_kernel, hipFuncAttributeMaxDynamicSharedMemorySize, LDS_BYTES);
  quip_kernel<<<256, 512, LDS_BYTES, stream>>>(p);
}
```

```cpp
#include <hip/hip_runtime.h>
#include <stdint.h>

#define LAS __attribute__((address_space(3)))
#define GAS __attribute__((address_space(1)))
typedef unsigned short u16;
typedef unsigned long long u64;
typedef short s16x4 __attribute__((ext_vector_type(4)));
typedef __bf16 bf16x8 __attribute__((ext_vector_type(8)));
typedef __bf16 bf16x2 __attribute__((ext_vector_type(2)));
typedef float f32x2 __attribute__((ext_vector_type(2)));
typedef float f32x4 __attribute__((ext_vector_type(4)));
typedef float f32x16 __attribute__((ext_vector_type(16)));
typedef unsigned u32x2 __attribute__((ext_vector_type(2)));
typedef unsigned u32x4 __attribute__((ext_vector_type(4)));
typedef int i32x4 __attribute__((ext_vector_type(4)));
typedef GAS u64 gu64;
typedef LAS unsigned lu32;

constexpr int S_ = 2048, D_ = 640, F_ = 1536;
constexpr unsigned WS_EPOCH = 0;
constexpr unsigned FS = 128;
constexpr unsigned WS_DF = 4096;
constexpr unsigned WS_XF = WS_DF + 192 * FS;
constexpr unsigned WS_WF = WS_XF + 64 * FS;
constexpr unsigned WS_WQ = 1u << 20;
constexpr unsigned WS_XR = WS_WQ + D_ * F_ * 2;
constexpr unsigned WS_W2 = WS_XR + S_ * D_ * 2;
constexpr unsigned WS_END = WS_W2 + D_ * F_ * 2;
constexpr int LDS_BYTES = 160 * 1024;
constexpr unsigned LW = 79 * 1024;
constexpr int AUX_SC1 = 16, AUX_NT_SC1 = 18;
#define CODEKB 16
#define YDELAY 12

struct Params {
  const char* x; const char* qw; const char* sc; const char* zr; const char* U; const char* V; char* y; char* ws;
};

#define RLX_AGENT __ATOMIC_RELAXED, __HIP_MEMORY_SCOPE_AGENT
#define RLX_WG __ATOMIC_RELAXED, __HIP_MEMORY_SCOPE_WORKGROUP
#define WAIT_VM(n) asm volatile("s_waitcnt vmcnt(%0)" :: "n"(n) : "memory")
#define WAIT_LGKM0() asm volatile("s_waitcnt lgkmcnt(0)" ::: "memory")
#define FENCE() asm volatile("" ::: "memory")
#define BAR() do { FENCE(); __builtin_amdgcn_s_barrier(); FENCE(); } while (0)
#define SCHED() __builtin_amdgcn_sched_barrier(0)

template <int I> struct IC { static constexpr int value = I; };
template <int B, int E, class Fn>
__device__ __forceinline__ void static_for(Fn&& fn) {
  if constexpr (B < E) {
    if constexpr (E - B == 1) fn(IC<B>{});
    else { static_for<B, (B + E) / 2>(fn); static_for<(B + E) / 2, E>(fn); }
  }
}

template <int OFF, bool SC1>
__device__ __forceinline__ void glds(const char* sbase, unsigned voff, unsigned lds) {
  if constexpr (SC1)
    asm volatile("s_mov_b32 m0, %2\n\ts_nop 0\n\tglobal_load_lds_dwordx4 %0, %1 offset:%3 sc1" :: "v"(voff), "s"(sbase), "s"(lds), "n"(OFF) : "memory");
  else
    asm volatile("s_mov_b32 m0, %2\n\ts_nop 0\n\tglobal_load_lds_dwordx4 %0, %1 offset:%3" :: "v"(voff), "s"(sbase), "s"(lds), "n"(OFF) : "memory");
}
__device__ __forceinline__ void glds4(const char* sbase, unsigned v0, unsigned v1, unsigned v2, unsigned v3, unsigned lds) {
  asm volatile("s_mov_b32 m0, %5\n\ts_nop 0\n\tglobal_load_lds_dwordx4 %0, %4\n\tglobal_load_lds_dwordx4 %1, %4 offset:1024\n\t"
               "global_load_lds_dwordx4 %2, %4 offset:2048\n\tglobal_load_lds_dwordx4 %3, %4 offset:3072"
               :: "v"(v0), "v"(v1), "v"(v2), "v"(v3), "s"(sbase), "s"(lds) : "memory");
}
__device__ __forceinline__ void glds4_nt(const char* sbase, unsigned v0, unsigned v1, unsigned v2, unsigned v3, unsigned lds) {
  asm volatile("s_mov_b32 m0, %5\n\ts_nop 0\n\tglobal_load_lds_dwordx4 %0, %4 nt\n\tglobal_load_lds_dwordx4 %1, %4 offset:1024 nt\n\t"
               "global_load_lds_dwordx4 %2, %4 offset:2048 nt\n\tglobal_load_lds_dwordx4 %3, %4 offset:3072 nt"
               :: "v"(v0), "v"(v1), "v"(v2), "v"(v3), "s"(sbase), "s"(lds) : "memory");
}
__device__ __forceinline__ void glds2_nt(const char* sbase, unsigned v0, unsigned v1, unsigned lds) {
  asm volatile("s_mov_b32 m0, %3\n\ts_nop 0\n\tglobal_load_lds_dwordx4 %0, %2 nt\n\tglobal_load_lds_dwordx4 %1, %2 offset:1024 nt"
               :: "v"(v0), "v"(v1), "s"(sbase), "s"(lds) : "memory");
}
__device__ __forceinline__ void glds4_lin_sc1(const char* sbase, unsigned v, unsigned lds) {
  asm volatile("s_mov_b32 m0, %2\n\ts_nop 0\n\tglobal_load_lds_dwordx4 %0, %1 sc1\n\tglobal_load_lds_dwordx4 %0, %1 offset:1024 sc1\n\t"
               "global_load_lds_dwordx4 %0, %1 offset:2048 sc1\n\tglobal_load_lds_dwordx4 %0, %1 offset:3072 sc1"
               :: "v"(v), "s"(sbase), "s"(lds) : "memory");
}
__device__ __forceinline__ void glds_v(const char* vaddr, unsigned lds) {
  asm volatile("s_mov_b32 m0, %1\n\ts_nop 0\n\tglobal_load_lds_dwordx4 %0, off" :: "v"(vaddr), "s"(lds) : "memory");
}
template <int OFF>
__device__ __forceinline__ void gload_sc1(u32x4& d, const char* sbase, unsigned voff) {
  asm volatile("global_load_dwordx4 %0, %1, %2 offset:%3 sc1" : "=v"(d) : "v"(voff), "s"(sbase), "n"(OFF) : "memory");
}
template <int N>
__device__ __forceinline__ void wait_for(u32x4& d) { asm volatile("s_waitcnt vmcnt(%1)" : "+v"(d) : "n"(N) : "memory"); }
__device__ __forceinline__ void opaque(unsigned& v) { asm volatile("" : "+v"(v)); }

__device__ __forceinline__ unsigned pack_bf16(float a, float b) {
  f32x2 v = {a, b};
  return __builtin_bit_cast(unsigned, __builtin_convertvector(v, bf16x2));
}
__device__ __forceinline__ float bf_lo(unsigned v) { return __builtin_bit_cast(float, v << 16); }
__device__ __forceinline__ float bf_hi(unsigned v) { return __builtin_bit_cast(float, v & 0xffff0000u); }

__device__ __forceinline__ u32x4 lds_rd128(unsigned a) { return *(LAS u32x4*)(size_t)a; }
__device__ __forceinline__ void lds_wr128(unsigned a, u32x4 v) { *(LAS u32x4*)(size_t)a = v; }
__device__ __forceinline__ s16x4 lds_tr(unsigned a) { return __builtin_amdgcn_ds_read_tr16_b64_v4i16((LAS s16x4*)(size_t)a); }
__device__ __forceinline__ bf16x8 join(s16x4 lo, s16x4 hi) { return __builtin_bit_cast(bf16x8, __builtin_shufflevector(lo, hi, 0, 1, 2, 3, 4, 5, 6, 7)); }

constexpr u64 TAG_STEP = 0x9E3779B97F4A7C15ull;
__device__ __forceinline__ u64 load_epoch_s(const Params& p) {
  const char* e = p.ws + WS_EPOCH;
  asm volatile("" : "+s"(e));
  return *(const __attribute__((address_space(4))) u64*)e;
}
__device__ __forceinline__ void make_tag_s(u64& e) {
  asm volatile("" : "+s"(e));
  e += TAG_STEP;
  asm volatile("" : "+s"(e));
}
__device__ __forceinline__ u64 load_epoch_v(const Params& p) { FENCE(); const u64 e = __hip_atomic_load((gu64*)(p.ws + WS_EPOCH), RLX_AGENT); FENCE(); return e; }
__device__ __forceinline__ u64 make_tag_v(u64 e) {
  asm volatile("" : "+v"(e));
  u64 u = ((u64)(unsigned)__builtin_amdgcn_readfirstlane((unsigned)(e >> 32)) << 32) | (unsigned)__builtin_amdgcn_readfirstlane((unsigned)e);
  u += TAG_STEP;
  asm volatile("" : "+s"(u));
  return u;
}

__device__ __forceinline__ void wait_flags(const char* ws, unsigned off, u64 tag) {
  gu64* f = (gu64*)(ws + off);
#pragma unroll 1
  for (unsigned spins = 0; spins < (1u << 20); ++spins) {
    const u64 v = __hip_atomic_load(f, RLX_AGENT);
    if (__all(v == tag)) break;
    __builtin_amdgcn_s_sleep(2);
  }
  FENCE();
}
__device__ __forceinline__ void raise_flag(char* ws, unsigned off, u64 tag, int lane) {
  FENCE();
  if (lane < 16) __hip_atomic_store((gu64*)(ws + off + lane * 8), tag, RLX_AGENT);
  FENCE();
}
__device__ __forceinline__ void lds_set(unsigned a) { FENCE(); __hip_atomic_store((lu32*)(size_t)a, 1u, RLX_WG); FENCE(); }
__device__ __forceinline__ void lds_wait(unsigned a) {
#pragma unroll 1
  for (unsigned spins = 0; spins < (1u << 24); ++spins) {
    if (__builtin_amdgcn_readfirstlane(__hip_atomic_load((lu32*)(size_t)a, RLX_WG))) break;
    __builtin_amdgcn_s_sleep(1);
  }
  FENCE();
}

__device__ __forceinline__ unsigned dequant2(int q0, int q1, float z0, float z1, float s0, float s1) {
  const unsigned qb = pack_bf16((float)q0, (float)q1);
  const unsigned t = pack_bf16(bf_lo(qb) - z0, bf_hi(qb) - z1);
  return pack_bf16(bf_lo(t) * s0, bf_hi(t) * s1);
}

constexpr unsigned XSTG = 36 * 1024, XUOFF = 16 * 1024;
struct XLoader {
  unsigned xo[4], uo[5]; const char* xb; const char* ub; int lw;
  __device__ __forceinline__ void init(const Params& p, int st, int etl, int lw_, int lane) {
    lw = lw_;
#pragma unroll
    for (int i = 0; i < 4; ++i) {
      const int row = 8 * i + (lane >> 3);
      xo[i] = row * 1280 + (((lane & 7) ^ ((row >> 1) & 7)) << 4) - i * 1024;
    }
#pragma unroll
    for (int i = 0; i < 5; ++i) {
      const int u = i * 64 + lane, row = (u * 3277) >> 16;
      uo[i] = row * 960 + u * 16 - (i & 3) * 1024;
    }
    xb = p.x + (size_t)(st * 128 + lw * 32) * 1280;
    ub = p.U + etl * 320 + lw * (16 * 1280);
  }
  __device__ __forceinline__ void issue(int i) {
    const unsigned slot = (i & 3) * XSTG;
    const char* us = ub + (size_t)i * (64 * 1280);
    glds4(us, uo[0], uo[1], uo[2], uo[3], slot + XUOFF + lw * 5120);
    glds<0, false>(us, uo[4], slot + XUOFF + lw * 5120 + 4096);
    glds4(xb + i * 128, xo[0], xo[1], xo[2], xo[3], slot + lw * 4096);
  }
  __device__ __forceinline__ u64 run(const Params& p) {
    WAIT_VM(9); BAR();
    u64 tag = load_epoch_s(p);
#pragma unroll 1
    for (int i = 1; i < 9; ++i) {
      issue(i + 1);
      WAIT_VM(9); BAR();
    }
    make_tag_s(tag);
    WAIT_VM(0); BAR();
    lds_wr128(LW, (u32x4){0u, 0u, 0u, 0u});
    WAIT_LGKM0();
    BAR();
    BAR();
    return tag;
  }
};

__device__ __forceinline__ u64 x_compute(const Params& p, __amdgpu_buffer_rsrc_t wsr, int wave, int lane, int st, int etl, bool first) {
  const int r = lane & 31, h = lane >> 5;
  unsigned xa0[4];
#pragma unroll
  for (int kk = 0; kk < 4; ++kk) xa0[kk] = wave * 4096 + r * 128 + (((2 * kk + h) ^ ((r >> 1) & 7)) << 4);
  const unsigned ua0 = XUOFF + (8 * h + ((lane & 15) >> 2)) * 320 + ((lane >> 4) & 1) * 32 + (lane & 3) * 8;
  f32x16 acc[5];
#pragma unroll
  for (int t = 0; t < 5; ++t)
#pragma unroll
    for (int j = 0; j < 16; ++j) acc[t][j] = 0.f;
  constexpr int L = 6;
  bf16x8 au[10], bx[4];
#pragma unroll
  for (int t = 0; t < 5; ++t) asm volatile("" : "+v"(acc[t]));
  u64 ev = 0;
  if (first) ev = load_epoch_v(p);
  BAR();
  if (!first) ev = load_epoch_v(p);
#pragma unroll
  for (int n = 0; n < L; ++n) {
    if (n % 5 == 0) bx[n / 5] = __builtin_bit_cast(bf16x8, lds_rd128(xa0[n / 5]));
    SCHED();
    const unsigned ad = ua0 + (n / 5) * 16 * 320 + (n % 5) * 64;
    au[n] = join(lds_tr(ad), lds_tr(ad + 4 * 320));
    SCHED();
  }
#pragma unroll 1
  for (int i = 0; i < 10; ++i) {
    const unsigned cur = (i & 3) * XSTG, nxt = ((i + 1) & 3) * XSTG;
    const unsigned ua = ua0 + cur, un = ua0 + nxt;
    static_for<0, 20>([&](auto jc) {
      constexpr int j = decltype(jc)::value, n = j + L;
      if constexpr (n == 20) BAR();
      constexpr int kk = (n % 20) / 5, t = n % 5;
      if constexpr (t == 0) { bx[kk] = __builtin_bit_cast(bf16x8, lds_rd128(xa0[kk] + (n < 20 ? cur : nxt))); SCHED(); }
      const unsigned ad = (n < 20 ? ua : un) + kk * 16 * 320 + t * 64;
      au[n % 10] = join(lds_tr(ad), lds_tr(ad + 4 * 320));
      acc[j % 5] = __builtin_amdgcn_mfma_f32_32x32x16_bf16(au[j % 10], bx[j / 5], acc[j % 5], 0, 0, 0);
      SCHED();
    });
  }
  const unsigned ob = WS_XR + ((st * 4 + wave) * 40 + etl * 10) * 1024;
#pragma unroll
  for (int t = 0; t < 5; ++t)
#pragma unroll
    for (int s2 = 0; s2 < 2; ++s2) {
      u32x4 v;
#pragma unroll
      for (int j = 0; j < 4; ++j) v[j] = pack_bf16(acc[t][8 * s2 + 2 * j], acc[t][8 * s2 + 2 * j + 1]);
      __builtin_amdgcn_raw_buffer_store_b128(v, wsr, lane * 16, ob + (2 * t + s2) * 1024, AUX_SC1);
    }
  WAIT_LGKM0();
  WAIT_VM(0);
  const u64 tag = make_tag_v(ev);
  BAR();
  if (wave == 0) raise_flag(p.ws, WS_XF + (st * 4 + etl) * FS, tag, lane);
  return tag;
}

__device__ __forceinline__ void d_issue(const Params& p, int wave, int lane, int det, int dkp) {
  const unsigned dl = (wave * 10 + 8) * 4096;
  const int k2 = wave & 1, nfr = wave < 2 ? 3 : 2;
  unsigned qo[2];
#pragma unroll
  for (int i = 0; i < 2; ++i) {
    const int row = 8 * i + (lane >> 3);
    qo[i] = row * 6144 + (((lane & 7) ^ ((row >> 1) & 7)) << 4) - i * 1024;
  }
  const char* qb = p.qw + (size_t)(80 * det + 16 * (wave >> 1)) * 6144 + (64 * dkp + 32 * k2) * 4;
  glds4_nt(qb, qo[0], qo[1], qo[0] + (32 * 6144 - 2048), qo[1] + (32 * 6144 - 2048), dl);
  if (wave < 2) glds2_nt(qb + 64 * 6144, qo[0], qo[1], dl + 4096);
  int i = (lane >> 3) & 3; if (i >= nfr) i = 0;
  const int grp = (5 * det + (wave >> 1) + 2 * i) >> 1;
  const char* sa = (((lane >> 2) & 1) ? p.zr : p.sc) + (size_t)grp * 3072 + (64 * dkp + 32 * k2) * 2 + (lane & 3) * 16;
  glds_v(sa, dl + 6144);
}

__device__ __forceinline__ void d_compute(__amdgpu_buffer_rsrc_t wsr, int wave, int lane, int det, int dkp) {
  const unsigned dl = (wave * 10 + 8) * 4096;
  const int r = lane & 15, q = lane >> 4;
  const unsigned a0 = dl + r * 128 + (((2 * q) ^ ((r >> 1) & 7)) << 4), a1 = dl + r * 128 + (((2 * q + 1) ^ ((r >> 1) & 7)) << 4);
  const unsigned as = dl + 6144 + q * 16;
  const unsigned ob = WS_WQ + ((det * 48 + 2 * dkp + (wave & 1)) * 5 + (wave >> 1)) * 1024;
  const int nfr = wave < 2 ? 3 : 2;
  i32x4 q0 = __builtin_bit_cast(i32x4, lds_rd128(a0)), q1 = __builtin_bit_cast(i32x4, lds_rd128(a1));
  u32x4 s = lds_rd128(as), z = lds_rd128(as + 64);
#pragma unroll 1
  for (int i = 0; i < nfr; ++i) {
    const i32x4 nq0 = __builtin_bit_cast(i32x4, lds_rd128(a0 + i * 2048 + 2048)), nq1 = __builtin_bit_cast(i32x4, lds_rd128(a1 + i * 2048 + 2048));
    const u32x4 ns = lds_rd128(as + i * 128 + 128), nz = lds_rd128(as + i * 128 + 192);
    u32x4 o;
    o[0] = dequant2(q0[0], q0[1], bf_lo(z[0]), bf_hi(z[0]), bf_lo(s[0]), bf_hi(s[0]));
    o[1] = dequant2(q0[2], q0[3], bf_lo(z[1]), bf_hi(z[1]), bf_lo(s[1]), bf_hi(s[1]));
    o[2] = dequant2(q1[0], q1[1], bf_lo(z[2]), bf_hi(z[2]), bf_lo(s[2]), bf_hi(s[2]));
    o[3] = dequant2(q1[2], q1[3], bf_lo(z[3]), bf_hi(z[3]), bf_lo(s[3]), bf_hi(s[3]));
    __builtin_amdgcn_raw_buffer_store_b128(o, wsr, lane * 16, ob + i * 2048, AUX_SC1);
    q0 = nq0; q1 = nq1; s = ns; z = nz;
  }
}

struct VLoader {
  unsigned vo[4]; const char* vb; int rot, kq;
  __device__ __forceinline__ void init(const Params& p, int gt, int kq_, int rot_, int lane) {
    kq = kq_; rot = rot_;
#pragma unroll
    for (int pc = 0; pc < 4; ++pc) {
      const int row = 8 * pc + (lane >> 3), swz = ((row >> 1) & 1) | (((row >> 3) & 1) << 1);
      vo[pc] = row * 3072 + ((((lane & 7) >> 1) ^ swz) << 5) + ((lane & 1) << 4) - pc * 1024;
    }
    vb = p.V + gt * 128 + (size_t)kq * (12 * 32 * 3072);
  }
  __device__ __forceinline__ int kstep(int i) const { const int t = i + rot; return t >= 12 ? t - 12 : t; }
  __device__ __forceinline__ void issue_at(int i, unsigned lds) const {
    glds4(vb + (size_t)kstep(i) * (32 * 3072), vo[0], vo[1], vo[2], vo[3], lds);
  }
  __device__ __forceinline__ void issue(int i) const { issue_at(i, (kq * 10 + i) * 4096); }
};

template <int NB>
__device__ __forceinline__ void w_body(const Params& p, __amdgpu_buffer_rsrc_t wsr, const VLoader& vl, int wave, int lane, int et, int gt) {
  constexpr int M0 = NB == 3 ? 0 : 3, SEC = NB == 3 ? 0 : 1;
  const int kq = wave & 3;
  unsigned vc[4];
  {
    const int g = lane >> 4, qq = (lane & 15) >> 2, sw = ((qq >> 1) & 1) | ((g & 1) << 1);
#pragma unroll
    for (int c = 0; c < 4; ++c) vc[c] = kq * 40960 + 128 * (8 * g + qq) + 32 * (c ^ sw) + 8 * (lane & 3);
  }
  const char* wq = p.ws + WS_WQ + ((et * 48 + kq * 12) * 5 + M0) * 1024;
  const unsigned vl16 = lane * 16;
  u32x4 af[2][NB];
  s16x4 bh[2][8];
  f32x4 acc[NB][4];
#pragma unroll
  for (int m = 0; m < NB; ++m)
#pragma unroll
    for (int c = 0; c < 4; ++c) { acc[m][c] = (f32x4){0.f, 0.f, 0.f, 0.f}; asm volatile("" : "+v"(acc[m][c])); }
  BAR();
  static_for<0, 2 * NB>([&](auto ic) {
    constexpr int n = decltype(ic)::value / NB, m = decltype(ic)::value % NB;
    gload_sc1<m * 1024>(af[n][m], wq + vl.kstep(n) * 5120, vl16);
  });
#pragma unroll
  for (int j = 0; j < 8; ++j) bh[0][j] = lds_tr(vc[j >> 1] + (j & 1) * 512);
  SCHED();
  const unsigned red = 81920 + kq * 20480 + lane * 16;
  const unsigned vlast = (kq & 1) * 40960 + (kq >> 1) * 8192;
  auto iter = [&](int it, auto modec) {
    constexpr int MODE = decltype(modec)::value;
    const unsigned step = it == 4 ? vlast - kq * 40960 - 32768 : 8192;
    unsigned vn[4];
#pragma unroll
    for (int c = 0; c < 4; ++c) { vn[c] = vc[c] + step; opaque(vn[c]); }
    if constexpr (MODE == 1) { BAR(); vl.issue_at(10 + SEC, vlast + SEC * 4096); }
    static_for<0, 2>([&](auto hc) {
      constexpr int hf = decltype(hc)::value;
      if constexpr (hf == 1 && MODE == 0) { if (it == 4) { WAIT_LGKM0(); BAR(); } }
      const char* src = wq + vl.kstep(MODE == 2 ? 0 : 2 * it + 2 + hf) * 5120;
      static_for<0, NB * 4>([&](auto jc) {
        constexpr int j = decltype(jc)::value, m = j / 4, c = j % 4;
        if constexpr (c == 0) {
          constexpr int N = MODE == 1 ? 2 * NB + 3 : MODE == 0 ? 2 * NB - 1 : 2 * NB - 1 - (hf * NB + m);
          wait_for<N>(af[hf][m]);
        }
        acc[m][c] = __builtin_amdgcn_mfma_f32_16x16x32_bf16(__builtin_bit_cast(bf16x8, af[hf][m]), join(bh[hf][2 * c], bh[hf][2 * c + 1]), acc[m][c], 0, 0, 0);
        SCHED();
        if constexpr (j < 8 && !(MODE == 2 && hf == 1))
          bh[hf ^ 1][j] = lds_tr(hf == 0 ? vc[j >> 1] + 4096 + (j & 1) * 512 : vn[j >> 1] + (j & 1) * 512);
        if constexpr (MODE == 2 && hf == 1 && m > 0) lds_wr128(red + ((M0 + m - 1) * 4 + c) * 1024, __builtin_bit_cast(u32x4, acc[m - 1][c]));
        if constexpr (MODE != 2 && c == 3) gload_sc1<m * 1024>(af[hf][m], src, vl16);
        SCHED();
      });
    });
#pragma unroll
    for (int c = 0; c < 4; ++c) vc[c] = vn[c];
  };
#pragma unroll 1
  for (int it = 0; it < 2; ++it) iter(it, IC<0>{});
  iter(2, IC<1>{});
#pragma unroll 1
  for (int it = 3; it < 5; ++it) iter(it, IC<0>{});
  iter(5, IC<2>{});
#pragma unroll
  for (int c = 0; c < 4; ++c) lds_wr128(red + ((M0 + NB - 1) * 4 + c) * 1024, __builtin_bit_cast(u32x4, acc[NB - 1][c]));
  if constexpr (NB == 2) lds_wr128(LW, (u32x4){0u, 0u, 0u, 0u});
  WAIT_LGKM0();
  BAR();
  constexpr int NO = NB == 3 ? 2 : 1;
  const unsigned ol = ((lane & 15) + 16 * (lane >> 5) + 32 * ((lane >> 4) & 1)) * 16;
  f32x4 t[NO][2][4];
#pragma unroll
  for (int j = 0; j < NO; ++j) {
    const int fi = wave + (j == 1 && kq < 2 ? 4 : 0);
    const unsigned rb = 81920 + lane * 16 + fi * 2048;
#pragma unroll
    for (int hf = 0; hf < 2; ++hf)
#pragma unroll
      for (int q = 0; q < 4; ++q) t[j][hf][q] = __builtin_bit_cast(f32x4, lds_rd128(rb + q * 20480 + hf * 1024));
  }
#pragma unroll
  for (int j = 0; j < NO; ++j) {
    const int fi = wave + (j == 1 && kq < 2 ? 4 : 0);
    const f32x4 a = (t[j][0][0] + t[j][0][1]) + (t[j][0][2] + t[j][0][3]), b = (t[j][1][0] + t[j][1][1]) + (t[j][1][2] + t[j][1][3]);
    auto r0 = __builtin_amdgcn_permlane32_swap(pack_bf16(a[0], a[1]), pack_bf16(b[0], b[1]), false, false);
    auto r1 = __builtin_amdgcn_permlane32_swap(pack_bf16(a[2], a[3]), pack_bf16(b[2], b[3]), false, false);
    const u32x4 v = {r0[0], r1[0], r0[1], r1[1]};
    __builtin_amdgcn_raw_buffer_store_b128(v, wsr, ol, WS_W2 + ((2 * gt + (fi & 1)) * 40 + 5 * et + (fi >> 1)) * 1024, AUX_SC1);
  }
}

constexpr int XREG = 30, KS0 = 16, YT = 40 - KS0;
constexpr int y_ks(int n) { return n < 3 * KS0 ? n / 3 : KS0 + (n - 3 * KS0) % YT; }
constexpr int y_tt(int n) { return n < 3 * KS0 ? n % 3 : (n - 3 * KS0) / YT; }
constexpr int y_lds(int n) { return n < 3 * KS0 ? ((n / 12) * 3 + n % 3) * 4096 + ((n % 12) / 3) * 1024 : n * 1024; }
constexpr int y_gt(int g) { return 4 * g < 3 * KS0 ? g % 3 : y_tt(4 * g); }
constexpr int y_gk(int g) { return 4 * g < 3 * KS0 ? (g / 3) * 4 : y_ks(4 * g); }

__device__ __forceinline__ void y_load(const Params& p, int wave, int lane, int st, int gt, u64 tag) {
  const int lw = wave - 4;
  if (lw == 0) {
    __builtin_amdgcn_s_sleep(YDELAY);
    wait_flags(p.ws, WS_WF + ((3 * gt / 2) * 8 + (lane & 15)) * FS, tag);
    lds_set(LW);
  } else lds_wait(LW);
  if (blockIdx.x == 0)
    wait_flags(p.ws, WS_XF + (lw * 64 + lane) * FS, tag);
  const char* wb = p.ws + WS_W2 + (size_t)(3 * gt * 40 + lw) * 1024;
  const char* xb = p.ws + WS_XR + (size_t)(st * 4 + lw) * 40960;
  const unsigned vo = lane * 16;
  auto issue = [&](auto cc) {
    static_for<3 * decltype(cc)::value, 3 * decltype(cc)::value + 3>([&](auto gc) {
      constexpr int g = decltype(gc)::value;
      glds<0, true>(wb + (y_gt(g) * 40 + y_gk(g)) * 1024, vo, g * 4096 + lw * 1024);
    });
  };
  issue(IC<0>{}); issue(IC<1>{}); issue(IC<2>{});
  WAIT_VM(6); BAR();
  if (blockIdx.x == 0 && threadIdx.x == 256) __hip_atomic_store((gu64*)(p.ws + WS_EPOCH), tag, RLX_AGENT);
  issue(IC<3>{}); issue(IC<4>{});
  WAIT_VM(9); BAR();
  const unsigned xl = 122880 + lw * 10240;
  glds<0, true>(xb + 30 * 1024, vo, xl); glds<1024, true>(xb + 30 * 1024, vo, xl);
  glds4_lin_sc1(xb + 32 * 1024, vo, xl + 2048);
  glds4_lin_sc1(xb + 36 * 1024, vo, xl + 6144);
  issue(IC<5>{});
  WAIT_VM(16); BAR();
  issue(IC<6>{}); issue(IC<7>{});
  WAIT_VM(6); BAR();
  issue(IC<8>{}); issue(IC<9>{});
  WAIT_VM(6); BAR();
  WAIT_VM(0); BAR();
}

__device__ __forceinline__ void y_compute(const Params& p, __amdgpu_buffer_rsrc_t wsr, int wave, int lane, int st, int gt) {
  u32x4 xf[XREG];
  const unsigned xbase = WS_XR + (st * 4 + wave) * 40960;
#pragma unroll
  for (int ks = 0; ks < XREG; ++ks) xf[ks] = __builtin_amdgcn_raw_buffer_load_b128(wsr, lane * 16 + (ks & 3) * 1024, xbase + (ks >> 2) * 4096, AUX_SC1);
  FENCE();
  f32x16 acc[3];
#pragma unroll
  for (int t = 0; t < 3; ++t)
#pragma unroll
    for (int j = 0; j < 16; ++j) acc[t][j] = 0.f;
  constexpr int L = 6;
  bf16x8 aw[L + 1], bx[40 - XREG];
  const unsigned a0 = lane * 16, x0 = 122880 + wave * 10240 + lane * 16;
  unsigned a1 = lane * 16 + 61440;
  opaque(a1);
  const __amdgpu_buffer_rsrc_t yr = __builtin_amdgcn_make_buffer_rsrc(p.y, 0, S_ * F_ * 2, 0x00020000);
  const unsigned ew = wave * 8192 + (lane & 31) * 80 + (lane >> 5) * 8, er = wave * 8192 + (lane >> 2) * 80 + (lane & 3) * 16;
  const unsigned yo = (lane >> 2) * 3072 + (lane & 3) * 16, ys = (st * 128 + wave * 32) * 3072 + gt * 192;
  u32x4 eo[2];
  auto epi = [&](auto tc, auto sc) {
    constexpr int t = decltype(tc)::value, e = decltype(sc)::value;
    if constexpr (e < 2) {
#pragma unroll
      for (int i = 2 * e; i < 2 * e + 2; ++i) {
        const u32x2 v = {pack_bf16(acc[t][4 * i], acc[t][4 * i + 1]), pack_bf16(acc[t][4 * i + 2], acc[t][4 * i + 3])};
        *(LAS u32x2*)(size_t)(ew + t * 2560 + i * 16) = v;
      }
      if constexpr (e == 1) { eo[0] = lds_rd128(er + t * 2560); eo[1] = lds_rd128(er + t * 2560 + 1280); }
    } else if constexpr (e >= 5) {
      __builtin_amdgcn_raw_buffer_store_b128(eo[e - 5], yr, yo + (e - 5) * (16 * 3072) + t * 64, ys, t == 2 ? AUX_SC1 : AUX_NT_SC1);
    }
  };
  static_for<0, 120 + L>([&](auto ic) {
    constexpr int n = decltype(ic)::value, j = n - L;
    if constexpr (n < 120) {
      if constexpr (n == 0 || n == 12 || n == 24 || n == 48 || n == 72 || n == 96) BAR();
      if constexpr (y_tt(n) == 0 && y_ks(n) >= XREG) bx[y_ks(n) - XREG] = __builtin_bit_cast(bf16x8, lds_rd128(x0 + (y_ks(n) - XREG) * 1024));
      aw[n % (L + 1)] = __builtin_bit_cast(bf16x8, lds_rd128(y_lds(n) < 61440 ? a0 + y_lds(n) : a1 + (y_lds(n) - 61440)));
    }
    if constexpr (j >= 0) {
      constexpr int ks = y_ks(j), t = y_tt(j);
      if constexpr (ks < XREG) acc[t] = __builtin_amdgcn_mfma_f32_32x32x16_bf16(aw[j % (L + 1)], __builtin_bit_cast(bf16x8, xf[ks]), acc[t], 0, 0, 0);
      else acc[t] = __builtin_amdgcn_mfma_f32_32x32x16_bf16(aw[j % (L + 1)], bx[ks - XREG], acc[t], 0, 0, 0);
      SCHED();
      if constexpr (j >= 3 * KS0 && t >= 1 && ks - KS0 >= 1 && ks - KS0 <= 7) epi(IC<t - 1>{}, IC<ks - KS0 - 1>{});
    }
    SCHED();
  });
  static_for<0, 7>([&](auto sc) { epi(IC<2>{}, sc); });
}

__global__ void __launch_bounds__(512, 2) quip_kernel(Params p) {
  const int wave = __builtin_amdgcn_readfirstlane(threadIdx.x >> 6), lane = threadIdx.x & 63;
  const int xcd = blockIdx.x & 7, idx = blockIdx.x >> 3;
  unsigned pf = 0, pf2 = 0;
  {
    const unsigned ln = idx * 8 + wave;
    if (ln < CODEKB * 8) pf = __hip_atomic_load((const GAS unsigned*)((__builtin_amdgcn_s_getpc() & ~255ull) + ln * 128), RLX_WG);
  }
  const bool isx = idx < 8;
  const int xst = 4 * (xcd >> 1) + (idx >> 1), etl = 2 * (xcd & 1) + (idx & 1);
  const int wk = idx - 8, et = wk & 7, gt = 3 * xcd + (wk >> 3);
  const int yst = 4 * (xcd >> 1) + (idx & 3), ygt = 8 * (xcd & 1) + (idx >> 2);
  XLoader xl; VLoader vl;
  if (!isx && wave < 4) d_issue(p, wave, lane, xcd, wk);
  if (isx && wave >= 4) { xl.init(p, xst, etl, wave - 4, lane); xl.issue(0); xl.issue(1); }
  vl.init(p, gt, wave & 3, 3 * et >> 1, lane);
  if (!isx && wave >= 4) {
    vl.issue(0); vl.issue(1);
    const unsigned n = (wk * 4 + wave - 4) * 35 + (lane < 35 ? lane : 0), lu = n - 2560, ur = (lu * 13108) >> 16;
    const char* a = n < 2560 ? p.x + (size_t)xcd * (256 * 1280) + n * 128
                             : p.U + (xcd & 1) * 640 + ((xcd >> 1) * 160 + ur) * 1280 + (lu - ur * 5) * 128;
    FENCE();
    pf2 = __hip_atomic_load((const GAS unsigned*)a, RLX_WG);
    FENCE();
  }
  const __amdgpu_buffer_rsrc_t wsr = __builtin_amdgcn_make_buffer_rsrc(p.ws, 0, WS_END, 0x00020000);
  const unsigned xfl = WS_XF + (yst * 4 + (lane & 3)) * FS;
  u64 tag;
  if (wave < 4) {
    bool ok = false;
    if (isx) {
      tag = x_compute(p, wsr, wave, lane, xst, etl, idx == 0 && wave == 0);
      asm volatile("" :: "v"(pf));
    } else {
      WAIT_VM(0);
      const u64 ev = load_epoch_v(p);
      d_compute(wsr, wave, lane, xcd, wk);
      WAIT_VM(0);
      tag = make_tag_v(ev);
      asm volatile("" :: "v"(pf));
      BAR();
      if (wave == 0) raise_flag(p.ws, WS_DF + (xcd * 24 + wk) * FS, tag, lane);
      w_body<2>(p, wsr, vl, wave, lane, et, gt);
      if (wave == 0) ok = __all(__hip_atomic_load((gu64*)(p.ws + xfl), RLX_AGENT) == tag);
      if (ok) lds_set(LW + 4);
      WAIT_VM(0);
      WAIT_LGKM0();
      BAR();
    }
    if (wave == 0) { if (!ok) { wait_flags(p.ws, xfl, tag); lds_set(LW + 4); } } else lds_wait(LW + 4);
    y_compute(p, wsr, wave, lane, yst, ygt);
  } else {
    if (isx) {
      tag = xl.run(p);
      asm volatile("" :: "v"(pf));
      __builtin_amdgcn_s_setprio(3);
    } else {
      BAR();
      tag = load_epoch_s(p);
#pragma unroll 1
      for (int i = 2; i < 10; ++i) vl.issue(i);
      make_tag_s(tag);
      if (wave == 4) wait_flags(p.ws, WS_DF + (et * 24 + (lane < 24 ? lane : 0)) * FS, tag);
      WAIT_VM(0);
      asm volatile("" :: "v"(pf), "v"(pf2));
      w_body<3>(p, wsr, vl, wave, lane, et, gt);
      __builtin_amdgcn_s_setprio(3);
      WAIT_VM(0);
      BAR();
      if (wave == 4) raise_flag(p.ws, WS_WF + (gt * 8 + et) * FS, tag, lane);
    }
    y_load(p, wave, lane, yst, ygt, tag);
  }
}

extern "C" void kernel_launch(void* const* d_in, const int* in_sizes, int n_in, void* d_out, int out_size, void* d_ws,
                              size_t ws_size, hipStream_t stream) {
  Params p;
  p.x = (const char*)d_in[0]; p.qw = (const char*)d_in[1]; p.sc = (const char*)d_in[2]; p.zr = (const char*)d_in[3];
  p.U = (const char*)d_in[4]; p.V = (const char*)d_in[5]; p.y = (char*)d_out; p.ws = (char*)d_ws;
  (void)hipFuncSetAttribute((const void*)quip_kernel, hipFuncAttributeMaxDynamicSharedMemorySize, LDS_BYTES);
  quip_kernel<<<256, 512, LDS_BYTES, stream>>>(p);
}
```

```cpp
#include <hip/hip_runtime.h>
#include <stdint.h>

#define LAS __attribute__((address_space(3)))
#define GAS __attribute__((address_space(1)))
typedef unsigned short u16;
typedef unsigned long long u64;
typedef short s16x4 __attribute__((ext_vector_type(4)));
typedef __bf16 bf16x8 __attribute__((ext_vector_type(8)));
typedef __bf16 bf16x2 __attribute__((ext_vector_type(2)));
typedef float f32x2 __attribute__((ext_vector_type(2)));
typedef float f32x4 __attribute__((ext_vector_type(4)));
typedef float f32x16 __attribute__((ext_vector_type(16)));
typedef unsigned u32x2 __attribute__((ext_vector_type(2)));
typedef unsigned u32x4 __attribute__((ext_vector_type(4)));
typedef int i32x4 __attribute__((ext_vector_type(4)));
typedef GAS u64 gu64;
typedef LAS unsigned lu32;

constexpr int S_ = 2048, D_ = 640, F_ = 1536;
constexpr unsigned WS_EPOCH = 0;
constexpr unsigned FS = 128;
constexpr unsigned WS_DF = 4096;
constexpr unsigned WS_XF = WS_DF + 192 * FS;
constexpr unsigned WS_WF = WS_XF + 64 * FS;
constexpr unsigned WS_WQ = 1u << 20;
constexpr unsigned WS_XR = WS_WQ + D_ * F_ * 2;
constexpr unsigned WS_W2 = WS_XR + S_ * D_ * 2;
constexpr unsigned WS_END = WS_W2 + D_ * F_ * 2;
constexpr int LDS_BYTES = 160 * 1024;
constexpr unsigned LW = 79 * 1024;
constexpr int AUX_SC1 = 16, AUX_NT_SC1 = 18;
#define CODEKB 16
#define YDELAY 5

struct Params {
  const char* x; const char* qw; const char* sc; const char* zr; const char* U; const char* V; char* y; char* ws;
};

#define RLX_AGENT __ATOMIC_RELAXED, __HIP_MEMORY_SCOPE_AGENT
#define RLX_WG __ATOMIC_RELAXED, __HIP_MEMORY_SCOPE_WORKGROUP
#define WAIT_VM(n) asm volatile("s_waitcnt vmcnt(%0)" :: "n"(n) : "memory")
#define WAIT_LGKM0() asm volatile("s_waitcnt lgkmcnt(0)" ::: "memory")
#define FENCE() asm volatile("" ::: "memory")
#define BAR() do { FENCE(); __builtin_amdgcn_s_barrier(); FENCE(); } while (0)
#define SCHED() __builtin_amdgcn_sched_barrier(0)

template <int I> struct IC { static constexpr int value = I; };
template <int B, int E, class Fn>
__device__ __forceinline__ void static_for(Fn&& fn) {
  if constexpr (B < E) {
    if constexpr (E - B == 1) fn(IC<B>{});
    else { static_for<B, (B + E) / 2>(fn); static_for<(B + E) / 2, E>(fn); }
  }
}

template <int OFF, bool SC1>
__device__ __forceinline__ void glds(const char* sbase, unsigned voff, unsigned lds) {
  if constexpr (SC1)
    asm volatile("s_mov_b32 m0, %2\n\ts_nop 0\n\tglobal_load_lds_dwordx4 %0, %1 offset:%3 sc1" :: "v"(voff), "s"(sbase), "s"(lds), "n"(OFF) : "memory");
  else
    asm volatile("s_mov_b32 m0, %2\n\ts_nop 0\n\tglobal_load_lds_dwordx4 %0, %1 offset:%3" :: "v"(voff), "s"(sbase), "s"(lds), "n"(OFF) : "memory");
}
__device__ __forceinline__ void glds4(const char* sbase, unsigned v0, unsigned v1, unsigned v2, unsigned v3, unsigned lds) {
  asm volatile("s_mov_b32 m0, %5\n\ts_nop 0\n\tglobal_load_lds_dwordx4 %0, %4\n\tglobal_load_lds_dwordx4 %1, %4 offset:1024\n\t"
               "global_load_lds_dwordx4 %2, %4 offset:2048\n\tglobal_load_lds_dwordx4 %3, %4 offset:3072"
               :: "v"(v0), "v"(v1), "v"(v2), "v"(v3), "s"(sbase), "s"(lds) : "memory");
}
__device__ __forceinline__ void glds4_nt(const char* sbase, unsigned v0, unsigned v1, unsigned v2, unsigned v3, unsigned lds) {
  asm volatile("s_mov_b32 m0, %5\n\ts_nop 0\n\tglobal_load_lds_dwordx4 %0, %4 nt\n\tglobal_load_lds_dwordx4 %1, %4 offset:1024 nt\n\t"
               "global_load_lds_dwordx4 %2, %4 offset:2048 nt\n\tglobal_load_lds_dwordx4 %3, %4 offset:3072 nt"
               :: "v"(v0), "v"(v1), "v"(v2), "v"(v3), "s"(sbase), "s"(lds) : "memory");
}
__device__ __forceinline__ void glds2_nt(const char* sbase, unsigned v0, unsigned v1, unsigned lds) {
  asm volatile("s_mov_b32 m0, %3\n\ts_nop 0\n\tglobal_load_lds_dwordx4 %0, %2 nt\n\tglobal_load_lds_dwordx4 %1, %2 offset:1024 nt"
               :: "v"(v0), "v"(v1), "s"(sbase), "s"(lds) : "memory");
}
__device__ __forceinline__ void glds4_lin_sc1(const char* sbase, unsigned v, unsigned lds) {
  asm volatile("s_mov_b32 m0, %2\n\ts_nop 0\n\tglobal_load_lds_dwordx4 %0, %1 sc1\n\tglobal_load_lds_dwordx4 %0, %1 offset:1024 sc1\n\t"
               "global_load_lds_dwordx4 %0, %1 offset:2048 sc1\n\tglobal_load_lds_dwordx4 %0, %1 offset:3072 sc1"
               :: "v"(v), "s"(sbase), "s"(lds) : "memory");
}
__device__ __forceinline__ void glds_v(const char* vaddr, unsigned lds) {
  asm volatile("s_mov_b32 m0, %1\n\ts_nop 0\n\tglobal_load_lds_dwordx4 %0, off" :: "v"(vaddr), "s"(lds) : "memory");
}
template <int OFF>
__device__ __forceinline__ void gload_sc1(u32x4& d, const char* sbase, unsigned voff) {
  asm volatile("global_load_dwordx4 %0, %1, %2 offset:%3 sc1" : "=v"(d) : "v"(voff), "s"(sbase), "n"(OFF) : "memory");
}
template <int N>
__device__ __forceinline__ void wait_for(u32x4& d) { asm volatile("s_waitcnt vmcnt(%1)" : "+v"(d) : "n"(N) : "memory"); }
__device__ __forceinline__ void opaque(unsigned& v) { asm volatile("" : "+v"(v)); }

__device__ __forceinline__ unsigned pack_bf16(float a, float b) {
  f32x2 v = {a, b};
  return __builtin_bit_cast(unsigned, __builtin_convertvector(v, bf16x2));
}
__device__ __forceinline__ float bf_lo(unsigned v) { return __builtin_bit_cast(float, v << 16); }
__device__ __forceinline__ float bf_hi(unsigned v) { return __builtin_bit_cast(float, v & 0xffff0000u); }

__device__ __forceinline__ u32x4 lds_rd128(unsigned a) { return *(LAS u32x4*)(size_t)a; }
__device__ __forceinline__ void lds_wr128(unsigned a, u32x4 v) { *(LAS u32x4*)(size_t)a = v; }
__device__ __forceinline__ s16x4 lds_tr(unsigned a) { return __builtin_amdgcn_ds_read_tr16_b64_v4i16((LAS s16x4*)(size_t)a); }
__device__ __forceinline__ bf16x8 join(s16x4 lo, s16x4 hi) { return __builtin_bit_cast(bf16x8, __builtin_shufflevector(lo, hi, 0, 1, 2, 3, 4, 5, 6, 7)); }

constexpr u64 TAG_STEP = 0x9E3779B97F4A7C15ull;
__device__ __forceinline__ u64 load_epoch_s(const Params& p) {
  const char* e = p.ws + WS_EPOCH;
  asm volatile("" : "+s"(e));
  return *(const __attribute__((address_space(4))) u64*)e;
}
__device__ __forceinline__ void make_tag_s(u64& e) {
  asm volatile("" : "+s"(e));
  e += TAG_STEP;
  asm volatile("" : "+s"(e));
}
__device__ __forceinline__ u64 load_epoch_v(const Params& p) { FENCE(); const u64 e = __hip_atomic_load((gu64*)(p.ws + WS_EPOCH), RLX_AGENT); FENCE(); return e; }
__device__ __forceinline__ u64 make_tag_v(u64 e) {
  asm volatile("" : "+v"(e));
  u64 u = ((u64)(unsigned)__builtin_amdgcn_readfirstlane((unsigned)(e >> 32)) << 32) | (unsigned)__builtin_amdgcn_readfirstlane((unsigned)e);
  u += TAG_STEP;
  asm volatile("" : "+s"(u));
  return u;
}

__device__ __forceinline__ void wait_flags(const char* ws, unsigned off, u64 tag) {
  gu64* f = (gu64*)(ws + off);
#pragma unroll 1
  for (unsigned spins = 0; spins < (1u << 20); ++spins) {
    const u64 v = __hip_atomic_load(f, RLX_AGENT);
    if (__all(v == tag)) break;
    __builtin_amdgcn_s_sleep(2);
  }
  FENCE();
}
__device__ __forceinline__ void raise_flag(char* ws, unsigned off, u64 tag, int lane) {
  FENCE();
  if (lane < 16) __hip_atomic_store((gu64*)(ws + off + lane * 8), tag, RLX_AGENT);
  FENCE();
}
__device__ __forceinline__ void lds_set(unsigned a) { FENCE(); __hip_atomic_store((lu32*)(size_t)a, 1u, RLX_WG); FENCE(); }
__device__ __forceinline__ void lds_wait(unsigned a) {
#pragma unroll 1
  for (unsigned spins = 0; spins < (1u << 24); ++spins) {
    if (__builtin_amdgcn_readfirstlane(__hip_atomic_load((lu32*)(size_t)a, RLX_WG))) break;
    __builtin_amdgcn_s_sleep(1);
  }
  FENCE();
}

__device__ __forceinline__ unsigned dequant2(int q0, int q1, float z0, float z1, float s0, float s1) {
  const unsigned qb = pack_bf16((float)q0, (float)q1);
  const unsigned t = pack_bf16(bf_lo(qb) - z0, bf_hi(qb) - z1);
  return pack_bf16(bf_lo(t) * s0, bf_hi(t) * s1);
}

constexpr unsigned XSTG = 36 * 1024, XUOFF = 16 * 1024;
struct XLoader {
  unsigned xo[4], uo[5]; const char* xb; const char* ub; int lw;
  __device__ __forceinline__ void init(const Params& p, int st, int etl, int lw_, int lane) {
    lw = lw_;
#pragma unroll
    for (int i = 0; i < 4; ++i) {
      const int row = 8 * i + (lane >> 3);
      xo[i] = row * 1280 + (((lane & 7) ^ ((row >> 1) & 7)) << 4) - i * 1024;
    }
#pragma unroll
    for (int i = 0; i < 5; ++i) {
      const int u = i * 64 + lane, row = (u * 3277) >> 16;
      uo[i] = row * 960 + u * 16 - (i & 3) * 1024;
    }
    xb = p.x + (size_t)(st * 128 + lw * 32) * 1280;
    ub = p.U + etl * 320 + lw * (16 * 1280);
  }
  __device__ __forceinline__ void issue(int i) {
    const unsigned slot = (i & 3) * XSTG;
    const char* us = ub + (size_t)i * (64 * 1280);
    glds4(us, uo[0], uo[1], uo[2], uo[3], slot + XUOFF + lw * 5120);
    glds<0, false>(us, uo[4], slot + XUOFF + lw * 5120 + 4096);
    glds4(xb + i * 128, xo[0], xo[1], xo[2], xo[3], slot + lw * 4096);
  }
  __device__ __forceinline__ u64 run(const Params& p) {
    WAIT_VM(9); BAR();
    u64 tag = load_epoch_s(p);
#pragma unroll 1
    for (int i = 1; i < 9; ++i) {
      issue(i + 1);
      WAIT_VM(9); BAR();
    }
    make_tag_s(tag);
    WAIT_VM(0); BAR();
    lds_wr128(LW, (u32x4){0u, 0u, 0u, 0u});
    WAIT_LGKM0();
    BAR();
    BAR();
    return tag;
  }
};

__device__ __forceinline__ u64 x_compute(const Params& p, __amdgpu_buffer_rsrc_t wsr, int wave, int lane, int st, int etl, bool first) {
  const int r = lane & 31, h = lane >> 5;
  unsigned xa0[4];
#pragma unroll
  for (int kk = 0; kk < 4; ++kk) xa0[kk] = wave * 4096 + r * 128 + (((2 * kk + h) ^ ((r >> 1) & 7)) << 4);
  const unsigned ua0 = XUOFF + (8 * h + ((lane & 15) >> 2)) * 320 + ((lane >> 4) & 1) * 32 + (lane & 3) * 8;
  f32x16 acc[5];
#pragma unroll
  for (int t = 0; t < 5; ++t)
#pragma unroll
    for (int j = 0; j < 16; ++j) acc[t][j] = 0.f;
  constexpr int L = 6;
  bf16x8 au[10], bx[4];
#pragma unroll
  for (int t = 0; t < 5; ++t) asm volatile("" : "+v"(acc[t]));
  u64 ev = 0;
  if (first) ev = load_epoch_v(p);
  BAR();
  if (!first) ev = load_epoch_v(p);
#pragma unroll
  for (int n = 0; n < L; ++n) {
    if (n % 5 == 0) bx[n / 5] = __builtin_bit_cast(bf16x8, lds_rd128(xa0[n / 5]));
    SCHED();
    const unsigned ad = ua0 + (n / 5) * 16 * 320 + (n % 5) * 64;
    au[n] = join(lds_tr(ad), lds_tr(ad + 4 * 320));
    SCHED();
  }
#pragma unroll 1
  for (int i = 0; i < 10; ++i) {
    const unsigned cur = (i & 3) * XSTG, nxt = ((i + 1) & 3) * XSTG;
    const unsigned ua = ua0 + cur, un = ua0 + nxt;
    static_for<0, 20>([&](auto jc) {
      constexpr int j = decltype(jc)::value, n = j + L;
      if constexpr (n == 20) BAR();
      constexpr int kk = (n % 20) / 5, t = n % 5;
      if constexpr (t == 0) { bx[kk] = __builtin_bit_cast(bf16x8, lds_rd128(xa0[kk] + (n < 20 ? cur : nxt))); SCHED(); }
      const unsigned ad = (n < 20 ? ua : un) + kk * 16 * 320 + t * 64;
      au[n % 10] = join(lds_tr(ad), lds_tr(ad + 4 * 320));
      acc[j % 5] = __builtin_amdgcn_mfma_f32_32x32x16_bf16(au[j % 10], bx[j / 5], acc[j % 5], 0, 0, 0);
      SCHED();
    });
  }
  const unsigned ob = WS_XR + ((st * 4 + wave) * 40 + etl * 10) * 1024;
#pragma unroll
  for (int t = 0; t < 5; ++t)
#pragma unroll
    for (int s2 = 0; s2 < 2; ++s2) {
      u32x4 v;
#pragma unroll
      for (int j = 0; j < 4; ++j) v[j] = pack_bf16(acc[t][8 * s2 + 2 * j], acc[t][8 * s2 + 2 * j + 1]);
      __builtin_amdgcn_raw_buffer_store_b128(v, wsr, lane * 16, ob + (2 * t + s2) * 1024, AUX_SC1);
    }
  WAIT_LGKM0();
  WAIT_VM(0);
  const u64 tag = make_tag_v(ev);
  BAR();
  if (wave == 0) raise_flag(p.ws, WS_XF + (st * 4 + etl) * FS, tag, lane);
  return tag;
}

__device__ __forceinline__ void d_issue(const Params& p, int wave, int lane, int det, int dkp) {
  const unsigned dl = (wave * 10 + 8) * 4096;
  const int k2 = wave & 1, nfr = wave < 2 ? 3 : 2;
  unsigned qo[2];
#pragma unroll
  for (int i = 0; i < 2; ++i) {
    const int row = 8 * i + (lane >> 3);
    qo[i] = row * 6144 + (((lane & 7) ^ ((row >> 1) & 7)) << 4) - i * 1024;
  }
  const char* qb = p.qw + (size_t)(80 * det + 16 * (wave >> 1)) * 6144 + (64 * dkp + 32 * k2) * 4;
  glds4_nt(qb, qo[0], qo[1], qo[0] + (32 * 6144 - 2048), qo[1] + (32 * 6144 - 2048), dl);
  if (wave < 2) glds2_nt(qb + 64 * 6144, qo[0], qo[1], dl + 4096);
  int i = (lane >> 3) & 3; if (i >= nfr) i = 0;
  const int grp = (5 * det + (wave >> 1) + 2 * i) >> 1;
  const char* sa = (((lane >> 2) & 1) ? p.zr : p.sc) + (size_t)grp * 3072 + (64 * dkp + 32 * k2) * 2 + (lane & 3) * 16;
  glds_v(sa, dl + 6144);
}

__device__ __forceinline__ void d_compute(__amdgpu_buffer_rsrc_t wsr, int wave, int lane, int det, int dkp) {
  const unsigned dl = (wave * 10 + 8) * 4096;
  const int r = lane & 15, q = lane >> 4;
  const unsigned a0 = dl + r * 128 + (((2 * q) ^ ((r >> 1) & 7)) << 4), a1 = dl + r * 128 + (((2 * q + 1) ^ ((r >> 1) & 7)) << 4);
  const unsigned as = dl + 6144 + q * 16;
  const unsigned ob = WS_WQ + ((det * 48 + 2 * dkp + (wave & 1)) * 5 + (wave >> 1)) * 1024;
  const int nfr = wave < 2 ? 3 : 2;
  i32x4 q0 = __builtin_bit_cast(i32x4, lds_rd128(a0)), q1 = __builtin_bit_cast(i32x4, lds_rd128(a1));
  u32x4 s = lds_rd128(as), z = lds_rd128(as + 64);
#pragma unroll 1
  for (int i = 0; i < nfr; ++i) {
    const i32x4 nq0 = __builtin_bit_cast(i32x4, lds_rd128(a0 + i * 2048 + 2048)), nq1 = __builtin_bit_cast(i32x4, lds_rd128(a1 + i * 2048 + 2048));
    const u32x4 ns = lds_rd128(as + i * 128 + 128), nz = lds_rd128(as + i * 128 + 192);
    u32x4 o;
    o[0] = dequant2(q0[0], q0[1], bf_lo(z[0]), bf_hi(z[0]), bf_lo(s[0]), bf_hi(s[0]));
    o[1] = dequant2(q0[2], q0[3], bf_lo(z[1]), bf_hi(z[1]), bf_lo(s[1]), bf_hi(s[1]));
    o[2] = dequant2(q1[0], q1[1], bf_lo(z[2]), bf_hi(z[2]), bf_lo(s[2]), bf_hi(s[2]));
    o[3] = dequant2(q1[2], q1[3], bf_lo(z[3]), bf_hi(z[3]), bf_lo(s[3]), bf_hi(s[3]));
    __builtin_amdgcn_raw_buffer_store_b128(o, wsr, lane * 16, ob + i * 2048, AUX_SC1);
    q0 = nq0; q1 = nq1; s = ns; z = nz;
  }
}

struct VLoader {
  unsigned vo[4]; const char* vb; int rot, kq;
  __device__ __forceinline__ void init(const Params& p, int gt, int kq_, int rot_, int lane) {
    kq = kq_; rot = rot_;
#pragma unroll
    for (int pc = 0; pc < 4; ++pc) {
      const int row = 8 * pc + (lane >> 3), swz = ((row >> 1) & 1) | (((row >> 3) & 1) << 1);
      vo[pc] = row * 3072 + ((((lane & 7) >> 1) ^ swz) << 5) + ((lane & 1) << 4) - pc * 1024;
    }
    vb = p.V + gt * 128 + (size_t)kq * (12 * 32 * 3072);
  }
  __device__ __forceinline__ int kstep(int i) const { const int t = i + rot; return t >= 12 ? t - 12 : t; }
  __device__ __forceinline__ void issue_at(int i, unsigned lds) const {
    glds4(vb + (size_t)kstep(i) * (32 * 3072), vo[0], vo[1], vo[2], vo[3], lds);
  }
  __device__ __forceinline__ void issue(int i) const { issue_at(i, (kq * 10 + i) * 4096); }
};

template <int NB>
__device__ __forceinline__ void w_body(const Params& p, __amdgpu_buffer_rsrc_t wsr, const VLoader& vl, int wave, int lane, int et, int gt) {
  constexpr int M0 = NB == 3 ? 0 : 3, SEC = NB == 3 ? 0 : 1;
  const int kq = wave & 3;
  unsigned vc[4];
  {
    const int g = lane >> 4, qq = (lane & 15) >> 2, sw = ((qq >> 1) & 1) | ((g & 1) << 1);
#pragma unroll
    for (int c = 0; c < 4; ++c) vc[c] = kq * 40960 + 128 * (8 * g + qq) + 32 * (c ^ sw) + 8 * (lane & 3);
  }
  const char* wq = p.ws + WS_WQ + ((et * 48 + kq * 12) * 5 + M0) * 1024;
  const unsigned vl16 = lane * 16;
  u32x4 af[2][NB];
  s16x4 bh[2][8];
  f32x4 acc[NB][4];
#pragma unroll
  for (int m = 0; m < NB; ++m)
#pragma unroll
    for (int c = 0; c < 4; ++c) { acc[m][c] = (f32x4){0.f, 0.f, 0.f, 0.f}; asm volatile("" : "+v"(acc[m][c])); }
  BAR();
  static_for<0, 2 * NB>([&](auto ic) {
    constexpr int n = decltype(ic)::value / NB, m = decltype(ic)::value % NB;
    gload_sc1<m * 1024>(af[n][m], wq + vl.kstep(n) * 5120, vl16);
  });
#pragma unroll
  for (int j = 0; j < 8; ++j) bh[0][j] = lds_tr(vc[j >> 1] + (j & 1) * 512);
  SCHED();
  const unsigned red = 81920 + kq * 20480 + lane * 16;
  const unsigned vlast = (kq & 1) * 40960 + (kq >> 1) * 8192;
  auto iter = [&](int it, auto modec) {
    constexpr int MODE = decltype(modec)::value;
    const unsigned step = it == 4 ? vlast - kq * 40960 - 32768 : 8192;
    unsigned vn[4];
#pragma unroll
    for (int c = 0; c < 4; ++c) { vn[c] = vc[c] + step; opaque(vn[c]); }
    if constexpr (MODE == 1) { BAR(); vl.issue_at(10 + SEC, vlast + SEC * 4096); }
    static_for<0, 2>([&](auto hc) {
      constexpr int hf = decltype(hc)::value;
      if constexpr (hf == 1 && MODE == 0) { if (it == 4) { WAIT_LGKM0(); BAR(); } }
      const char* src = wq + vl.kstep(MODE == 2 ? 0 : 2 * it + 2 + hf) * 5120;
      static_for<0, NB * 4>([&](auto jc) {
        constexpr int j = decltype(jc)::value, m = j / 4, c = j % 4;
        if constexpr (c == 0) {
          constexpr int N = MODE == 1 ? 2 * NB + 3 : MODE == 0 ? 2 * NB - 1 : 2 * NB - 1 - (hf * NB + m);
          wait_for<N>(af[hf][m]);
        }
        acc[m][c] = __builtin_amdgcn_mfma_f32_16x16x32_bf16(__builtin_bit_cast(bf16x8, af[hf][m]), join(bh[hf][2 * c], bh[hf][2 * c + 1]), acc[m][c], 0, 0, 0);
        SCHED();
        if constexpr (j < 8 && !(MODE == 2 && hf == 1))
          bh[hf ^ 1][j] = lds_tr(hf == 0 ? vc[j >> 1] + 4096 + (j & 1) * 512 : vn[j >> 1] + (j & 1) * 512);
        if constexpr (MODE == 2 && hf == 1 && m > 0) lds_wr128(red + ((M0 + m - 1) * 4 + c) * 1024, __builtin_bit_cast(u32x4, acc[m - 1][c]));
        if constexpr (MODE != 2 && c == 3) gload_sc1<m * 1024>(af[hf][m], src, vl16);
        SCHED();
      });
    });
#pragma unroll
    for (int c = 0; c < 4; ++c) vc[c] = vn[c];
  };
#pragma unroll 1
  for (int it = 0; it < 2; ++it) iter(it, IC<0>{});
  iter(2, IC<1>{});
#pragma unroll 1
  for (int it = 3; it < 5; ++it) iter(it, IC<0>{});
  iter(5, IC<2>{});
#pragma unroll
  for (int c = 0; c < 4; ++c) lds_wr128(red + ((M0 + NB - 1) * 4 + c) * 1024, __builtin_bit_cast(u32x4, acc[NB - 1][c]));
  if constexpr (NB == 2) lds_wr128(LW, (u32x4){0u, 0u, 0u, 0u});
  WAIT_LGKM0();
  BAR();
  constexpr int NO = NB == 3 ? 2 : 1;
  const unsigned ol = ((lane & 15) + 16 * (lane >> 5) + 32 * ((lane >> 4) & 1)) * 16;
  f32x4 t[NO][2][4];
#pragma unroll
  for (int j = 0; j < NO; ++j) {
    const int fi = wave + (j == 1 && kq < 2 ? 4 : 0);
    const unsigned rb = 81920 + lane * 16 + fi * 2048;
#pragma unroll
    for (int hf = 0; hf < 2; ++hf)
#pragma unroll
      for (int q = 0; q < 4; ++q) t[j][hf][q] = __builtin_bit_cast(f32x4, lds_rd128(rb + q * 20480 + hf * 1024));
  }
#pragma unroll
  for (int j = 0; j < NO; ++j) {
    const int fi = wave + (j == 1 && kq < 2 ? 4 : 0);
    const f32x4 a = (t[j][0][0] + t[j][0][1]) + (t[j][0][2] + t[j][0][3]), b = (t[j][1][0] + t[j][1][1]) + (t[j][1][2] + t[j][1][3]);
    auto r0 = __builtin_amdgcn_permlane32_swap(pack_bf16(a[0], a[1]), pack_bf16(b[0], b[1]), false, false);
    auto r1 = __builtin_amdgcn_permlane32_swap(pack_bf16(a[2], a[3]), pack_bf16(b[2], b[3]), false, false);
    const u32x4 v = {r0[0], r1[0], r0[1], r1[1]};
    __builtin_amdgcn_raw_buffer_store_b128(v, wsr, ol, WS_W2 + ((2 * gt + (fi & 1)) * 40 + 5 * et + (fi >> 1)) * 1024, AUX_SC1);
  }
}

constexpr int XREG = 30, KS0 = 16, YT = 40 - KS0;
constexpr int y_ks(int n) { return n < 3 * KS0 ? n / 3 : KS0 + (n - 3 * KS0) % YT; }
constexpr int y_tt(int n) { return n < 3 * KS0 ? n % 3 : (n - 3 * KS0) / YT; }
constexpr int y_lds(int n) { return n < 3 * KS0 ? ((n / 12) * 3 + n % 3) * 4096 + ((n % 12) / 3) * 1024 : n * 1024; }
constexpr int y_gt(int g) { return 4 * g < 3 * KS0 ? g % 3 : y_tt(4 * g); }
constexpr int y_gk(int g) { return 4 * g < 3 * KS0 ? (g / 3) * 4 : y_ks(4 * g); }

__device__ __forceinline__ void y_load(const Params& p, int wave, int lane, int st, int gt, u64 tag) {
  const int lw = wave - 4;
  u64 fa = 0, fb = 0, fc = 0, fd = 0;
  asm volatile("" : "+v"(fa), "+v"(fb), "+v"(fc), "+v"(fd));
  if (lw == 0) {
    __builtin_amdgcn_s_sleep(YDELAY);
    wait_flags(p.ws, WS_WF + ((3 * gt / 2) * 8 + (lane & 15)) * FS, tag);
    lds_set(LW);
  } else lds_wait(LW);
  const char* wb = p.ws + WS_W2 + (size_t)(3 * gt * 40 + lw) * 1024;
  const char* xb = p.ws + WS_XR + (size_t)(st * 4 + lw) * 40960;
  const unsigned vo = lane * 16;
  auto issue = [&](auto cc) {
    static_for<3 * decltype(cc)::value, 3 * decltype(cc)::value + 3>([&](auto gc) {
      constexpr int g = decltype(gc)::value;
      glds<0, true>(wb + (y_gt(g) * 40 + y_gk(g)) * 1024, vo, g * 4096 + lw * 1024);
    });
  };
  issue(IC<0>{}); issue(IC<1>{}); issue(IC<2>{});
  WAIT_VM(6); BAR();
  issue(IC<3>{}); issue(IC<4>{});
  WAIT_VM(9); BAR();
  const unsigned xl = 122880 + lw * 10240;
  glds<0, true>(xb + 30 * 1024, vo, xl); glds<1024, true>(xb + 30 * 1024, vo, xl);
  glds4_lin_sc1(xb + 32 * 1024, vo, xl + 2048);
  glds4_lin_sc1(xb + 36 * 1024, vo, xl + 6144);
  issue(IC<5>{});
  WAIT_VM(16); BAR();
  const bool adv = blockIdx.x == 0 && lw == 0;
  gu64* f = (gu64*)(p.ws + WS_XF + lane * FS);
  if (adv) {
    fa = __hip_atomic_load(f, RLX_AGENT); fb = __hip_atomic_load(f + 64 * FS / 8, RLX_AGENT);
    fc = __hip_atomic_load(f + 128 * FS / 8, RLX_AGENT); fd = __hip_atomic_load(f + 192 * FS / 8, RLX_AGENT);
  }
  FENCE();
  issue(IC<6>{}); issue(IC<7>{});
  WAIT_VM(6); BAR();
  issue(IC<8>{}); issue(IC<9>{});
  WAIT_VM(6); BAR();
  WAIT_VM(0);
  asm volatile("" : "+v"(fa), "+v"(fb), "+v"(fc), "+v"(fd));
  const bool all_up = __all(fa == tag && fb == tag && fc == tag && fd == tag);
  if (adv && all_up) raise_flag(p.ws, WS_EPOCH, tag, lane);
  BAR();
  if (adv && !all_up) {
#pragma unroll 1
    for (int i = 0; i < 4; ++i) wait_flags(p.ws, WS_XF + (i * 64 + lane) * FS, tag);
    raise_flag(p.ws, WS_EPOCH, tag, lane);
  }
}

__device__ __forceinline__ void y_compute(const Params& p, __amdgpu_buffer_rsrc_t wsr, int wave, int lane, int st, int gt) {
  u32x4 xf[XREG];
  const unsigned xbase = WS_XR + (st * 4 + wave) * 40960;
#pragma unroll
  for (int ks = 0; ks < XREG; ++ks) xf[ks] = __builtin_amdgcn_raw_buffer_load_b128(wsr, lane * 16 + (ks & 3) * 1024, xbase + (ks >> 2) * 4096, AUX_SC1);
  FENCE();
  f32x16 acc[3];
#pragma unroll
  for (int t = 0; t < 3; ++t)
#pragma unroll
    for (int j = 0; j < 16; ++j) acc[t][j] = 0.f;
  constexpr int L = 6;
  bf16x8 aw[L + 1], bx[40 - XREG];
  const unsigned a0 = lane * 16, x0 = 122880 + wave * 10240 + lane * 16;
  unsigned a1 = lane * 16 + 61440;
  opaque(a1);
  const __amdgpu_buffer_rsrc_t yr = __builtin_amdgcn_make_buffer_rsrc(p.y, 0, S_ * F_ * 2, 0x00020000);
  const unsigned ew = wave * 8192 + (lane & 31) * 80 + (lane >> 5) * 8, er = wave * 8192 + (lane >> 2) * 80 + (lane & 3) * 16;
  const unsigned yo = (lane >> 2) * 3072 + (lane & 3) * 16, ys = (st * 128 + wave * 32) * 3072 + gt * 192;
  u32x4 eo[2];
  auto epi = [&](auto tc, auto sc) {
    constexpr int t = decltype(tc)::value, e = decltype(sc)::value;
    if constexpr (e < 2) {
#pragma unroll
      for (int i = 2 * e; i < 2 * e + 2; ++i) {
        const u32x2 v = {pack_bf16(acc[t][4 * i], acc[t][4 * i + 1]), pack_bf16(acc[t][4 * i + 2], acc[t][4 * i + 3])};
        *(LAS u32x2*)(size_t)(ew + t * 2560 + i * 16) = v;
      }
      if constexpr (e == 1) { eo[0] = lds_rd128(er + t * 2560); eo[1] = lds_rd128(er + t * 2560 + 1280); }
    } else if constexpr (e >= 5) {
      __builtin_amdgcn_raw_buffer_store_b128(eo[e - 5], yr, yo + (e - 5) * (16 * 3072) + t * 64, ys, t == 2 ? AUX_SC1 : AUX_NT_SC1);
    }
  };
  static_for<0, 120 + L>([&](auto ic) {
    constexpr int n = decltype(ic)::value, j = n - L;
    if constexpr (n < 120) {
      if constexpr (n == 0 || n == 12 || n == 24 || n == 48 || n == 72 || n == 96) BAR();
      if constexpr (y_tt(n) == 0 && y_ks(n) >= XREG) bx[y_ks(n) - XREG] = __builtin_bit_cast(bf16x8, lds_rd128(x0 + (y_ks(n) - XREG) * 1024));
      aw[n % (L + 1)] = __builtin_bit_cast(bf16x8, lds_rd128(y_lds(n) < 61440 ? a0 + y_lds(n) : a1 + (y_lds(n) - 61440)));
    }
    if constexpr (j >= 0) {
      constexpr int ks = y_ks(j), t = y_tt(j);
      if constexpr (ks < XREG) acc[t] = __builtin_amdgcn_mfma_f32_32x32x16_bf16(aw[j % (L + 1)], __builtin_bit_cast(bf16x8, xf[ks]), acc[t], 0, 0, 0);
      else acc[t] = __builtin_amdgcn_mfma_f32_32x32x16_bf16(aw[j % (L + 1)], bx[ks - XREG], acc[t], 0, 0, 0);
      SCHED();
      if constexpr (j >= 3 * KS0 && t >= 1 && ks - KS0 >= 1 && ks - KS0 <= 7) epi(IC<t - 1>{}, IC<ks - KS0 - 1>{});
    }
    SCHED();
  });
  static_for<0, 7>([&](auto sc) { epi(IC<2>{}, sc); });
}

__global__ void __launch_bounds__(512, 2) quip_kernel(Params p) {
  const int wave = __builtin_amdgcn_readfirstlane(threadIdx.x >> 6), lane = threadIdx.x & 63;
  const int xcd = blockIdx.x & 7, idx = blockIdx.x >> 3;
  unsigned pf = 0, pf2 = 0;
  {
    const unsigned ln = idx * 8 + wave;
    if (ln < CODEKB * 8) pf = __hip_atomic_load((const GAS unsigned*)((__builtin_amdgcn_s_getpc() & ~255ull) + ln * 128), RLX_WG);
  }
  const bool isx = idx < 8;
  const int xst = 4 * (xcd >> 1) + (idx >> 1), etl = 2 * (xcd & 1) + (idx & 1);
  const int wk = idx - 8, et = wk & 7, gt = 3 * xcd + (wk >> 3);
  const int yst = 4 * (xcd >> 1) + (idx & 3), ygt = 8 * (xcd & 1) + (idx >> 2);
  XLoader xl; VLoader vl;
  if (!isx && wave < 4) d_issue(p, wave, lane, xcd, wk);
  if (isx && wave >= 4) { xl.init(p, xst, etl, wave - 4, lane); xl.issue(0); xl.issue(1); }
  vl.init(p, gt, wave & 3, 3 * et >> 1, lane);
  if (!isx && wave >= 4) {
    vl.issue(0); vl.issue(1);
    const unsigned n = (wk * 4 + wave - 4) * 35 + (lane < 35 ? lane : 0), lu = n - 2560, ur = (lu * 13108) >> 16;
    const char* a = n < 2560 ? p.x + (size_t)xcd * (256 * 1280) + n * 128
                             : p.U + (xcd & 1) * 640 + ((xcd >> 1) * 160 + ur) * 1280 + (lu - ur * 5) * 128;
    FENCE();
    pf2 = __hip_atomic_load((const GAS unsigned*)a, RLX_WG);
    FENCE();
  }
  const __amdgpu_buffer_rsrc_t wsr = __builtin_amdgcn_make_buffer_rsrc(p.ws, 0, WS_END, 0x00020000);
  const unsigned xfl = WS_XF + (yst * 4 + (lane & 3)) * FS;
  u64 tag;
  if (wave < 4) {
    bool ok = false;
    if (isx) {
      tag = x_compute(p, wsr, wave, lane, xst, etl, idx == 0 && wave == 0);
      asm volatile("" :: "v"(pf));
    } else {
      WAIT_VM(0);
      const u64 ev = load_epoch_v(p);
      d_compute(wsr, wave, lane, xcd, wk);
      WAIT_VM(0);
      tag = make_tag_v(ev);
      asm volatile("" :: "v"(pf));
      BAR();
      if (wave == 0) raise_flag(p.ws, WS_DF + (xcd * 24 + wk) * FS, tag, lane);
      w_body<2>(p, wsr, vl, wave, lane, et, gt);
      if (wave == 0) ok = __all(__hip_atomic_load((gu64*)(p.ws + xfl), RLX_AGENT) == tag);
      if (ok) lds_set(LW + 4);
      WAIT_VM(0);
      WAIT_LGKM0();
      BAR();
    }
    if (wave == 0) { if (!ok) { wait_flags(p.ws, xfl, tag); lds_set(LW + 4); } } else lds_wait(LW + 4);
    y_compute(p, wsr, wave, lane, yst, ygt);
  } else {
    if (isx) {
      tag = xl.run(p);
      asm volatile("" :: "v"(pf));
      __builtin_amdgcn_s_setprio(3);
    } else {
      BAR();
      tag = load_epoch_s(p);
#pragma unroll 1
      for (int i = 2; i < 10; ++i) vl.issue(i);
      make_tag_s(tag);
      if (wave == 4) wait_flags(p.ws, WS_DF + (et * 24 + (lane < 24 ? lane : 0)) * FS, tag);
      WAIT_VM(0);
      asm volatile("" :: "v"(pf), "v"(pf2));
      w_body<3>(p, wsr, vl, wave, lane, et, gt);
      __builtin_amdgcn_s_setprio(3);
      WAIT_VM(0);
      BAR();
      if (wave == 4) raise_flag(p.ws, WS_WF + (gt * 8 + et) * FS, tag, lane);
    }
    y_load(p, wave, lane, yst, ygt, tag);
  }
}

extern "C" void kernel_launch(void* const* d_in, const int* in_sizes, int n_in, void* d_out, int out_size, void* d_ws,
                              size_t ws_size, hipStream_t stream) {
  Params p;
  p.x = (const char*)d_in[0]; p.qw = (const char*)d_in[1]; p.sc = (const char*)d_in[2]; p.zr = (const char*)d_in[3];
  p.U = (const char*)d_in[4]; p.V = (const char*)d_in[5]; p.y = (char*)d_out; p.ws = (char*)d_ws;
  (void)hipFuncSetAttribute((const void*)quip_kernel, hipFuncAttributeMaxDynamicSharedMemorySize, LDS_BYTES);
  quip_kernel<<<256, 512, LDS_BYTES, stream>>>(p);
}
```

```cpp
#include <hip/hip_runtime.h>
#include <stdint.h>

#define LAS __attribute__((address_space(3)))
#define GAS __attribute__((address_space(1)))
typedef unsigned short u16;
typedef unsigned long long u64;
typedef short s16x4 __attribute__((ext_vector_type(4)));
typedef __bf16 bf16x8 __attribute__((ext_vector_type(8)));
typedef __bf16 bf16x2 __attribute__((ext_vector_type(2)));
typedef float f32x2 __attribute__((ext_vector_type(2)));
typedef float f32x4 __attribute__((ext_vector_type(4)));
typedef float f32x16 __attribute__((ext_vector_type(16)));
typedef unsigned u32x2 __attribute__((ext_vector_type(2)));
typedef unsigned u32x4 __attribute__((ext_vector_type(4)));
typedef int i32x4 __attribute__((ext_vector_type(4)));
typedef GAS u64 gu64;
typedef LAS unsigned lu32;

constexpr int S_ = 2048, D_ = 640, F_ = 1536;
constexpr unsigned WS_EPOCH = 0;
constexpr unsigned FS = 128;
constexpr unsigned WS_DF = 4096;
constexpr unsigned WS_XF = WS_DF + 192 * FS;
constexpr unsigned WS_WF = WS_XF + 64 * FS;
constexpr unsigned WS_WQ = 1u << 20;
constexpr unsigned WS_XR = WS_WQ + D_ * F_ * 2;
constexpr unsigned WS_W2 = WS_XR + S_ * D_ * 2;
constexpr unsigned WS_END = WS_W2 + D_ * F_ * 2;
constexpr int LDS_BYTES = 160 * 1024;
constexpr unsigned LW = 79 * 1024;
constexpr int AUX_SC1 = 16, AUX_NT_SC1 = 18;
constexpr unsigned CODEKB = 18;
constexpr int YDELAY = 5;

struct Params {
  const char* x; const char* qw; const char* sc; const char* zr; const char* U; const char* V; char* y; char* ws;
};

#define RLX_AGENT __ATOMIC_RELAXED, __HIP_MEMORY_SCOPE_AGENT
#define RLX_WG __ATOMIC_RELAXED, __HIP_MEMORY_SCOPE_WORKGROUP
#define WAIT_VM(n) asm volatile("s_waitcnt vmcnt(%0)" :: "n"(n) : "memory")
#define WAIT_LGKM0() asm volatile("s_waitcnt lgkmcnt(0)" ::: "memory")
#define FENCE() asm volatile("" ::: "memory")
#define BAR() do { FENCE(); __builtin_amdgcn_s_barrier(); FENCE(); } while (0)
#define SCHED() __builtin_amdgcn_sched_barrier(0)

template <int I> struct IC { static constexpr int value = I; };
template <int B, int E, class Fn>
__device__ __forceinline__ void static_for(Fn&& fn) {
  if constexpr (B < E) {
    if constexpr (E - B == 1) fn(IC<B>{});
    else { static_for<B, (B + E) / 2>(fn); static_for<(B + E) / 2, E>(fn); }
  }
}

template <int OFF, bool SC1>
__device__ __forceinline__ void glds(const char* sbase, unsigned voff, unsigned lds) {
  if constexpr (SC1)
    asm volatile("s_mov_b32 m0, %2\n\ts_nop 0\n\tglobal_load_lds_dwordx4 %0, %1 offset:%3 sc1" :: "v"(voff), "s"(sbase), "s"(lds), "n"(OFF) : "memory");
  else
    asm volatile("s_mov_b32 m0, %2\n\ts_nop 0\n\tglobal_load_lds_dwordx4 %0, %1 offset:%3" :: "v"(voff), "s"(sbase), "s"(lds), "n"(OFF) : "memory");
}
__device__ __forceinline__ void glds4(const char* sbase, unsigned v0, unsigned v1, unsigned v2, unsigned v3, unsigned lds) {
  asm volatile("s_mov_b32 m0, %5\n\ts_nop 0\n\tglobal_load_lds_dwordx4 %0, %4\n\tglobal_load_lds_dwordx4 %1, %4 offset:1024\n\t"
               "global_load_lds_dwordx4 %2, %4 offset:2048\n\tglobal_load_lds_dwordx4 %3, %4 offset:3072"
               :: "v"(v0), "v"(v1), "v"(v2), "v"(v3), "s"(sbase), "s"(lds) : "memory");
}
__device__ __forceinline__ void glds4_nt(const char* sbase, unsigned v0, unsigned v1, unsigned v2, unsigned v3, unsigned lds) {
  asm volatile("s_mov_b32 m0, %5\n\ts_nop 0\n\tglobal_load_lds_dwordx4 %0, %4 nt\n\tglobal_load_lds_dwordx4 %1, %4 offset:1024 nt\n\t"
               "global_load_lds_dwordx4 %2, %4 offset:2048 nt\n\tglobal_load_lds_dwordx4 %3, %4 offset:3072 nt"
               :: "v"(v0), "v"(v1), "v"(v2), "v"(v3), "s"(sbase), "s"(lds) : "memory");
}
__device__ __forceinline__ void glds2_nt(const char* sbase, unsigned v0, unsigned v1, unsigned lds) {
  asm volatile("s_mov_b32 m0, %3\n\ts_nop 0\n\tglobal_load_lds_dwordx4 %0, %2 nt\n\tglobal_load_lds_dwordx4 %1, %2 offset:1024 nt"
               :: "v"(v0), "v"(v1), "s"(sbase), "s"(lds) : "memory");
}
__device__ __forceinline__ void glds4_lin_sc1(const char* sbase, unsigned v, unsigned lds) {
  asm volatile("s_mov_b32 m0, %2\n\ts_nop 0\n\tglobal_load_lds_dwordx4 %0, %1 sc1\n\tglobal_load_lds_dwordx4 %0, %1 offset:1024 sc1\n\t"
               "global_load_lds_dwordx4 %0, %1 offset:2048 sc1\n\tglobal_load_lds_dwordx4 %0, %1 offset:3072 sc1"
               :: "v"(v), "s"(sbase), "s"(lds) : "memory");
}
__device__ __forceinline__ void glds_v(const char* vaddr, unsigned lds) {
  asm volatile("s_mov_b32 m0, %1\n\ts_nop 0\n\tglobal_load_lds_dwordx4 %0, off" :: "v"(vaddr), "s"(lds) : "memory");
}
template <int OFF>
__device__ __forceinline__ void gload_sc1(u32x4& d, const char* sbase, unsigned voff) {
  asm volatile("global_load_dwordx4 %0, %1, %2 offset:%3 sc1" : "=v"(d) : "v"(voff), "s"(sbase), "n"(OFF) : "memory");
}
template <int N>
__device__ __forceinline__ void wait_for(u32x4& d) { asm volatile("s_waitcnt vmcnt(%1)" : "+v"(d) : "n"(N) : "memory"); }
__device__ __forceinline__ void opaque(unsigned& v) { asm volatile("" : "+v"(v)); }

__device__ __forceinline__ unsigned pack_bf16(float a, float b) {
  f32x2 v = {a, b};
  return __builtin_bit_cast(unsigned, __builtin_convertvector(v, bf16x2));
}
__device__ __forceinline__ float bf_lo(unsigned v) { return __builtin_bit_cast(float, v << 16); }
__device__ __forceinline__ float bf_hi(unsigned v) { return __builtin_bit_cast(float, v & 0xffff0000u); }

__device__ __forceinline__ u32x4 lds_rd128(unsigned a) { return *(LAS u32x4*)(size_t)a; }
__device__ __forceinline__ void lds_wr128(unsigned a, u32x4 v) { *(LAS u32x4*)(size_t)a = v; }
__device__ __forceinline__ s16x4 lds_tr(unsigned a) { return __builtin_amdgcn_ds_read_tr16_b64_v4i16((LAS s16x4*)(size_t)a); }
__device__ __forceinline__ bf16x8 join(s16x4 lo, s16x4 hi) { return __builtin_bit_cast(bf16x8, __builtin_shufflevector(lo, hi, 0, 1, 2, 3, 4, 5, 6, 7)); }

constexpr u64 TAG_STEP = 0x9E3779B97F4A7C15ull;
__device__ __forceinline__ u64 load_epoch_s(const Params& p) {
  const char* e = p.ws + WS_EPOCH;
  asm volatile("" : "+s"(e));
  return *(const __attribute__((address_space(4))) u64*)e;
}
__device__ __forceinline__ void make_tag_s(u64& e) {
  asm volatile("" : "+s"(e));
  e += TAG_STEP;
  asm volatile("" : "+s"(e));
}
__device__ __forceinline__ u64 load_epoch_v(const Params& p) { FENCE(); const u64 e = __hip_atomic_load((gu64*)(p.ws + WS_EPOCH), RLX_AGENT); FENCE(); return e; }
__device__ __forceinline__ u64 make_tag_v(u64 e) {
  asm volatile("" : "+v"(e));
  u64 u = ((u64)(unsigned)__builtin_amdgcn_readfirstlane((unsigned)(e >> 32)) << 32) | (unsigned)__builtin_amdgcn_readfirstlane((unsigned)e);
  u += TAG_STEP;
  asm volatile("" : "+s"(u));
  return u;
}

__device__ __forceinline__ void wait_flags(const char* ws, unsigned off, u64 tag) {
  gu64* f = (gu64*)(ws + off);
#pragma unroll 1
  for (unsigned spins = 0; spins < (1u << 20); ++spins) {
    const u64 v = __hip_atomic_load(f, RLX_AGENT);
    if (__all(v == tag)) break;
    __builtin_amdgcn_s_sleep(2);
  }
  FENCE();
}
__device__ __forceinline__ void raise_flag(char* ws, unsigned off, u64 tag, int lane) {
  FENCE();
  if (lane < 16) __hip_atomic_store((gu64*)(ws + off + lane * 8), tag, RLX_AGENT);
  FENCE();
}
__device__ __forceinline__ void lds_set(unsigned a) { FENCE(); __hip_atomic_store((lu32*)(size_t)a, 1u, RLX_WG); FENCE(); }
__device__ __forceinline__ void lds_wait(unsigned a) {
#pragma unroll 1
  for (unsigned spins = 0; spins < (1u << 24); ++spins) {
    if (__builtin_amdgcn_readfirstlane(__hip_atomic_load((lu32*)(size_t)a, RLX_WG))) break;
    __builtin_amdgcn_s_sleep(1);
  }
  FENCE();
}

__device__ __forceinline__ unsigned dequant2(int q0, int q1, float z0, float z1, float s0, float s1) {
  const unsigned qb = pack_bf16((float)q0, (float)q1);
  const unsigned t = pack_bf16(bf_lo(qb) - z0, bf_hi(qb) - z1);
  return pack_bf16(bf_lo(t) * s0, bf_hi(t) * s1);
}

constexpr unsigned XSTG = 36 * 1024, XUOFF = 16 * 1024;
struct XLoader {
  unsigned xo[4], uo[5]; const char* xb; const char* ub; int lw;
  __device__ __forceinline__ void init(const Params& p, int st, int etl, int lw_, int lane) {
    lw = lw_;
#pragma unroll
    for (int i = 0; i < 4; ++i) {
      const int row = 8 * i + (lane >> 3);
      xo[i] = row * 1280 + (((lane & 7) ^ ((row >> 1) & 7)) << 4) - i * 1024;
    }
#pragma unroll
    for (int i = 0; i < 5; ++i) {
      const int u = i * 64 + lane, row = (u * 3277) >> 16;
      uo[i] = row * 960 + u * 16 - (i & 3) * 1024;
    }
    xb = p.x + (size_t)(st * 128 + lw * 32) * 1280;
    ub = p.U + etl * 320 + lw * (16 * 1280);
  }
  __device__ __forceinline__ void issue(int i) {
    const unsigned slot = (i & 3) * XSTG;
    const char* us = ub + (size_t)i * (64 * 1280);
    glds4(us, uo[0], uo[1], uo[2], uo[3], slot + XUOFF + lw * 5120);
    glds<0, false>(us, uo[4], slot + XUOFF + lw * 5120 + 4096);
    glds4(xb + i * 128, xo[0], xo[1], xo[2], xo[3], slot + lw * 4096);
  }
  __device__ __forceinline__ u64 run(const Params& p) {
    WAIT_VM(9); BAR();
    u64 tag = load_epoch_s(p);
#pragma unroll 1
    for (int i = 1; i < 9; ++i) {
      issue(i + 1);
      WAIT_VM(9); BAR();
    }
    make_tag_s(tag);
    WAIT_VM(0); BAR();
    lds_wr128(LW, (u32x4){0u, 0u, 0u, 0u});
    WAIT_LGKM0();
    BAR();
    BAR();
    return tag;
  }
};

__device__ __forceinline__ u64 x_compute(const Params& p, __amdgpu_buffer_rsrc_t wsr, int wave, int lane, int st, int etl, bool first) {
  const int r = lane & 31, h = lane >> 5;
  unsigned xa0[4];
#pragma unroll
  for (int kk = 0; kk < 4; ++kk) xa0[kk] = wave * 4096 + r * 128 + (((2 * kk + h) ^ ((r >> 1) & 7)) << 4);
  const unsigned ua0 = XUOFF + (8 * h + ((lane & 15) >> 2)) * 320 + ((lane >> 4) & 1) * 32 + (lane & 3) * 8;
  f32x16 acc[5];
#pragma unroll
  for (int t = 0; t < 5; ++t)
#pragma unroll
    for (int j = 0; j < 16; ++j) acc[t][j] = 0.f;
  constexpr int L = 6;
  bf16x8 au[10], bx[4];
#pragma unroll
  for (int t = 0; t < 5; ++t) asm volatile("" : "+v"(acc[t]));
  u64 ev = 0;
  if (first) ev = load_epoch_v(p);
  BAR();
  if (!first) ev = load_epoch_v(p);
#pragma unroll
  for (int n = 0; n < L; ++n) {
    if (n % 5 == 0) bx[n / 5] = __builtin_bit_cast(bf16x8, lds_rd128(xa0[n / 5]));
    SCHED();
    const unsigned ad = ua0 + (n / 5) * 16 * 320 + (n % 5) * 64;
    au[n] = join(lds_tr(ad), lds_tr(ad + 4 * 320));
    SCHED();
  }
#pragma unroll 1
  for (int i = 0; i < 10; ++i) {
    const unsigned cur = (i & 3) * XSTG, nxt = ((i + 1) & 3) * XSTG;
    const unsigned ua = ua0 + cur, un = ua0 + nxt;
    static_for<0, 20>([&](auto jc) {
      constexpr int j = decltype(jc)::value, n = j + L;
      if constexpr (n == 20) BAR();
      constexpr int kk = (n % 20) / 5, t = n % 5;
      if constexpr (t == 0) { bx[kk] = __builtin_bit_cast(bf16x8, lds_rd128(xa0[kk] + (n < 20 ? cur : nxt))); SCHED(); }
      const unsigned ad = (n < 20 ? ua : un) + kk * 16 * 320 + t * 64;
      au[n % 10] = join(lds_tr(ad), lds_tr(ad + 4 * 320));
      acc[j % 5] = __builtin_amdgcn_mfma_f32_32x32x16_bf16(au[j % 10], bx[j / 5], acc[j % 5], 0, 0, 0);
      SCHED();
    });
  }
  const unsigned ob = WS_XR + ((st * 4 + wave) * 40 + etl * 10) * 1024;
#pragma unroll
  for (int t = 0; t < 5; ++t)
#pragma unroll
    for (int s2 = 0; s2 < 2; ++s2) {
      u32x4 v;
#pragma unroll
      for (int j = 0; j < 4; ++j) v[j] = pack_bf16(acc[t][8 * s2 + 2 * j], acc[t][8 * s2 + 2 * j + 1]);
      __builtin_amdgcn_raw_buffer_store_b128(v, wsr, lane * 16, ob + (2 * t + s2) * 1024, AUX_SC1);
    }
  WAIT_LGKM0();
  WAIT_VM(0);
  const u64 tag = make_tag_v(ev);
  BAR();
  if (wave == 0) raise_flag(p.ws, WS_XF + (st * 4 + etl) * FS, tag, lane);
  return tag;
}

__device__ __forceinline__ void d_issue(const Params& p, int wave, int lane, int det, int dkp) {
  const unsigned dl = (wave * 10 + 8) * 4096;
  const int k2 = wave & 1, nfr = wave < 2 ? 3 : 2;
  unsigned qo[2];
#pragma unroll
  for (int i = 0; i < 2; ++i) {
    const int row = 8 * i + (lane >> 3);
    qo[i] = row * 6144 + (((lane & 7) ^ ((row >> 1) & 7)) << 4) - i * 1024;
  }
  const char* qb = p.qw + (size_t)(80 * det + 16 * (wave >> 1)) * 6144 + (64 * dkp + 32 * k2) * 4;
  glds4_nt(qb, qo[0], qo[1], qo[0] + (32 * 6144 - 2048), qo[1] + (32 * 6144 - 2048), dl);
  if (wave < 2) glds2_nt(qb + 64 * 6144, qo[0], qo[1], dl + 4096);
  int i = (lane >> 3) & 3; if (i >= nfr) i = 0;
  const int grp = (5 * det + (wave >> 1) + 2 * i) >> 1;
  const char* sa = (((lane >> 2) & 1) ? p.zr : p.sc) + (size_t)grp * 3072 + (64 * dkp + 32 * k2) * 2 + (lane & 3) * 16;
  glds_v(sa, dl + 6144);
}

__device__ __forceinline__ void d_compute(__amdgpu_buffer_rsrc_t wsr, int wave, int lane, int det, int dkp) {
  const unsigned dl = (wave * 10 + 8) * 4096;
  const int r = lane & 15, q = lane >> 4;
  const unsigned a0 = dl + r * 128 + (((2 * q) ^ ((r >> 1) & 7)) << 4), a1 = dl + r * 128 + (((2 * q + 1) ^ ((r >> 1) & 7)) << 4);
  const unsigned as = dl + 6144 + q * 16;
  const unsigned ob = WS_WQ + ((det * 48 + 2 * dkp + (wave & 1)) * 5 + (wave >> 1)) * 1024;
  const int nfr = wave < 2 ? 3 : 2;
  i32x4 q0 = __builtin_bit_cast(i32x4, lds_rd128(a0)), q1 = __builtin_bit_cast(i32x4, lds_rd128(a1));
  u32x4 s = lds_rd128(as), z = lds_rd128(as + 64);
#pragma unroll 1
  for (int i = 0; i < nfr; ++i) {
    const i32x4 nq0 = __builtin_bit_cast(i32x4, lds_rd128(a0 + i * 2048 + 2048)), nq1 = __builtin_bit_cast(i32x4, lds_rd128(a1 + i * 2048 + 2048));
    const u32x4 ns = lds_rd128(as + i * 128 + 128), nz = lds_rd128(as + i * 128 + 192);
    u32x4 o;
    o[0] = dequant2(q0[0], q0[1], bf_lo(z[0]), bf_hi(z[0]), bf_lo(s[0]), bf_hi(s[0]));
    o[1] = dequant2(q0[2], q0[3], bf_lo(z[1]), bf_hi(z[1]), bf_lo(s[1]), bf_hi(s[1]));
    o[2] = dequant2(q1[0], q1[1], bf_lo(z[2]), bf_hi(z[2]), bf_lo(s[2]), bf_hi(s[2]));
    o[3] = dequant2(q1[2], q1[3], bf_lo(z[3]), bf_hi(z[3]), bf_lo(s[3]), bf_hi(s[3]));
    __builtin_amdgcn_raw_buffer_store_b128(o, wsr, lane * 16, ob + i * 2048, AUX_SC1);
    q0 = nq0; q1 = nq1; s = ns; z = nz;
  }
}

struct VLoader {
  unsigned vo[4]; const char* vb; int rot, kq;
  __device__ __forceinline__ void init(const Params& p, int gt, int kq_, int rot_, int lane) {
    kq = kq_; rot = rot_;
#pragma unroll
    for (int pc = 0; pc < 4; ++pc) {
      const int row = 8 * pc + (lane >> 3), swz = ((row >> 1) & 1) | (((row >> 3) & 1) << 1);
      vo[pc] = row * 3072 + ((((lane & 7) >> 1) ^ swz) << 5) + ((lane & 1) << 4) - pc * 1024;
    }
    vb = p.V + gt * 128 + (size_t)kq * (12 * 32 * 3072);
  }
  __device__ __forceinline__ int kstep(int i) const { const int t = i + rot; return t >= 12 ? t - 12 : t; }
  __device__ __forceinline__ void issue_at(int i, unsigned lds) const {
    glds4(vb + (size_t)kstep(i) * (32 * 3072), vo[0], vo[1], vo[2], vo[3], lds);
  }
  __device__ __forceinline__ void issue(int i) const { issue_at(i, (kq * 10 + i) * 4096); }
};

template <int NB>
__device__ __forceinline__ void w_body(const Params& p, __amdgpu_buffer_rsrc_t wsr, const VLoader& vl, int wave, int lane, int et, int gt) {
  constexpr int M0 = NB == 3 ? 0 : 3, SEC = NB == 3 ? 0 : 1;
  const int kq = wave & 3;
  unsigned vc[4];
  {
    const int g = lane >> 4, qq = (lane & 15) >> 2, sw = ((qq >> 1) & 1) | ((g & 1) << 1);
#pragma unroll
    for (int c = 0; c < 4; ++c) vc[c] = kq * 40960 + 128 * (8 * g + qq) + 32 * (c ^ sw) + 8 * (lane & 3);
  }
  const char* wq = p.ws + WS_WQ + ((et * 48 + kq * 12) * 5 + M0) * 1024;
  const unsigned vl16 = lane * 16;
  u32x4 af[2][NB];
  s16x4 bh[2][8];
  f32x4 acc[NB][4];
#pragma unroll
  for (int m = 0; m < NB; ++m)
#pragma unroll
    for (int c = 0; c < 4; ++c) { acc[m][c] = (f32x4){0.f, 0.f, 0.f, 0.f}; asm volatile("" : "+v"(acc[m][c])); }
  BAR();
  static_for<0, 2 * NB>([&](auto ic) {
    constexpr int n = decltype(ic)::value / NB, m = decltype(ic)::value % NB;
    gload_sc1<m * 1024>(af[n][m], wq + vl.kstep(n) * 5120, vl16);
  });
#pragma unroll
  for (int j = 0; j < 8; ++j) bh[0][j] = lds_tr(vc[j >> 1] + (j & 1) * 512);
  SCHED();
  const unsigned red = 81920 + kq * 20480 + lane * 16;
  const unsigned vlast = (kq & 1) * 40960 + (kq >> 1) * 8192;
  auto iter = [&](int it, auto modec) {
    constexpr int MODE = decltype(modec)::value;
    const unsigned step = it == 4 ? vlast - kq * 40960 - 32768 : 8192;
    unsigned vn[4];
#pragma unroll
    for (int c = 0; c < 4; ++c) { vn[c] = vc[c] + step; opaque(vn[c]); }
    if constexpr (MODE == 1) { BAR(); vl.issue_at(10 + SEC, vlast + SEC * 4096); }
    static_for<0, 2>([&](auto hc) {
      constexpr int hf = decltype(hc)::value;
      if constexpr (hf == 1 && MODE == 0) { if (it == 4) { WAIT_LGKM0(); BAR(); } }
      const char* src = wq + vl.kstep(MODE == 2 ? 0 : 2 * it + 2 + hf) * 5120;
      static_for<0, NB * 4>([&](auto jc) {
        constexpr int j = decltype(jc)::value, m = j / 4, c = j % 4;
        if constexpr (c == 0) {
          constexpr int N = MODE == 1 ? 2 * NB + 3 : MODE == 0 ? 2 * NB - 1 : 2 * NB - 1 - (hf * NB + m);
          wait_for<N>(af[hf][m]);
        }
        acc[m][c] = __builtin_amdgcn_mfma_f32_16x16x32_bf16(__builtin_bit_cast(bf16x8, af[hf][m]), join(bh[hf][2 * c], bh[hf][2 * c + 1]), acc[m][c], 0, 0, 0);
        SCHED();
        if constexpr (j < 8 && !(MODE == 2 && hf == 1))
          bh[hf ^ 1][j] = lds_tr(hf == 0 ? vc[j >> 1] + 4096 + (j & 1) * 512 : vn[j >> 1] + (j & 1) * 512);
        if constexpr (MODE == 2 && hf == 1 && m > 0) lds_wr128(red + ((M0 + m - 1) * 4 + c) * 1024, __builtin_bit_cast(u32x4, acc[m - 1][c]));
        if constexpr (MODE != 2 && c == 3) gload_sc1<m * 1024>(af[hf][m], src, vl16);
        SCHED();
      });
    });
#pragma unroll
    for (int c = 0; c < 4; ++c) vc[c] = vn[c];
  };
#pragma unroll 1
  for (int it = 0; it < 2; ++it) iter(it, IC<0>{});
  iter(2, IC<1>{});
#pragma unroll 1
  for (int it = 3; it < 5; ++it) iter(it, IC<0>{});
  iter(5, IC<2>{});
#pragma unroll
  for (int c = 0; c < 4; ++c) lds_wr128(red + ((M0 + NB - 1) * 4 + c) * 1024, __builtin_bit_cast(u32x4, acc[NB - 1][c]));
  if constexpr (NB == 2) lds_wr128(LW, (u32x4){0u, 0u, 0u, 0u});
  WAIT_LGKM0();
  BAR();
  constexpr int NO = NB == 3 ? 2 : 1;
  const unsigned ol = ((lane & 15) + 16 * (lane >> 5) + 32 * ((lane >> 4) & 1)) * 16;
  f32x4 t[NO][2][4];
#pragma unroll
  for (int j = 0; j < NO; ++j) {
    const int fi = wave + (j == 1 && kq < 2 ? 4 : 0);
    const unsigned rb = 81920 + lane * 16 + fi * 2048;
#pragma unroll
    for (int hf = 0; hf < 2; ++hf)
#pragma unroll
      for (int q = 0; q < 4; ++q) t[j][hf][q] = __builtin_bit_cast(f32x4, lds_rd128(rb + q * 20480 + hf * 1024));
  }
#pragma unroll
  for (int j = 0; j < NO; ++j) {
    const int fi = wave + (j == 1 && kq < 2 ? 4 : 0);
    const f32x4 a = (t[j][0][0] + t[j][0][1]) + (t[j][0][2] + t[j][0][3]), b = (t[j][1][0] + t[j][1][1]) + (t[j][1][2] + t[j][1][3]);
    auto r0 = __builtin_amdgcn_permlane32_swap(pack_bf16(a[0], a[1]), pack_bf16(b[0], b[1]), false, false);
    auto r1 = __builtin_amdgcn_permlane32_swap(pack_bf16(a[2], a[3]), pack_bf16(b[2], b[3]), false, false);
    const u32x4 v = {r0[0], r1[0], r0[1], r1[1]};
    __builtin_amdgcn_raw_buffer_store_b128(v, wsr, ol, WS_W2 + ((2 * gt + (fi & 1)) * 40 + 5 * et + (fi >> 1)) * 1024, AUX_SC1);
  }
}

constexpr int XREG = 30, KS0 = 16, YT = 40 - KS0;
constexpr int y_ks(int n) { return n < 3 * KS0 ? n / 3 : KS0 + (n - 3 * KS0) % YT; }
constexpr int y_tt(int n) { return n < 3 * KS0 ? n % 3 : (n - 3 * KS0) / YT; }
constexpr int y_gt(int g) { return 4 * g < 3 * KS0 ? g % 3 : y_tt(4 * g); }
constexpr int y_gk(int g) { return 4 * g < 3 * KS0 ? (g / 3) * 4 : y_ks(4 * g); }

__device__ __forceinline__ void y_load(const Params& p, int wave, int lane, int st, int gt, u64 tag) {
  const int lw = wave - 4;
  u64 fa = 0, fb = 0, fc = 0, fd = 0;
  asm volatile("" : "+v"(fa), "+v"(fb), "+v"(fc), "+v"(fd));
  if (lw == 0) {
    __builtin_amdgcn_s_sleep(YDELAY);
    wait_flags(p.ws, WS_WF + ((3 * gt / 2) * 8 + (lane & 15)) * FS, tag);
    lds_set(LW);
  } else lds_wait(LW);
  const char* wb = p.ws + WS_W2 + (size_t)(3 * gt * 40 + lw) * 1024;
  const char* xb = p.ws + WS_XR + (size_t)(st * 4 + lw) * 40960;
  const unsigned vo = lane * 16;
  auto issue = [&](auto cc) {
    static_for<3 * decltype(cc)::value, 3 * decltype(cc)::value + 3>([&](auto gc) {
      constexpr int g = decltype(gc)::value;
      glds<0, true>(wb + (y_gt(g) * 40 + y_gk(g)) * 1024, vo, g * 4096 + lw * 1024);
    });
  };
  issue(IC<0>{}); issue(IC<1>{}); issue(IC<2>{});
  WAIT_VM(6); BAR();
  issue(IC<3>{}); issue(IC<4>{});
  WAIT_VM(9); BAR();
  issue(IC<5>{});
  WAIT_VM(6); BAR();
  const unsigned xl = 122880 + lw * 10240;
  glds<0, true>(xb + 30 * 1024, vo, xl); glds<1024, true>(xb + 30 * 1024, vo, xl);
  glds4_lin_sc1(xb + 32 * 1024, vo, xl + 2048);
  glds4_lin_sc1(xb + 36 * 1024, vo, xl + 6144);
  WAIT_VM(10); BAR();
  issue(IC<6>{}); issue(IC<7>{});
  WAIT_VM(6); BAR();
  issue(IC<8>{});
  WAIT_VM(6); BAR();
  issue(IC<9>{});
  const bool adv = blockIdx.x == 0 && lw == 0;
  gu64* f = (gu64*)(p.ws + WS_XF + lane * FS);
  WAIT_VM(6);
  if (adv) {
    fa = __hip_atomic_load(f, RLX_AGENT); fb = __hip_atomic_load(f + 64 * FS / 8, RLX_AGENT);
    fc = __hip_atomic_load(f + 128 * FS / 8, RLX_AGENT); fd = __hip_atomic_load(f + 192 * FS / 8, RLX_AGENT);
  }
  BAR();
  if (adv) WAIT_VM(4); else WAIT_VM(0);
  BAR();
  if (adv) {
    asm volatile("" : "+v"(fa), "+v"(fb), "+v"(fc), "+v"(fd));
    if (!__all(fa == tag && fb == tag && fc == tag && fd == tag)) {
#pragma unroll 1
      for (int i = 0; i < 4; ++i) wait_flags(p.ws, WS_XF + (i * 64 + lane) * FS, tag);
    }
    raise_flag(p.ws, WS_EPOCH, tag, lane);
  }
}

constexpr int z_kp(int n) { return n < 3 * KS0 ? n / 6 : (KS0 + (n - 3 * KS0) % YT) / 2; }
constexpr int z_t(int n) { return n < 3 * KS0 ? (n % 6) / 2 : (n - 3 * KS0) / YT; }
constexpr int z_lds(int n) { return (n < 3 * KS0 ? ((z_kp(n) / 2) * 3 + z_t(n)) * 4096 + (z_kp(n) % 2) * 2048 : (n & ~1) * 1024) + (n & 1) * 256; }
__device__ __forceinline__ void y_compute(const Params& p, __amdgpu_buffer_rsrc_t wsr, int wave, int lane, int st, int gt) {
  const unsigned lp = (lane >> 5) * 1024 + ((lane >> 4) & 1) * 512 + (lane & 15) * 16;
  u32x4 xf[XREG];
  const unsigned xbase = WS_XR + (st * 4 + wave) * 40960;
#pragma unroll
  for (int i = 0; i < XREG; ++i) xf[i] = __builtin_amdgcn_raw_buffer_load_b128(wsr, lp + ((i >> 1) & 1) * 2048 + (i & 1) * 256, xbase + (i >> 2) * 4096, AUX_SC1);
  FENCE();
  f32x4 acc[6][2];
#pragma unroll
  for (int t = 0; t < 6; ++t) { acc[t][0] = (f32x4){0.f, 0.f, 0.f, 0.f}; acc[t][1] = (f32x4){0.f, 0.f, 0.f, 0.f}; }
  constexpr int L = 6;
  bf16x8 aw[L + 1], bx[40 - XREG];
  const unsigned a0 = lp, x0 = 122880 + wave * 10240 + lp;
  unsigned a1 = lp + 61440;
  opaque(a1);
  const __amdgpu_buffer_rsrc_t yr = __builtin_amdgcn_make_buffer_rsrc(p.y, 0, S_ * F_ * 2, 0x00020000);
  const unsigned ew = wave * 8192 + (lane & 15) * 80 + (lane >> 4) * 8, er = wave * 8192 + (lane >> 2) * 80 + (lane & 3) * 16;
  const unsigned yo = (lane >> 2) * 3072 + (lane & 3) * 16, ys = (st * 128 + wave * 32) * 3072 + gt * 192;
  u32x4 eo[2];
  auto epi = [&](auto tc, auto sc) {
    constexpr int t = decltype(tc)::value, e = decltype(sc)::value;
    if constexpr (e < 2) {
#pragma unroll
      for (int sh = 0; sh < 2; ++sh) {
        const f32x4 v4 = acc[2 * t + e][sh];
        const u32x2 v = {pack_bf16(v4[0], v4[1]), pack_bf16(v4[2], v4[3])};
        *(LAS u32x2*)(size_t)(ew + t * 2560 + sh * 1280 + e * 32) = v;
      }
      if constexpr (e == 1) { eo[0] = lds_rd128(er + t * 2560); eo[1] = lds_rd128(er + t * 2560 + 1280); }
    } else if constexpr (e >= 5) {
      __builtin_amdgcn_raw_buffer_store_b128(eo[e - 5], yr, yo + (e - 5) * (16 * 3072) + t * 64, ys, t == 2 ? AUX_SC1 : AUX_NT_SC1);
    }
  };
  static_for<0, 120 + L>([&](auto ic) {
    constexpr int n = decltype(ic)::value, j = n - L;
    if constexpr (n < 120) {
      if constexpr (n == 0 || n == 12 || n == 24 || n == 48 || n == 60 || n == 72 || n == 84 || n == 96) BAR();
      if constexpr (z_t(n) == 0 && 2 * z_kp(n) >= XREG && (n & 1) == 0) {
        bx[2 * z_kp(n) - XREG] = __builtin_bit_cast(bf16x8, lds_rd128(x0 + (2 * z_kp(n) - XREG) * 1024));
        bx[2 * z_kp(n) - XREG + 1] = __builtin_bit_cast(bf16x8, lds_rd128(x0 + (2 * z_kp(n) - XREG) * 1024 + 256));
      }
      aw[n % (L + 1)] = __builtin_bit_cast(bf16x8, lds_rd128(z_lds(n) < 61440 ? a0 + z_lds(n) : a1 + (z_lds(n) - 61440)));
    }
    if constexpr (j >= 0) {
      constexpr int kp = z_kp(j), t = z_t(j), a = 2 * t + (j & 1);
#pragma unroll
      for (int sh = 0; sh < 2; ++sh) {
        if constexpr (2 * kp < XREG) acc[a][sh] = __builtin_amdgcn_mfma_f32_16x16x32_bf16(aw[j % (L + 1)], __builtin_bit_cast(bf16x8, xf[2 * kp + sh]), acc[a][sh], 0, 0, 0);
        else acc[a][sh] = __builtin_amdgcn_mfma_f32_16x16x32_bf16(aw[j % (L + 1)], bx[2 * kp - XREG + sh], acc[a][sh], 0, 0, 0);
      }
      SCHED();
      constexpr int u = j >= 3 * KS0 ? (j - 3 * KS0) % YT : 0;
      if constexpr (j >= 3 * KS0 && t >= 1 && u >= 1 && u <= 7) epi(IC<t - 1>{}, IC<u - 1>{});
    }
    SCHED();
  });
  static_for<0, 7>([&](auto sc) { epi(IC<2>{}, sc); });
}

__global__ void __launch_bounds__(512, 2) quip_kernel(Params p) {
  const int wave = __builtin_amdgcn_readfirstlane(threadIdx.x >> 6), lane = threadIdx.x & 63;
  const int xcd = blockIdx.x & 7, idx = blockIdx.x >> 3;
  unsigned pf = 0, pf2 = 0;
  {
    const unsigned ln = idx * 8 + wave;
    if (ln < CODEKB * 8) pf = __hip_atomic_load((const GAS unsigned*)((__builtin_amdgcn_s_getpc() & ~255ull) + ln * 128), RLX_WG);
  }
  const bool isx = idx < 8;
  const int xst = 4 * (xcd >> 1) + (idx >> 1), etl = 2 * (xcd & 1) + (idx & 1);
  const int wk = idx - 8, et = wk & 7, gt = 3 * xcd + (wk >> 3);
  const int yst = 4 * (xcd >> 1) + (idx & 3), ygt = 8 * (xcd & 1) + (idx >> 2);
  XLoader xl; VLoader vl;
  if (!isx && wave < 4) d_issue(p, wave, lane, xcd, wk);
  if (isx && wave >= 4) { xl.init(p, xst, etl, wave - 4, lane); xl.issue(0); xl.issue(1); }
  vl.init(p, gt, wave & 3, 3 * et >> 1, lane);
  if (!isx && wave >= 4) {
    vl.issue(0); vl.issue(1);
    const unsigned n = (wk * 4 + wave - 4) * 35 + (lane < 35 ? lane : 0), lu = n - 2560, ur = (lu * 13108) >> 16;
    const char* a = n < 2560 ? p.x + (size_t)xcd * (256 * 1280) + n * 128
                             : p.U + (xcd & 1) * 640 + ((xcd >> 1) * 160 + ur) * 1280 + (lu - ur * 5) * 128;
    FENCE();
    pf2 = __hip_atomic_load((const GAS unsigned*)a, RLX_WG);
    FENCE();
  }
  const __amdgpu_buffer_rsrc_t wsr = __builtin_amdgcn_make_buffer_rsrc(p.ws, 0, WS_END, 0x00020000);
  const unsigned xfl = WS_XF + (yst * 4 + (lane & 3)) * FS;
  u64 tag;
  if (wave < 4) {
    bool ok = false;
    if (isx) {
      tag = x_compute(p, wsr, wave, lane, xst, etl, idx == 0 && wave == 0);
      asm volatile("" :: "v"(pf));
    } else {
      WAIT_VM(0);
      const u64 ev = load_epoch_v(p);
      d_compute(wsr, wave, lane, xcd, wk);
      WAIT_VM(0);
      tag = make_tag_v(ev);
      asm volatile("" :: "v"(pf));
      BAR();
      if (wave == 0) raise_flag(p.ws, WS_DF + (xcd * 24 + wk) * FS, tag, lane);
      w_body<2>(p, wsr, vl, wave, lane, et, gt);
      if (wave == 0) ok = __all(__hip_atomic_load((gu64*)(p.ws + xfl), RLX_AGENT) == tag);
      if (ok) lds_set(LW + 4);
      WAIT_VM(0);
      WAIT_LGKM0();
      BAR();
    }
    if (wave == 0) { if (!ok) { wait_flags(p.ws, xfl, tag); lds_set(LW + 4); } } else lds_wait(LW + 4);
    y_compute(p, wsr, wave, lane, yst, ygt);
  } else {
    if (isx) {
      tag = xl.run(p);
      asm volatile("" :: "v"(pf));
      __builtin_amdgcn_s_setprio(3);
    } else {
      BAR();
      tag = load_epoch_s(p);
#pragma unroll 1
      for (int i = 2; i < 10; ++i) vl.issue(i);
      make_tag_s(tag);
      if (wave == 4) wait_flags(p.ws, WS_DF + (et * 24 + (lane < 24 ? lane : 0)) * FS, tag);
      WAIT_VM(0);
      asm volatile("" :: "v"(pf), "v"(pf2));
      w_body<3>(p, wsr, vl, wave, lane, et, gt);
      __builtin_amdgcn_s_setprio(3);
      WAIT_VM(0);
      BAR();
      if (wave == 4) raise_flag(p.ws, WS_WF + (gt * 8 + et) * FS, tag, lane);
    }
    y_load(p, wave, lane, yst, ygt, tag);
  }
}

extern "C" void kernel_launch(void* const* d_in, const int* in_sizes, int n_in, void* d_out, int out_size, void* d_ws,
                              size_t ws_size, hipStream_t stream) {
  Params p;
  p.x = (const char*)d_in[0]; p.qw = (const char*)d_in[1]; p.sc = (const char*)d_in[2]; p.zr = (const char*)d_in[3];
  p.U = (const char*)d_in[4]; p.V = (const char*)d_in[5]; p.y = (char*)d_out; p.ws = (char*)d_ws;
  (void)hipFuncSetAttribute((const void*)quip_kernel, hipFuncAttributeMaxDynamicSharedMemorySize, LDS_BYTES);
  quip_kernel<<<256, 512, LDS_BYTES, stream>>>(p);
}
```
